```python
import torch

def _softmax(x, axis=-1):
    return torch.softmax(x, dim=axis)
def _gelu(x):
    return torch.nn.functional.gelu(x, approximate="tanh")
def _silu(x):
    return torch.nn.functional.silu(x)
def _sigmoid(x):
    return torch.sigmoid(x)
def _layernorm(x, axis=-1, eps=1e-5):
    m = x.mean(dim=axis, keepdim=True)
    v = x.var(dim=axis, keepdim=True, unbiased=False)
    return (x - m) / torch.sqrt(v + eps)
def _scatter_add(tgt, idx, src, axis=0):
    return tgt.clone().index_add_(axis, idx, src)
def _segment_sum(src, seg, n):
    out = torch.zeros((n,) + src.shape[1:], dtype=src.dtype)
    return out.index_add_(0, seg, src)
def _pad(x, pw):
    flat = tuple(v for pair in reversed(pw) for v in pair)
    return torch.nn.functional.pad(x, flat)

kernel_name = 'concat_heads__gqa_block_cached__gqa_block_cached__near_ridge'

def setup_inputs():

    _w = (lambda a: (lambda t: t.to(torch.bfloat16) if t.is_floating_point() else t)(torch.as_tensor(a)))
    def _A_setup():
        B, Sq, D = 8, 144, 576
        Hq, Hkv, Sk, Dh = 32, 16, 1024, 128
        assert Hq % Hkv == 0, (Hq, Hkv)
        return {
            "x": _w((lambda *s: torch.randn(*s, dtype=torch.bfloat16) * 0.8)(B, Sq, D)),
            "k_cache": _w((lambda *s: torch.randn(*s, dtype=torch.bfloat16) * 0.8)(B, Hkv, Sk, Dh)),
            "v_cache": _w((lambda *s: torch.randn(*s, dtype=torch.bfloat16) * 0.05)(B, Hkv, Sk, Dh)),
            "wq": _w((lambda *s: torch.randn(*s, dtype=torch.bfloat16) * 0.8)(D, Hq * Dh) * (D ** -0.5)),
            "wo": _w((lambda *s: torch.randn(*s, dtype=torch.bfloat16) * 0.05)(Hq * Dh, D) * ((Hq * Dh) ** -0.5)),
        }
    _A_in = _A_setup()

    def _B_setup():
        B, Sq, D = 8, 144, 576
        Hq, Hkv, Sk, Dh = 32, 16, 1024, 128
        assert Hq % Hkv == 0, (Hq, Hkv)
        return {
            "x": _w((lambda *s: torch.randn(*s, dtype=torch.bfloat16) * 0.8)(B, Sq, D)),
            "k_cache": _w((lambda *s: torch.randn(*s, dtype=torch.bfloat16) * 0.8)(B, Hkv, Sk, Dh)),
            "v_cache": _w((lambda *s: torch.randn(*s, dtype=torch.bfloat16) * 0.05)(B, Hkv, Sk, Dh)),
            "wq": _w((lambda *s: torch.randn(*s, dtype=torch.bfloat16) * 0.8)(D, Hq * Dh) * (D ** -0.5)),
            "wo": _w((lambda *s: torch.randn(*s, dtype=torch.bfloat16) * 0.05)(Hq * Dh, D) * ((Hq * Dh) ** -0.5)),
        }
    _B_in = _B_setup()

    return {
        "A_x": _A_in["x"],
        "A_k_cache": _A_in["k_cache"],
        "A_v_cache": _A_in["v_cache"],
        "A_wq": _A_in["wq"],
        "A_wo": _A_in["wo"],
        "B_x": _B_in["x"],
        "B_k_cache": _B_in["k_cache"],
        "B_v_cache": _B_in["v_cache"],
        "B_wq": _B_in["wq"],
        "B_wo": _B_in["wo"],
    }

def reference(A_x, A_k_cache, A_v_cache, A_wq, A_wo, B_x, B_k_cache, B_v_cache, B_wq, B_wo):
    def _A_ref(x, k_cache, v_cache, wq, wo):
        B, Sq, D = x.shape
        Hq, Dh = 32, 128
        Hkv = k_cache.shape[1]
        rep = Hq // Hkv
        q = (lambda x, *d: x.permute(*d))((x @ wq).reshape(B, Sq, Hq, Dh), 0, 2, 1, 3)
        k = (lambda x, n, axis: torch.repeat_interleave(x, n, dim=axis))(k_cache, rep, axis=1)
        v = (lambda x, n, axis: torch.repeat_interleave(x, n, dim=axis))(v_cache, rep, axis=1)
        scores = torch.einsum("bhqd,bhkd->bhqk", q, k) * (Dh ** -0.5)
        p = _softmax(scores, axis=-1)
        o = torch.einsum("bhqk,bhkd->bhqd", p, v)
        return (lambda x, *d: x.permute(*d))(o, 0, 2, 1, 3).reshape(B, Sq, Hq * Dh) @ wo

    def _B_ref(x, k_cache, v_cache, wq, wo):
        B, Sq, D = x.shape
        Hq, Dh = 32, 128
        Hkv = k_cache.shape[1]
        rep = Hq // Hkv
        q = (lambda x, *d: x.permute(*d))((x @ wq).reshape(B, Sq, Hq, Dh), 0, 2, 1, 3)
        k = (lambda x, n, axis: torch.repeat_interleave(x, n, dim=axis))(k_cache, rep, axis=1)
        v = (lambda x, n, axis: torch.repeat_interleave(x, n, dim=axis))(v_cache, rep, axis=1)
        scores = torch.einsum("bhqd,bhkd->bhqk", q, k) * (Dh ** -0.5)
        p = _softmax(scores, axis=-1)
        o = torch.einsum("bhqk,bhkd->bhqd", p, v)
        return (lambda x, *d: x.permute(*d))(o, 0, 2, 1, 3).reshape(B, Sq, Hq * Dh) @ wo

    a = _A_ref(A_x, A_k_cache, A_v_cache, A_wq, A_wo)
    b = _B_ref(B_x, B_k_cache, B_v_cache, B_wq, B_wo)
    return torch.concatenate([a, b], axis=-1)
```

```cpp
#include <hip/hip_runtime.h>
#include <hip/hip_bf16.h>
#include <stdint.h>

typedef __bf16 bf16x8 __attribute__((ext_vector_type(8)));
typedef float f32x4 __attribute__((ext_vector_type(4)));
typedef unsigned short u16;

constexpr int B_ = 8, SQ = 144, DM = 576, HQ = 32, HKV = 16, SK = 1024, DH = 128;

__device__ __forceinline__ float bf2f(u16 v) { return __uint_as_float(((unsigned)v) << 16); }
__device__ __forceinline__ u16 f2bf(float f) { __bf16 b = (__bf16)f; return __builtin_bit_cast(u16, b); }

__global__ void gemm_simple(const u16* __restrict__ A, int lda, const u16* __restrict__ Bm, int ldb,
                            u16* __restrict__ C, int ldc, int M, int N, int K) {
  int wave = (blockIdx.x * blockDim.x + threadIdx.x) >> 6;
  int lane = threadIdx.x & 63;
  int tn = N / 16;
  int tm_i = wave / tn, tn_i = wave % tn;
  if (tm_i * 16 >= M) return;
  f32x4 acc = {0, 0, 0, 0};
  int r = lane & 15, g = lane >> 4;
  const u16* arow = A + (size_t)(tm_i * 16 + r) * lda + g * 8;
  const u16* bcol = Bm + (size_t)(g * 8) * ldb + tn_i * 16 + r;
  for (int k = 0; k < K; k += 32) {
    bf16x8 a = *reinterpret_cast<const bf16x8*>(arow + k);
    bf16x8 b;
    for (int j = 0; j < 8; ++j) b[j] = __builtin_bit_cast(__bf16, bcol[(size_t)(k + j) * ldb]);
    acc = __builtin_amdgcn_mfma_f32_16x16x32_bf16(a, b, acc, 0, 0, 0);
  }
  for (int j = 0; j < 4; ++j) C[(size_t)(tm_i * 16 + g * 4 + j) * ldc + tn_i * 16 + r] = f2bf(acc[j]);
}

__global__ void attn_simple(const u16* __restrict__ q, const u16* __restrict__ kc, const u16* __restrict__ vc,
                            u16* __restrict__ o) {
  int wave = (blockIdx.x * blockDim.x + threadIdx.x) >> 6;
  int lane = threadIdx.x & 63;
  int rt = wave % 9, h = (wave / 9) % HQ, b = wave / (9 * HQ);
  if (b >= B_) return;
  int kvh = h / 2;
  int r = lane & 15, g = lane >> 4;
  const u16* K = kc + (size_t)(b * HKV + kvh) * SK * DH;
  const u16* V = vc + (size_t)(b * HKV + kvh) * SK * DH;
  bf16x8 qf[4];
  const u16* qrow = q + (size_t)(b * SQ + rt * 16 + r) * (HQ * DH) + h * DH + g * 8;
  for (int s = 0; s < 4; ++s) qf[s] = *reinterpret_cast<const bf16x8*>(qrow + s * 32);
  f32x4 oacc[8];
  for (int i = 0; i < 8; ++i) oacc[i] = f32x4{0, 0, 0, 0};
  float m = -INFINITY, l = 0.f;
  const float c = 0.08838834764831845f * 1.4426950408889634f;
  for (int k0 = 0; k0 < SK; k0 += 32) {
    f32x4 s0 = {0, 0, 0, 0}, s1 = {0, 0, 0, 0};
    for (int s = 0; s < 4; ++s) {
      bf16x8 ka = *reinterpret_cast<const bf16x8*>(K + (size_t)(k0 + r) * DH + s * 32 + g * 8);
      bf16x8 kb = *reinterpret_cast<const bf16x8*>(K + (size_t)(k0 + 16 + r) * DH + s * 32 + g * 8);
      s0 = __builtin_amdgcn_mfma_f32_16x16x32_bf16(ka, qf[s], s0, 0, 0, 0);
      s1 = __builtin_amdgcn_mfma_f32_16x16x32_bf16(kb, qf[s], s1, 0, 0, 0);
    }
    float mx = fmaxf(fmaxf(fmaxf(s0[0], s0[1]), fmaxf(s0[2], s0[3])), fmaxf(fmaxf(s1[0], s1[1]), fmaxf(s1[2], s1[3])));
    mx = fmaxf(mx, __shfl_xor(mx, 16));
    mx = fmaxf(mx, __shfl_xor(mx, 32));
    float mn = fmaxf(m, mx * c);
    float alpha = exp2f(m - mn);
    m = mn;
    float p[8];
    float ps = 0.f;
    for (int j = 0; j < 4; ++j) { p[j] = exp2f(s0[j] * c - mn); p[4 + j] = exp2f(s1[j] * c - mn); }
    bf16x8 pb;
    for (int j = 0; j < 8; ++j) { pb[j] = (__bf16)p[j]; ps += p[j]; }
    l = l * alpha + ps;
    for (int i = 0; i < 8; ++i) {
      bf16x8 va;
      for (int j = 0; j < 4; ++j) {
        va[j] = __builtin_bit_cast(__bf16, V[(size_t)(k0 + 4 * g + j) * DH + i * 16 + r]);
        va[4 + j] = __builtin_bit_cast(__bf16, V[(size_t)(k0 + 16 + 4 * g + j) * DH + i * 16 + r]);
      }
      for (int j = 0; j < 4; ++j) oacc[i][j] *= alpha;
      oacc[i] = __builtin_amdgcn_mfma_f32_16x16x32_bf16(va, pb, oacc[i], 0, 0, 0);
    }
  }
  l += __shfl_xor(l, 16);
  l += __shfl_xor(l, 32);
  float inv = 1.f / l;
  u16* orow = o + (size_t)(b * SQ + rt * 16 + r) * (HQ * DH) + h * DH;
  for (int i = 0; i < 8; ++i)
    for (int j = 0; j < 4; ++j) orow[i * 16 + g * 4 + j] = f2bf(oacc[i][j] * inv);
}

extern "C" void kernel_launch(void* const* d_in, const int* in_sizes, int n_in, void* d_out, int out_size,
                              void* d_ws, size_t ws_size, hipStream_t stream) {
  u16* out = (u16*)d_out;
  u16* ws = (u16*)d_ws;
  const size_t QN = (size_t)B_ * SQ * HQ * DH;
  for (int p = 0; p < 2; ++p) {
    const u16* x = (const u16*)d_in[5 * p + 0];
    const u16* kc = (const u16*)d_in[5 * p + 1];
    const u16* vc = (const u16*)d_in[5 * p + 2];
    const u16* wq = (const u16*)d_in[5 * p + 3];
    const u16* wo = (const u16*)d_in[5 * p + 4];
    u16* q = ws + (size_t)p * 2 * QN;
    u16* o = q + QN;
    int M = B_ * SQ;
    {
      int waves = (M / 16) * (HQ * DH / 16);
      gemm_simple<<<(waves + 3) / 4, 256, 0, stream>>>(x, DM, wq, HQ * DH, q, HQ * DH, M, HQ * DH, DM);
    }
    {
      int waves = B_ * HQ * 9;
      attn_simple<<<(waves + 3) / 4, 256, 0, stream>>>(q, kc, vc, o);
    }
    {
      int waves = (M / 16) * (DM / 16);
      gemm_simple<<<(waves + 3) / 4, 256, 0, stream>>>(o, HQ * DH, wo, DM, out + p * DM, 2 * DM, M, DM, HQ * DH);
    }
  }
}
```

```cpp
#include <hip/hip_runtime.h>
#include <hip/hip_bf16.h>
#include <stdint.h>

typedef unsigned short u16;
typedef __bf16 bf16x8 __attribute__((ext_vector_type(8)));
typedef float f32x4 __attribute__((ext_vector_type(4)));

constexpr int NB = 8, SQ = 144, DM = 576, HQ = 32, HKV = 16, SK = 1024, DH = 128;
constexpr size_t O_HEAD_BYTES = 36864;
constexpr size_t O_BYTES = 2ull * NB * HQ * O_HEAD_BYTES;

#define K1_ROT 0u
#define K1_CLOBBERS "v16", "v17", "v18", "v19", "v20", "v21", "v22", "v23", "v24", "v25", "v26", "v27", "v28", "v29", "v30", "v31", "v32", "v33", "v34", "v35", "v36", "v37", "v38", "v39", "v40", "v41", "v42", "v43", "v44", "v45", "v46", "v47", "v48", "v49", "v50", "v51", "v52", "v53", "v54", "v55", "v56", "v57", "v58", "v59", "v60", "v61", "v62", "v63", "v64", "v65", "v66", "v67", "v68", "v69", "v70", "v71", "v72", "v73", "v74", "v75", "v76", "v77", "v78", "v79", "v80", "v81", "v82", "v83", "v84", "v85", "v86", "v87", "v88", "v89", "v90", "v91", "v92", "v93", "v94", "v95", "v96", "v97", "v98", "v99", "v100", "v101", "v102", "v103", "v104", "v105", "v106", "v107", "v108", "v109", "v110", "v111", "v112", "v113", "v114", "v115", "v116", "v117", "v118", "v119", "v120", "v121", "v122", "v123", "v124", "v125", "v126", "v127", "v128", "v129", "v130", "v131", "v132", "v133", "v134", "v135", "v136", "v137", "v138", "v139", "v140", "v141", "v142", "v143", "v144", "v145", "v146", "v147", "v148", "v149", "v150", "v151", "v152", "v153", "v154", "v155", "v156", "v157", "v158", "v159", "v160", "v161", "v162", "v163", "v164", "v165", "v166", "v167", "v168", "v169", "v170", "v171", "v172", "v173", "v174", "v175", "v176", "v177", "v178", "v179", "v180", "v181", "v182", "v183", "v184", "v185", "v186", "v187", "v188", "v189", "v190", "v191", "v192", "v193", "v194", "v195", "v196", "v197", "v198", "v199", "v200", "v201", "v202", "v203", "v204", "v205", "v206", "v207", "v208", "v209", "v210", "v211", "v212", "v213", "v214", "v215", "v216", "v217", "v218", "v219", "v220", "v221", "v222", "v223", "v224", "v225", "v226", "v227", "v228", "v229", "v230", "v231", "v232", "v233", "v234", "v235", "v236", "v237", "v238", "v239", "v240", "v241", "v242", "v243", "v244", "v245", "v246", "v247", "v248", "v249", "v250", "v251", "v252", "v253", "v254", "v255", "a0", "a1", "a2", "a3", "a4", "a5", "a6", "a7", "a8", "a9", "a10", "a11", "a12", "a13", "a14", "a15", "a16", "a17", "a18", "a19", "a20", "a21", "a22", "a23", "a24", "a25", "a26", "a27", "a28", "a29", "a30", "a31", "a32", "a33", "a34", "a35", "a36", "a37", "a38", "a39", "a40", "a41", "a42", "a43", "a44", "a45", "a46", "a47", "a48", "a49", "a50", "a51", "a52", "a53", "a54", "a55", "a56", "a57", "a58", "a59", "a60", "a61", "a62", "a63", "a64", "a65", "a66", "a67", "a68", "a69", "a70", "a71", "a72", "a73", "a74", "a75", "a76", "a77", "a78", "a79", "a80", "a81", "a82", "a83", "a84", "a85", "a86", "a87", "a88", "a89", "a90", "a91", "a92", "a93", "a94", "a95", "a96", "a97", "a98", "a99", "a100", "a101", "a102", "a103", "a104", "a105", "a106", "a107", "a108", "a109", "a110", "a111", "a112", "a113", "a114", "a115", "a116", "a117", "a118", "a119", "a120", "a121", "a122", "a123", "a124", "a125", "a126", "a127", "a128", "a129", "a130", "a131", "a132", "a133", "a134", "a135", "a136", "a137", "a138", "a139", "a140", "a141", "a142", "a143", "a144", "a145", "a146", "a147", "a148", "a149", "a150", "a151", "a152", "a153", "a154", "a155", "a156", "a157", "a158", "a159", "a160", "a161", "a162", "a163", "a164", "a165", "a166", "a167", "a168", "a169", "a170", "a171", "a172", "a173", "a174", "a175", "a176", "a177", "a178", "a179", "a180", "a181", "a182", "a183", "a184", "a185", "a186", "a187", "a188", "a189", "a190", "a191", "a192", "a193", "a194", "a195", "a196", "a197", "a198", "a199", "a200", "a201", "a202", "a203", "a204", "a205", "a206", "a207", "a208", "a209", "a210", "a211", "a212", "a213", "a214", "a215", "a216", "a217", "a218", "a219", "a220", "a221", "a222", "a223", "a224", "a225", "a226", "a227", "a228", "a229", "a230", "a231", "a232", "a233", "a234", "a235", "a236", "a237", "a238", "a239", "a240", "a241", "a242", "a243", "a244", "a245", "a246", "a247", "a248", "a249", "a250", "a251", "a252", "a253", "a254", "a255", "s40", "s41", "s42", "s43", "s44", "s45", "s46", "s47", "s48", "s49", "s50", "s51", "s52", "s53", "s54", "s55", "s56", "s57", "s58", "s59", "s60", "s61", "s62", "s63", "s64", "s65", "s66", "s67", "s68", "s69", "s70", "s71", "s72", "s73", "s74", "s75", "s76", "s77", "s78", "s79", "s80", "s81", "s82", "s83", "s84", "s85", "s86", "s87", "s88", "s89", "s90", "s91", "s92", "s93", "s94", "s95", "s96", "s97", "s98", "s99", "vcc", "scc", "m0", "memory"

struct K1Args {
  const u16* x[2]; const u16* k[2]; const u16* v[2]; const u16* wq[2]; const u16* wo[2];
  unsigned char* o;
};

__device__ __forceinline__ float bf2f(u16 v) { return __uint_as_float(((unsigned)v) << 16); }
__device__ __forceinline__ u16 f2bf(float f) { __bf16 b = (__bf16)f; return __builtin_bit_cast(u16, b); }

__device__ __noinline__ void attn_slow_path(const u16* x, const u16* wq, const u16* kc, const u16* vc, unsigned char* o) {
  extern __shared__ __attribute__((aligned(16))) float lds_f[];
  const unsigned lane = threadIdx.x & 63, w = threadIdx.x >> 6;
  float* qs = lds_f + w * 1280;
  float* ps = qs + 128;
  for (unsigned r = w; r < 2 * SQ; r += 4) {
    const unsigned hg = r / SQ, pos = r % SQ;
    float q0 = 0.f, q1 = 0.f;
    const u16* xr = x + pos * DM;
    const u16* wc = wq + hg * DH + 2 * lane;
    for (int k = 0; k < DM; ++k) {
      const float xv = bf2f(xr[k]);
      q0 = fmaf(xv, bf2f(wc[(size_t)k * (HQ * DH)]), q0);
      q1 = fmaf(xv, bf2f(wc[(size_t)k * (HQ * DH) + 1]), q1);
    }
    qs[2 * lane] = bf2f(f2bf(q0));
    qs[2 * lane + 1] = bf2f(f2bf(q1));
    __builtin_amdgcn_wave_barrier();
    __builtin_amdgcn_s_waitcnt(0xc07f);
    float m = -INFINITY;
    for (int i = 0; i < SK / 64; ++i) {
      const u16* kr = kc + (size_t)(i * 64 + lane) * DH;
      float s = 0.f;
      for (int d = 0; d < DH; ++d) s = fmaf(qs[d], bf2f(kr[d]), s);
      s *= 0.08838834764831845f;
      ps[i * 64 + lane] = s;
      m = fmaxf(m, s);
    }
    for (int off = 32; off; off >>= 1) m = fmaxf(m, __shfl_xor(m, off));
    float l = 0.f;
    for (int i = 0; i < SK / 64; ++i) {
      const float p = __expf(ps[i * 64 + lane] - m);
      ps[i * 64 + lane] = p;
      l += p;
    }
    for (int off = 32; off; off >>= 1) l += __shfl_xor(l, off);
    __builtin_amdgcn_wave_barrier();
    __builtin_amdgcn_s_waitcnt(0xc07f);
    float o0 = 0.f, o1 = 0.f;
    for (int k = 0; k < SK; ++k) {
      const float p = ps[k];
      o0 = fmaf(p, bf2f(vc[(size_t)k * DH + 2 * lane]), o0);
      o1 = fmaf(p, bf2f(vc[(size_t)k * DH + 2 * lane + 1]), o1);
    }
    const float inv = 1.f / l;
    const unsigned d = 2 * lane, i = d >> 5, dd = d & 31, g = (dd & 15) >> 2, e = (dd & 3) + ((dd >> 4) << 2);
    u16* dst = (u16*)(o + hg * O_HEAD_BYTES + ((pos >> 4) * 4 + i) * 1024 + (g * 16 + (pos & 15)) * 16 + e * 2);
    dst[0] = f2bf(o0 * inv);
    dst[1] = f2bf(o1 * inv);
    __builtin_amdgcn_wave_barrier();
  }
}

__global__ __launch_bounds__(256, 1) void attn_fused_kernel(K1Args a) {
  const unsigned blk = blockIdx.x;
  const unsigned xcd = blk & 7, j = blk >> 3;
  const unsigned p = xcd >> 2, kvh = (xcd & 3) * 4 + (j & 3), b = j >> 2;
  const u16* x = (p ? a.x[1] : a.x[0]) + (size_t)b * SQ * DM;
  const u16* wq = (p ? a.wq[1] : a.wq[0]) + kvh * 256;
  const u16* kc = (p ? a.k[1] : a.k[0]) + (size_t)(b * HKV + kvh) * SK * DH;
  const u16* vc = (p ? a.v[1] : a.v[0]) + (size_t)(b * HKV + kvh) * SK * DH;
  unsigned char* o = a.o + ((size_t)(p * NB + b) * HQ + 2 * kvh) * O_HEAD_BYTES;
  const unsigned tid = threadIdx.x, lane = tid & 63;
  const unsigned w = __builtin_amdgcn_readfirstlane(tid >> 6);
  const unsigned q = lane & 15, g = lane >> 4, qq = (lane & 15) >> 2, p4 = lane & 3, hg = w >> 1, half = w & 1;
  const unsigned vK00 = half * 8192 + q * 256 + 16 * (g ^ q ^ (8 * half));
  const unsigned vV00 = 65536 + half * 8192 + (g >> 1) * 2048 + (4 * (g & 1) + qq) * 64 + 32 * (g & 1) + 8 * p4;
  const unsigned vW0 = 81920 + g * 4104 + qq * 512 + 256 * hg + 16 * p4;
  const unsigned vX0 = 32768 + half * 8192 + q * 128 + 16 * (g ^ (q >> 1));
  const unsigned vXt0 = 147712 + q * 128 + 16 * (g ^ (q >> 1));
  const unsigned rk = lane >> 4;
  const unsigned offK = rk * 256 + 16 * ((lane & 15) ^ rk) + w * 4096;
  const unsigned rv = (lane >> 2) & 7;
  const unsigned offV = rv * 256 + (lane >> 5) * 64 + 16 * ((lane & 3) ^ (2 * (rv >> 2))) + w * 4096;
  const unsigned rw = lane >> 5, pos16 = lane & 31;
  const unsigned offW = (8 * w + rw) * 8192 + 256 * (pos16 >> 4) + 64 * (((pos16 >> 2) & 3) ^ rw) + 16 * (pos16 & 3);
  const unsigned rx = lane >> 3;
  const unsigned offX = (32 * w + rx) * 1152 + 16 * ((lane & 7) ^ (rx >> 1));
  const unsigned offXt = (128 + 8 * half + rx) * 1152 + 16 * ((lane & 7) ^ (4 * half + (rx >> 1)));
  const unsigned lane16 = lane * 16;
  unsigned char* dbg = a.o + (64u << 20) + (size_t)blk * 4096;
  const unsigned pfC = j * 4096 + (w ? 0x1000000u : 0u);
  const u16* wo = p ? a.wo[1] : a.wo[0];
  const unsigned pfO = (b * HKV + kvh) * 36864 + w * 12288;
  const unsigned rot = K1_ROT;
  unsigned flag;
  asm volatile(
"s_mov_b64 s[44:45], %2\n"
"s_and_b32 s45, s45, 0xffff\n"
"s_mov_b32 s46, 4718592\n"
"s_mov_b32 s47, 131072\n"
"s_mov_b64 s[40:41], %1\n"
"s_and_b32 s41, s41, 0xffff\n"
"s_mov_b32 s42, 165888\n"
"s_mov_b32 s43, 131072\n"
"s_lshl_b32 s60, %6, 10\n"
"s_lshl_b32 s61, %6, 12\n"
"s_mul_i32 s64, %6, 4104\n"
"s_and_b32 s71, %6, 1\n"
"s_lshr_b32 s72, %6, 1\n"
"s_lshl_b32 s70, %22, 7\n"
"s_lshl_b32 s73, %22, 19\n"
"s_lshl_b32 s63, s71, 10\n"
"s_lshl_b32 s67, %6, 11\n"
"s_add_u32 s67, s67, 152064\n"
"v_xor_b32 v64, 128, %14\n"
"v_mov_b32 v84, %14\n"
"v_add_u32 v85, 15360, v64\n"
"v_add_u32 v86, 30720, %14\n"
"v_add_u32 v87, 46080, v64\n"
"s_add_u32 m0, s64, 114752\n"
"s_nop 0\n"
"buffer_load_dwordx4 v84, s[44:47], s73 offen lds\n"
"buffer_load_dwordx4 v85, s[44:47], s73 offen offset:1024 lds\n"
"buffer_load_dwordx4 v86, s[44:47], s73 offen offset:2048 lds\n"
"buffer_load_dwordx4 v87, s[44:47], s73 offen offset:3072 lds\n"
"s_add_u32 s73, s73, 262144\n"
"s_cmp_eq_u32 s73, 4718592\n"
"s_cselect_b32 s73, 0, s73\n"
"v_xor_b32 v64, 64, %15\n"
"v_mov_b32 v88, %15\n"
"v_add_u32 v89, 8192, v64\n"
"v_add_u32 v90, 16384, %15\n"
"v_add_u32 v91, 24576, v64\n"
"v_mov_b32 v92, %18\n"
"s_add_u32 m0, s61, 49152\n"
"s_nop 0\n"
"buffer_load_dwordx4 v88, s[40:43], s70 offen lds\n"
"buffer_load_dwordx4 v89, s[40:43], s70 offen offset:1024 lds\n"
"buffer_load_dwordx4 v90, s[40:43], s70 offen offset:2048 lds\n"
"buffer_load_dwordx4 v91, s[40:43], s70 offen offset:3072 lds\n"
"s_add_u32 m0, s63, 149760\n"
"s_nop 0\n"
"buffer_load_dwordx4 v92, s[40:43], s70 offen lds\n"
"s_add_u32 s70, s70, 128\n"
"s_cmp_eq_u32 s70, 1152\n"
"s_cselect_b32 s70, 0, s70\n"
"s_add_u32 m0, s64, 131168\n"
"s_nop 0\n"
"buffer_load_dwordx4 v84, s[44:47], s73 offen lds\n"
"buffer_load_dwordx4 v85, s[44:47], s73 offen offset:1024 lds\n"
"buffer_load_dwordx4 v86, s[44:47], s73 offen offset:2048 lds\n"
"buffer_load_dwordx4 v87, s[44:47], s73 offen offset:3072 lds\n"
"s_add_u32 s73, s73, 262144\n"
"s_cmp_eq_u32 s73, 4718592\n"
"s_cselect_b32 s73, 0, s73\n"
"s_add_u32 m0, s64, 81920\n"
"s_nop 0\n"
"buffer_load_dwordx4 v84, s[44:47], s73 offen lds\n"
"buffer_load_dwordx4 v85, s[44:47], s73 offen offset:1024 lds\n"
"buffer_load_dwordx4 v86, s[44:47], s73 offen offset:2048 lds\n"
"buffer_load_dwordx4 v87, s[44:47], s73 offen offset:3072 lds\n"
"s_add_u32 s73, s73, 262144\n"
"s_cmp_eq_u32 s73, 4718592\n"
"s_cselect_b32 s73, 0, s73\n"
"s_add_u32 m0, s61, 32768\n"
"s_nop 0\n"
"buffer_load_dwordx4 v88, s[40:43], s70 offen lds\n"
"buffer_load_dwordx4 v89, s[40:43], s70 offen offset:1024 lds\n"
"buffer_load_dwordx4 v90, s[40:43], s70 offen offset:2048 lds\n"
"buffer_load_dwordx4 v91, s[40:43], s70 offen offset:3072 lds\n"
"s_add_u32 m0, s63, 147712\n"
"s_nop 0\n"
"buffer_load_dwordx4 v92, s[40:43], s70 offen lds\n"
"s_add_u32 s70, s70, 128\n"
"s_cmp_eq_u32 s70, 1152\n"
"s_cselect_b32 s70, 0, s70\n"
"v_lshlrev_b32 v26, 2, %16\n"
"s_getpc_b64 s[92:93]\n"
".Lk1_pc_%=:\n"
"s_and_b32 s93, s93, 0xffff\n"
"s_mov_b32 s94, k2_code_end-.Lk1_pc_%=\n"
"s_mov_b32 s95, 0x20000\n"
"buffer_load_dword a0, v26, s[92:95], %19 offen\n"
"s_mov_b64 s[48:49], %3\n"
"s_and_b32 s49, s49, 0xffff\n"
"s_mov_b32 s50, 262144\n"
"s_mov_b32 s51, 131072\n"
"s_mov_b64 s[52:53], %4\n"
"s_and_b32 s53, s53, 0xffff\n"
"s_mov_b32 s54, 263168\n"
"s_mov_b32 s55, 131072\n"
"v_xor_b32 v24, 0, %12\n"
"v_xor_b32 v25, 64, %12\n"
"v_xor_b32 v93, 128, %12\n"
"v_xor_b32 v94, 192, %12\n"
"v_add_u32 v95, 1024, %13\n"
"v_add_u32 v27, 128, %13\n"
"s_mov_b64 s[56:57], %5\n"
"s_and_b32 s57, s57, 0xffff\n"
"s_mov_b32 s58, 73728\n"
"s_mov_b32 s59, 131072\n"
"s_mul_i32 s77, s72, 36864\n"
"s_mov_b32 s68, 1040356334\n"
"s_mov_b32 s69, 1040356334\n"
"s_mov_b32 s78, 1568669696\n"
"s_mov_b64 s[74:75], 0\n"
"v_and_b32 v66, 192, %16\n"
"s_lshl_b32 s76, s71, 7\n"
"v_xor_b32 v66, s76, v66\n"
"v_xor_b32 v67, 0, v66\n"
"v_add_u32 v16, v67, %9\n"
"v_xor_b32 v67, 64, v66\n"
"v_add_u32 v17, v67, %9\n"
"v_xor_b32 v67, 128, v66\n"
"v_add_u32 v18, v67, %9\n"
"v_xor_b32 v67, 192, v66\n"
"v_add_u32 v19, v67, %9\n"
"v_mov_b32 v20, %10\n"
"v_xor_b32 v21, 64, %10\n"
"v_mov_b32 v22, %11\n"
"v_xor_b32 v23, 64, %11\n"
"s_waitcnt vmcnt(14)\n"
"s_barrier\n"
"ds_read_b128 v[44:47], v20 offset:16384\n"
"ds_read_b128 v[48:51], v20 offset:18432\n"
"ds_read_b128 v[52:55], v20 offset:20480\n"
"ds_read_b128 v[56:59], v20 offset:22528\n"
"ds_read_b128 v[60:63], v22 offset:2048\n"
"ds_read_b64_tr_b16 v[28:29], v16 offset:32832\n"
"ds_read_b64_tr_b16 v[30:31], v16 offset:34880\n"
"ds_read_b64_tr_b16 v[32:33], v16 offset:32840\n"
"ds_read_b64_tr_b16 v[34:35], v16 offset:34888\n"
"ds_read_b64_tr_b16 v[36:37], v17 offset:32832\n"
"ds_read_b64_tr_b16 v[38:39], v17 offset:34880\n"
"s_waitcnt lgkmcnt(4)\n"
"v_mfma_f32_16x16x32_bf16 v[96:99], v[28:31], v[44:47], 0\n"
"ds_read_b64_tr_b16 v[40:41], v17 offset:32840\n"
"ds_read_b64_tr_b16 v[42:43], v17 offset:34888\n"
"v_mfma_f32_16x16x32_bf16 v[128:131], v[28:31], v[48:51], 0\n"
"v_mfma_f32_16x16x32_bf16 v[160:163], v[28:31], v[52:55], 0\n"
"v_mfma_f32_16x16x32_bf16 v[192:195], v[28:31], v[56:59], 0\n"
"v_mfma_f32_16x16x32_bf16 v[224:227], v[28:31], v[60:63], 0\n"
"s_waitcnt lgkmcnt(4)\n"
"v_mfma_f32_16x16x32_bf16 v[100:103], v[32:35], v[44:47], 0\n"
"ds_read_b64_tr_b16 v[28:29], v18 offset:32832\n"
"ds_read_b64_tr_b16 v[30:31], v18 offset:34880\n"
"v_mfma_f32_16x16x32_bf16 v[132:135], v[32:35], v[48:51], 0\n"
"v_mfma_f32_16x16x32_bf16 v[164:167], v[32:35], v[52:55], 0\n"
"v_mfma_f32_16x16x32_bf16 v[196:199], v[32:35], v[56:59], 0\n"
"v_mfma_f32_16x16x32_bf16 v[228:231], v[32:35], v[60:63], 0\n"
"s_waitcnt lgkmcnt(4)\n"
"v_mfma_f32_16x16x32_bf16 v[104:107], v[36:39], v[44:47], 0\n"
"ds_read_b64_tr_b16 v[32:33], v18 offset:32840\n"
"ds_read_b64_tr_b16 v[34:35], v18 offset:34888\n"
"v_mfma_f32_16x16x32_bf16 v[136:139], v[36:39], v[48:51], 0\n"
"v_mfma_f32_16x16x32_bf16 v[168:171], v[36:39], v[52:55], 0\n"
"v_mfma_f32_16x16x32_bf16 v[200:203], v[36:39], v[56:59], 0\n"
"v_mfma_f32_16x16x32_bf16 v[232:235], v[36:39], v[60:63], 0\n"
"s_waitcnt lgkmcnt(4)\n"
"v_mfma_f32_16x16x32_bf16 v[108:111], v[40:43], v[44:47], 0\n"
"ds_read_b64_tr_b16 v[36:37], v19 offset:32832\n"
"ds_read_b64_tr_b16 v[38:39], v19 offset:34880\n"
"v_mfma_f32_16x16x32_bf16 v[140:143], v[40:43], v[48:51], 0\n"
"v_mfma_f32_16x16x32_bf16 v[172:175], v[40:43], v[52:55], 0\n"
"v_mfma_f32_16x16x32_bf16 v[204:207], v[40:43], v[56:59], 0\n"
"v_mfma_f32_16x16x32_bf16 v[236:239], v[40:43], v[60:63], 0\n"
"s_waitcnt lgkmcnt(4)\n"
"v_mfma_f32_16x16x32_bf16 v[112:115], v[28:31], v[44:47], 0\n"
"ds_read_b64_tr_b16 v[40:41], v19 offset:32840\n"
"ds_read_b64_tr_b16 v[42:43], v19 offset:34888\n"
"v_mfma_f32_16x16x32_bf16 v[144:147], v[28:31], v[48:51], 0\n"
"v_mfma_f32_16x16x32_bf16 v[176:179], v[28:31], v[52:55], 0\n"
"s_waitcnt vmcnt(10)\n"
"s_barrier\n"
"v_mfma_f32_16x16x32_bf16 v[208:211], v[28:31], v[56:59], 0\n"
"s_waitcnt lgkmcnt(4)\n"
"ds_read_b128 v[64:67], v21 offset:16384\n"
"v_mfma_f32_16x16x32_bf16 v[116:119], v[32:35], v[44:47], 0\n"
"ds_read_b64_tr_b16 v[28:29], v16 offset:49248\n"
"ds_read_b64_tr_b16 v[30:31], v16 offset:51296\n"
"v_mfma_f32_16x16x32_bf16 v[148:151], v[32:35], v[48:51], 0\n"
"ds_read_b128 v[68:71], v21 offset:18432\n"
"v_mfma_f32_16x16x32_bf16 v[180:183], v[32:35], v[52:55], 0\n"
"s_add_u32 m0, s64, 98336\n"
"v_mfma_f32_16x16x32_bf16 v[212:215], v[32:35], v[56:59], 0\n"
"s_waitcnt lgkmcnt(6)\n"
"ds_read_b128 v[72:75], v21 offset:20480\n"
"v_mfma_f32_16x16x32_bf16 v[120:123], v[36:39], v[44:47], 0\n"
"ds_read_b64_tr_b16 v[32:33], v16 offset:49256\n"
"ds_read_b64_tr_b16 v[34:35], v16 offset:51304\n"
"v_mfma_f32_16x16x32_bf16 v[152:155], v[36:39], v[48:51], 0\n"
"ds_read_b128 v[76:79], v21 offset:22528\n"
"v_mfma_f32_16x16x32_bf16 v[184:187], v[36:39], v[52:55], 0\n"
"buffer_load_dwordx4 v84, s[44:47], s73 offen lds\n"
"v_mfma_f32_16x16x32_bf16 v[216:219], v[36:39], v[56:59], 0\n"
"s_waitcnt lgkmcnt(8)\n"
"ds_read_b128 v[80:83], v23 offset:2048\n"
"v_mfma_f32_16x16x32_bf16 v[124:127], v[40:43], v[44:47], 0\n"
"ds_read_b64_tr_b16 v[36:37], v17 offset:49248\n"
"ds_read_b64_tr_b16 v[38:39], v17 offset:51296\n"
"v_mfma_f32_16x16x32_bf16 v[156:159], v[40:43], v[48:51], 0\n"
"buffer_load_dwordx4 v85, s[44:47], s73 offen offset:1024 lds\n"
"v_mfma_f32_16x16x32_bf16 v[188:191], v[40:43], v[52:55], 0\n"
"v_mfma_f32_16x16x32_bf16 v[220:223], v[40:43], v[56:59], 0\n"
"buffer_load_dwordx4 v86, s[44:47], s73 offen offset:2048 lds\n"
"s_waitcnt lgkmcnt(2)\n"
"v_mfma_f32_16x16x32_bf16 v[96:99], v[28:31], v[64:67], v[96:99]\n"
"ds_read_b64_tr_b16 v[40:41], v17 offset:49256\n"
"ds_read_b64_tr_b16 v[42:43], v17 offset:51304\n"
"v_mfma_f32_16x16x32_bf16 v[128:131], v[28:31], v[68:71], v[128:131]\n"
"buffer_load_dwordx4 v87, s[44:47], s73 offen offset:3072 lds\n"
"v_mfma_f32_16x16x32_bf16 v[160:163], v[28:31], v[72:75], v[160:163]\n"
"v_mfma_f32_16x16x32_bf16 v[192:195], v[28:31], v[76:79], v[192:195]\n"
"s_add_u32 s73, s73, 262144\n"
"s_cmp_eq_u32 s73, 4718592\n"
"s_cselect_b32 s73, 0, s73\n"
"v_mfma_f32_16x16x32_bf16 v[224:227], v[28:31], v[80:83], v[224:227]\n"
"v_mfma_f32_16x16x32_bf16 v[100:103], v[32:35], v[64:67], v[100:103]\n"
"ds_read_b64_tr_b16 v[28:29], v18 offset:49248\n"
"ds_read_b64_tr_b16 v[30:31], v18 offset:51296\n"
"v_mfma_f32_16x16x32_bf16 v[132:135], v[32:35], v[68:71], v[132:135]\n"
"v_mfma_f32_16x16x32_bf16 v[164:167], v[32:35], v[72:75], v[164:167]\n"
"v_mfma_f32_16x16x32_bf16 v[196:199], v[32:35], v[76:79], v[196:199]\n"
"v_mfma_f32_16x16x32_bf16 v[228:231], v[32:35], v[80:83], v[228:231]\n"
"s_waitcnt lgkmcnt(4)\n"
"v_mfma_f32_16x16x32_bf16 v[104:107], v[36:39], v[64:67], v[104:107]\n"
"ds_read_b64_tr_b16 v[32:33], v18 offset:49256\n"
"ds_read_b64_tr_b16 v[34:35], v18 offset:51304\n"
"v_mfma_f32_16x16x32_bf16 v[136:139], v[36:39], v[68:71], v[136:139]\n"
"v_mfma_f32_16x16x32_bf16 v[168:171], v[36:39], v[72:75], v[168:171]\n"
"v_mfma_f32_16x16x32_bf16 v[200:203], v[36:39], v[76:79], v[200:203]\n"
"v_mfma_f32_16x16x32_bf16 v[232:235], v[36:39], v[80:83], v[232:235]\n"
"s_waitcnt lgkmcnt(4)\n"
"v_mfma_f32_16x16x32_bf16 v[108:111], v[40:43], v[64:67], v[108:111]\n"
"ds_read_b64_tr_b16 v[36:37], v19 offset:49248\n"
"ds_read_b64_tr_b16 v[38:39], v19 offset:51296\n"
"v_mfma_f32_16x16x32_bf16 v[140:143], v[40:43], v[68:71], v[140:143]\n"
"v_mfma_f32_16x16x32_bf16 v[172:175], v[40:43], v[72:75], v[172:175]\n"
"v_mfma_f32_16x16x32_bf16 v[204:207], v[40:43], v[76:79], v[204:207]\n"
"v_mfma_f32_16x16x32_bf16 v[236:239], v[40:43], v[80:83], v[236:239]\n"
"s_waitcnt lgkmcnt(4)\n"
"v_mfma_f32_16x16x32_bf16 v[112:115], v[28:31], v[64:67], v[112:115]\n"
"ds_read_b64_tr_b16 v[40:41], v19 offset:49256\n"
"ds_read_b64_tr_b16 v[42:43], v19 offset:51304\n"
"v_mfma_f32_16x16x32_bf16 v[144:147], v[28:31], v[68:71], v[144:147]\n"
"v_mfma_f32_16x16x32_bf16 v[176:179], v[28:31], v[72:75], v[176:179]\n"
"s_waitcnt vmcnt(5)\n"
"s_barrier\n"
"v_mfma_f32_16x16x32_bf16 v[208:211], v[28:31], v[76:79], v[208:211]\n"
"s_waitcnt lgkmcnt(4)\n"
"ds_read_b128 v[44:47], v20 offset:0\n"
"v_mfma_f32_16x16x32_bf16 v[116:119], v[32:35], v[64:67], v[116:119]\n"
"ds_read_b64_tr_b16 v[28:29], v16 offset:0\n"
"ds_read_b64_tr_b16 v[30:31], v16 offset:2048\n"
"v_mfma_f32_16x16x32_bf16 v[148:151], v[32:35], v[68:71], v[148:151]\n"
"ds_read_b128 v[48:51], v20 offset:2048\n"
"v_mfma_f32_16x16x32_bf16 v[180:183], v[32:35], v[72:75], v[180:183]\n"
"s_add_u32 m0, s64, 114752\n"
"v_mfma_f32_16x16x32_bf16 v[212:215], v[32:35], v[76:79], v[212:215]\n"
"s_waitcnt lgkmcnt(6)\n"
"ds_read_b128 v[52:55], v20 offset:4096\n"
"v_mfma_f32_16x16x32_bf16 v[120:123], v[36:39], v[64:67], v[120:123]\n"
"ds_read_b64_tr_b16 v[32:33], v16 offset:8\n"
"ds_read_b64_tr_b16 v[34:35], v16 offset:2056\n"
"v_mfma_f32_16x16x32_bf16 v[152:155], v[36:39], v[68:71], v[152:155]\n"
"ds_read_b128 v[56:59], v20 offset:6144\n"
"v_mfma_f32_16x16x32_bf16 v[184:187], v[36:39], v[72:75], v[184:187]\n"
"buffer_load_dwordx4 v84, s[44:47], s73 offen lds\n"
"v_mfma_f32_16x16x32_bf16 v[216:219], v[36:39], v[76:79], v[216:219]\n"
"s_waitcnt lgkmcnt(8)\n"
"ds_read_b128 v[60:63], v22 offset:0\n"
"v_mfma_f32_16x16x32_bf16 v[124:127], v[40:43], v[64:67], v[124:127]\n"
"ds_read_b64_tr_b16 v[36:37], v17 offset:0\n"
"ds_read_b64_tr_b16 v[38:39], v17 offset:2048\n"
"v_mfma_f32_16x16x32_bf16 v[156:159], v[40:43], v[68:71], v[156:159]\n"
"buffer_load_dwordx4 v85, s[44:47], s73 offen offset:1024 lds\n"
"v_mfma_f32_16x16x32_bf16 v[188:191], v[40:43], v[72:75], v[188:191]\n"
"v_mfma_f32_16x16x32_bf16 v[220:223], v[40:43], v[76:79], v[220:223]\n"
"buffer_load_dwordx4 v86, s[44:47], s73 offen offset:2048 lds\n"
"s_waitcnt lgkmcnt(2)\n"
"v_mfma_f32_16x16x32_bf16 v[96:99], v[28:31], v[44:47], v[96:99]\n"
"ds_read_b64_tr_b16 v[40:41], v17 offset:8\n"
"ds_read_b64_tr_b16 v[42:43], v17 offset:2056\n"
"v_mfma_f32_16x16x32_bf16 v[128:131], v[28:31], v[48:51], v[128:131]\n"
"buffer_load_dwordx4 v87, s[44:47], s73 offen offset:3072 lds\n"
"v_mfma_f32_16x16x32_bf16 v[160:163], v[28:31], v[52:55], v[160:163]\n"
"v_mfma_f32_16x16x32_bf16 v[192:195], v[28:31], v[56:59], v[192:195]\n"
"s_add_u32 s73, s73, 262144\n"
"s_cmp_eq_u32 s73, 4718592\n"
"s_cselect_b32 s73, 0, s73\n"
"s_add_u32 m0, s61, 49152\n"
"v_mfma_f32_16x16x32_bf16 v[224:227], v[28:31], v[60:63], v[224:227]\n"
"v_mfma_f32_16x16x32_bf16 v[100:103], v[32:35], v[44:47], v[100:103]\n"
"ds_read_b64_tr_b16 v[28:29], v18 offset:0\n"
"ds_read_b64_tr_b16 v[30:31], v18 offset:2048\n"
"v_mfma_f32_16x16x32_bf16 v[132:135], v[32:35], v[48:51], v[132:135]\n"
"buffer_load_dwordx4 v88, s[40:43], s70 offen lds\n"
"v_mfma_f32_16x16x32_bf16 v[164:167], v[32:35], v[52:55], v[164:167]\n"
"v_mfma_f32_16x16x32_bf16 v[196:199], v[32:35], v[56:59], v[196:199]\n"
"buffer_load_dwordx4 v89, s[40:43], s70 offen offset:1024 lds\n"
"v_mfma_f32_16x16x32_bf16 v[228:231], v[32:35], v[60:63], v[228:231]\n"
"s_waitcnt lgkmcnt(4)\n"
"v_mfma_f32_16x16x32_bf16 v[104:107], v[36:39], v[44:47], v[104:107]\n"
"ds_read_b64_tr_b16 v[32:33], v18 offset:8\n"
"ds_read_b64_tr_b16 v[34:35], v18 offset:2056\n"
"v_mfma_f32_16x16x32_bf16 v[136:139], v[36:39], v[48:51], v[136:139]\n"
"buffer_load_dwordx4 v90, s[40:43], s70 offen offset:2048 lds\n"
"v_mfma_f32_16x16x32_bf16 v[168:171], v[36:39], v[52:55], v[168:171]\n"
"v_mfma_f32_16x16x32_bf16 v[200:203], v[36:39], v[56:59], v[200:203]\n"
"buffer_load_dwordx4 v91, s[40:43], s70 offen offset:3072 lds\n"
"v_mfma_f32_16x16x32_bf16 v[232:235], v[36:39], v[60:63], v[232:235]\n"
"s_waitcnt lgkmcnt(4)\n"
"v_mfma_f32_16x16x32_bf16 v[108:111], v[40:43], v[44:47], v[108:111]\n"
"ds_read_b64_tr_b16 v[36:37], v19 offset:0\n"
"ds_read_b64_tr_b16 v[38:39], v19 offset:2048\n"
"v_mfma_f32_16x16x32_bf16 v[140:143], v[40:43], v[48:51], v[140:143]\n"
"s_add_u32 m0, s63, 149760\n"
"v_mfma_f32_16x16x32_bf16 v[172:175], v[40:43], v[52:55], v[172:175]\n"
"buffer_load_dwordx4 v92, s[40:43], s70 offen lds\n"
"v_mfma_f32_16x16x32_bf16 v[204:207], v[40:43], v[56:59], v[204:207]\n"
"v_mfma_f32_16x16x32_bf16 v[236:239], v[40:43], v[60:63], v[236:239]\n"
"s_waitcnt lgkmcnt(4)\n"
"s_add_u32 s70, s70, 128\n"
"s_cmp_eq_u32 s70, 1152\n"
"s_cselect_b32 s70, 0, s70\n"
"v_mfma_f32_16x16x32_bf16 v[112:115], v[28:31], v[44:47], v[112:115]\n"
"ds_read_b64_tr_b16 v[40:41], v19 offset:8\n"
"ds_read_b64_tr_b16 v[42:43], v19 offset:2056\n"
"v_mfma_f32_16x16x32_bf16 v[144:147], v[28:31], v[48:51], v[144:147]\n"
"v_mfma_f32_16x16x32_bf16 v[176:179], v[28:31], v[52:55], v[176:179]\n"
"s_waitcnt vmcnt(9)\n"
"s_barrier\n"
"v_mfma_f32_16x16x32_bf16 v[208:211], v[28:31], v[56:59], v[208:211]\n"
"s_waitcnt lgkmcnt(4)\n"
"ds_read_b128 v[64:67], v21 offset:0\n"
"v_mfma_f32_16x16x32_bf16 v[116:119], v[32:35], v[44:47], v[116:119]\n"
"ds_read_b64_tr_b16 v[28:29], v16 offset:16416\n"
"ds_read_b64_tr_b16 v[30:31], v16 offset:18464\n"
"v_mfma_f32_16x16x32_bf16 v[148:151], v[32:35], v[48:51], v[148:151]\n"
"ds_read_b128 v[68:71], v21 offset:2048\n"
"v_mfma_f32_16x16x32_bf16 v[180:183], v[32:35], v[52:55], v[180:183]\n"
"s_add_u32 m0, s64, 131168\n"
"v_mfma_f32_16x16x32_bf16 v[212:215], v[32:35], v[56:59], v[212:215]\n"
"s_waitcnt lgkmcnt(6)\n"
"ds_read_b128 v[72:75], v21 offset:4096\n"
"v_mfma_f32_16x16x32_bf16 v[120:123], v[36:39], v[44:47], v[120:123]\n"
"ds_read_b64_tr_b16 v[32:33], v16 offset:16424\n"
"ds_read_b64_tr_b16 v[34:35], v16 offset:18472\n"
"v_mfma_f32_16x16x32_bf16 v[152:155], v[36:39], v[48:51], v[152:155]\n"
"ds_read_b128 v[76:79], v21 offset:6144\n"
"v_mfma_f32_16x16x32_bf16 v[184:187], v[36:39], v[52:55], v[184:187]\n"
"buffer_load_dwordx4 v84, s[44:47], s73 offen lds\n"
"v_mfma_f32_16x16x32_bf16 v[216:219], v[36:39], v[56:59], v[216:219]\n"
"s_waitcnt lgkmcnt(8)\n"
"ds_read_b128 v[80:83], v23 offset:0\n"
"v_mfma_f32_16x16x32_bf16 v[124:127], v[40:43], v[44:47], v[124:127]\n"
"ds_read_b64_tr_b16 v[36:37], v17 offset:16416\n"
"ds_read_b64_tr_b16 v[38:39], v17 offset:18464\n"
"v_mfma_f32_16x16x32_bf16 v[156:159], v[40:43], v[48:51], v[156:159]\n"
"buffer_load_dwordx4 v85, s[44:47], s73 offen offset:1024 lds\n"
"v_mfma_f32_16x16x32_bf16 v[188:191], v[40:43], v[52:55], v[188:191]\n"
"v_mfma_f32_16x16x32_bf16 v[220:223], v[40:43], v[56:59], v[220:223]\n"
"buffer_load_dwordx4 v86, s[44:47], s73 offen offset:2048 lds\n"
"s_waitcnt lgkmcnt(2)\n"
"v_mfma_f32_16x16x32_bf16 v[96:99], v[28:31], v[64:67], v[96:99]\n"
"ds_read_b64_tr_b16 v[40:41], v17 offset:16424\n"
"ds_read_b64_tr_b16 v[42:43], v17 offset:18472\n"
"v_mfma_f32_16x16x32_bf16 v[128:131], v[28:31], v[68:71], v[128:131]\n"
"buffer_load_dwordx4 v87, s[44:47], s73 offen offset:3072 lds\n"
"v_mfma_f32_16x16x32_bf16 v[160:163], v[28:31], v[72:75], v[160:163]\n"
"v_mfma_f32_16x16x32_bf16 v[192:195], v[28:31], v[76:79], v[192:195]\n"
"s_add_u32 s73, s73, 262144\n"
"s_cmp_eq_u32 s73, 4718592\n"
"s_cselect_b32 s73, 0, s73\n"
"v_mfma_f32_16x16x32_bf16 v[224:227], v[28:31], v[80:83], v[224:227]\n"
"v_mfma_f32_16x16x32_bf16 v[100:103], v[32:35], v[64:67], v[100:103]\n"
"ds_read_b64_tr_b16 v[28:29], v18 offset:16416\n"
"ds_read_b64_tr_b16 v[30:31], v18 offset:18464\n"
"v_mfma_f32_16x16x32_bf16 v[132:135], v[32:35], v[68:71], v[132:135]\n"
"v_mfma_f32_16x16x32_bf16 v[164:167], v[32:35], v[72:75], v[164:167]\n"
"v_mfma_f32_16x16x32_bf16 v[196:199], v[32:35], v[76:79], v[196:199]\n"
"v_mfma_f32_16x16x32_bf16 v[228:231], v[32:35], v[80:83], v[228:231]\n"
"s_waitcnt lgkmcnt(4)\n"
"v_mfma_f32_16x16x32_bf16 v[104:107], v[36:39], v[64:67], v[104:107]\n"
"ds_read_b64_tr_b16 v[32:33], v18 offset:16424\n"
"ds_read_b64_tr_b16 v[34:35], v18 offset:18472\n"
"v_mfma_f32_16x16x32_bf16 v[136:139], v[36:39], v[68:71], v[136:139]\n"
"v_mfma_f32_16x16x32_bf16 v[168:171], v[36:39], v[72:75], v[168:171]\n"
"v_mfma_f32_16x16x32_bf16 v[200:203], v[36:39], v[76:79], v[200:203]\n"
"v_mfma_f32_16x16x32_bf16 v[232:235], v[36:39], v[80:83], v[232:235]\n"
"s_waitcnt lgkmcnt(4)\n"
"v_mfma_f32_16x16x32_bf16 v[108:111], v[40:43], v[64:67], v[108:111]\n"
"ds_read_b64_tr_b16 v[36:37], v19 offset:16416\n"
"ds_read_b64_tr_b16 v[38:39], v19 offset:18464\n"
"v_mfma_f32_16x16x32_bf16 v[140:143], v[40:43], v[68:71], v[140:143]\n"
"v_mfma_f32_16x16x32_bf16 v[172:175], v[40:43], v[72:75], v[172:175]\n"
"v_mfma_f32_16x16x32_bf16 v[204:207], v[40:43], v[76:79], v[204:207]\n"
"v_mfma_f32_16x16x32_bf16 v[236:239], v[40:43], v[80:83], v[236:239]\n"
"s_waitcnt lgkmcnt(4)\n"
"v_mfma_f32_16x16x32_bf16 v[112:115], v[28:31], v[64:67], v[112:115]\n"
"ds_read_b64_tr_b16 v[40:41], v19 offset:16424\n"
"ds_read_b64_tr_b16 v[42:43], v19 offset:18472\n"
"v_mfma_f32_16x16x32_bf16 v[144:147], v[28:31], v[68:71], v[144:147]\n"
"v_mfma_f32_16x16x32_bf16 v[176:179], v[28:31], v[72:75], v[176:179]\n"
"s_waitcnt vmcnt(4)\n"
"s_barrier\n"
"v_mfma_f32_16x16x32_bf16 v[208:211], v[28:31], v[76:79], v[208:211]\n"
"s_waitcnt lgkmcnt(4)\n"
"ds_read_b128 v[44:47], v20 offset:16384\n"
"v_mfma_f32_16x16x32_bf16 v[116:119], v[32:35], v[64:67], v[116:119]\n"
"ds_read_b64_tr_b16 v[28:29], v16 offset:32832\n"
"ds_read_b64_tr_b16 v[30:31], v16 offset:34880\n"
"v_mfma_f32_16x16x32_bf16 v[148:151], v[32:35], v[68:71], v[148:151]\n"
"ds_read_b128 v[48:51], v20 offset:18432\n"
"v_mfma_f32_16x16x32_bf16 v[180:183], v[32:35], v[72:75], v[180:183]\n"
"s_add_u32 m0, s64, 81920\n"
"v_mfma_f32_16x16x32_bf16 v[212:215], v[32:35], v[76:79], v[212:215]\n"
"s_waitcnt lgkmcnt(6)\n"
"ds_read_b128 v[52:55], v20 offset:20480\n"
"v_mfma_f32_16x16x32_bf16 v[120:123], v[36:39], v[64:67], v[120:123]\n"
"ds_read_b64_tr_b16 v[32:33], v16 offset:32840\n"
"ds_read_b64_tr_b16 v[34:35], v16 offset:34888\n"
"v_mfma_f32_16x16x32_bf16 v[152:155], v[36:39], v[68:71], v[152:155]\n"
"ds_read_b128 v[56:59], v20 offset:22528\n"
"v_mfma_f32_16x16x32_bf16 v[184:187], v[36:39], v[72:75], v[184:187]\n"
"buffer_load_dwordx4 v84, s[44:47], s73 offen lds\n"
"v_mfma_f32_16x16x32_bf16 v[216:219], v[36:39], v[76:79], v[216:219]\n"
"s_waitcnt lgkmcnt(8)\n"
"ds_read_b128 v[60:63], v22 offset:2048\n"
"v_mfma_f32_16x16x32_bf16 v[124:127], v[40:43], v[64:67], v[124:127]\n"
"ds_read_b64_tr_b16 v[36:37], v17 offset:32832\n"
"ds_read_b64_tr_b16 v[38:39], v17 offset:34880\n"
"v_mfma_f32_16x16x32_bf16 v[156:159], v[40:43], v[68:71], v[156:159]\n"
"buffer_load_dwordx4 v85, s[44:47], s73 offen offset:1024 lds\n"
"v_mfma_f32_16x16x32_bf16 v[188:191], v[40:43], v[72:75], v[188:191]\n"
"v_mfma_f32_16x16x32_bf16 v[220:223], v[40:43], v[76:79], v[220:223]\n"
"buffer_load_dwordx4 v86, s[44:47], s73 offen offset:2048 lds\n"
"s_waitcnt lgkmcnt(2)\n"
"v_mfma_f32_16x16x32_bf16 v[96:99], v[28:31], v[44:47], v[96:99]\n"
"ds_read_b64_tr_b16 v[40:41], v17 offset:32840\n"
"ds_read_b64_tr_b16 v[42:43], v17 offset:34888\n"
"v_mfma_f32_16x16x32_bf16 v[128:131], v[28:31], v[48:51], v[128:131]\n"
"buffer_load_dwordx4 v87, s[44:47], s73 offen offset:3072 lds\n"
"v_mfma_f32_16x16x32_bf16 v[160:163], v[28:31], v[52:55], v[160:163]\n"
"v_mfma_f32_16x16x32_bf16 v[192:195], v[28:31], v[56:59], v[192:195]\n"
"s_add_u32 s73, s73, 262144\n"
"s_cmp_eq_u32 s73, 4718592\n"
"s_cselect_b32 s73, 0, s73\n"
"s_add_u32 m0, s61, 32768\n"
"v_mfma_f32_16x16x32_bf16 v[224:227], v[28:31], v[60:63], v[224:227]\n"
"v_mfma_f32_16x16x32_bf16 v[100:103], v[32:35], v[44:47], v[100:103]\n"
"ds_read_b64_tr_b16 v[28:29], v18 offset:32832\n"
"ds_read_b64_tr_b16 v[30:31], v18 offset:34880\n"
"v_mfma_f32_16x16x32_bf16 v[132:135], v[32:35], v[48:51], v[132:135]\n"
"buffer_load_dwordx4 v88, s[40:43], s70 offen lds\n"
"v_mfma_f32_16x16x32_bf16 v[164:167], v[32:35], v[52:55], v[164:167]\n"
"v_mfma_f32_16x16x32_bf16 v[196:199], v[32:35], v[56:59], v[196:199]\n"
"buffer_load_dwordx4 v89, s[40:43], s70 offen offset:1024 lds\n"
"v_mfma_f32_16x16x32_bf16 v[228:231], v[32:35], v[60:63], v[228:231]\n"
"s_waitcnt lgkmcnt(4)\n"
"v_mfma_f32_16x16x32_bf16 v[104:107], v[36:39], v[44:47], v[104:107]\n"
"ds_read_b64_tr_b16 v[32:33], v18 offset:32840\n"
"ds_read_b64_tr_b16 v[34:35], v18 offset:34888\n"
"v_mfma_f32_16x16x32_bf16 v[136:139], v[36:39], v[48:51], v[136:139]\n"
"buffer_load_dwordx4 v90, s[40:43], s70 offen offset:2048 lds\n"
"v_mfma_f32_16x16x32_bf16 v[168:171], v[36:39], v[52:55], v[168:171]\n"
"v_mfma_f32_16x16x32_bf16 v[200:203], v[36:39], v[56:59], v[200:203]\n"
"buffer_load_dwordx4 v91, s[40:43], s70 offen offset:3072 lds\n"
"v_mfma_f32_16x16x32_bf16 v[232:235], v[36:39], v[60:63], v[232:235]\n"
"s_waitcnt lgkmcnt(4)\n"
"v_mfma_f32_16x16x32_bf16 v[108:111], v[40:43], v[44:47], v[108:111]\n"
"ds_read_b64_tr_b16 v[36:37], v19 offset:32832\n"
"ds_read_b64_tr_b16 v[38:39], v19 offset:34880\n"
"v_mfma_f32_16x16x32_bf16 v[140:143], v[40:43], v[48:51], v[140:143]\n"
"s_add_u32 m0, s63, 147712\n"
"v_mfma_f32_16x16x32_bf16 v[172:175], v[40:43], v[52:55], v[172:175]\n"
"buffer_load_dwordx4 v92, s[40:43], s70 offen lds\n"
"v_mfma_f32_16x16x32_bf16 v[204:207], v[40:43], v[56:59], v[204:207]\n"
"v_mfma_f32_16x16x32_bf16 v[236:239], v[40:43], v[60:63], v[236:239]\n"
"s_waitcnt lgkmcnt(4)\n"
"s_add_u32 s70, s70, 128\n"
"s_cmp_eq_u32 s70, 1152\n"
"s_cselect_b32 s70, 0, s70\n"
"v_mfma_f32_16x16x32_bf16 v[112:115], v[28:31], v[44:47], v[112:115]\n"
"ds_read_b64_tr_b16 v[40:41], v19 offset:32840\n"
"ds_read_b64_tr_b16 v[42:43], v19 offset:34888\n"
"v_mfma_f32_16x16x32_bf16 v[144:147], v[28:31], v[48:51], v[144:147]\n"
"v_mfma_f32_16x16x32_bf16 v[176:179], v[28:31], v[52:55], v[176:179]\n"
"s_waitcnt vmcnt(9)\n"
"s_barrier\n"
"v_mfma_f32_16x16x32_bf16 v[208:211], v[28:31], v[56:59], v[208:211]\n"
"s_waitcnt lgkmcnt(4)\n"
"ds_read_b128 v[64:67], v21 offset:16384\n"
"v_mfma_f32_16x16x32_bf16 v[116:119], v[32:35], v[44:47], v[116:119]\n"
"ds_read_b64_tr_b16 v[28:29], v16 offset:49248\n"
"ds_read_b64_tr_b16 v[30:31], v16 offset:51296\n"
"v_mfma_f32_16x16x32_bf16 v[148:151], v[32:35], v[48:51], v[148:151]\n"
"ds_read_b128 v[68:71], v21 offset:18432\n"
"v_mfma_f32_16x16x32_bf16 v[180:183], v[32:35], v[52:55], v[180:183]\n"
"s_add_u32 m0, s64, 98336\n"
"v_mfma_f32_16x16x32_bf16 v[212:215], v[32:35], v[56:59], v[212:215]\n"
"s_waitcnt lgkmcnt(6)\n"
"ds_read_b128 v[72:75], v21 offset:20480\n"
"v_mfma_f32_16x16x32_bf16 v[120:123], v[36:39], v[44:47], v[120:123]\n"
"ds_read_b64_tr_b16 v[32:33], v16 offset:49256\n"
"ds_read_b64_tr_b16 v[34:35], v16 offset:51304\n"
"v_mfma_f32_16x16x32_bf16 v[152:155], v[36:39], v[48:51], v[152:155]\n"
"ds_read_b128 v[76:79], v21 offset:22528\n"
"v_mfma_f32_16x16x32_bf16 v[184:187], v[36:39], v[52:55], v[184:187]\n"
"buffer_load_dwordx4 v84, s[44:47], s73 offen lds\n"
"v_mfma_f32_16x16x32_bf16 v[216:219], v[36:39], v[56:59], v[216:219]\n"
"s_waitcnt lgkmcnt(8)\n"
"ds_read_b128 v[80:83], v23 offset:2048\n"
"v_mfma_f32_16x16x32_bf16 v[124:127], v[40:43], v[44:47], v[124:127]\n"
"ds_read_b64_tr_b16 v[36:37], v17 offset:49248\n"
"ds_read_b64_tr_b16 v[38:39], v17 offset:51296\n"
"v_mfma_f32_16x16x32_bf16 v[156:159], v[40:43], v[48:51], v[156:159]\n"
"buffer_load_dwordx4 v85, s[44:47], s73 offen offset:1024 lds\n"
"v_mfma_f32_16x16x32_bf16 v[188:191], v[40:43], v[52:55], v[188:191]\n"
"v_mfma_f32_16x16x32_bf16 v[220:223], v[40:43], v[56:59], v[220:223]\n"
"buffer_load_dwordx4 v86, s[44:47], s73 offen offset:2048 lds\n"
"s_waitcnt lgkmcnt(2)\n"
"v_mfma_f32_16x16x32_bf16 v[96:99], v[28:31], v[64:67], v[96:99]\n"
"ds_read_b64_tr_b16 v[40:41], v17 offset:49256\n"
"ds_read_b64_tr_b16 v[42:43], v17 offset:51304\n"
"v_mfma_f32_16x16x32_bf16 v[128:131], v[28:31], v[68:71], v[128:131]\n"
"buffer_load_dwordx4 v87, s[44:47], s73 offen offset:3072 lds\n"
"v_mfma_f32_16x16x32_bf16 v[160:163], v[28:31], v[72:75], v[160:163]\n"
"v_mfma_f32_16x16x32_bf16 v[192:195], v[28:31], v[76:79], v[192:195]\n"
"s_add_u32 s73, s73, 262144\n"
"s_cmp_eq_u32 s73, 4718592\n"
"s_cselect_b32 s73, 0, s73\n"
"s_add_u32 m0, s61, 0\n"
"s_mov_b32 s65, 0\n"
"v_mfma_f32_16x16x32_bf16 v[224:227], v[28:31], v[80:83], v[224:227]\n"
"v_mfma_f32_16x16x32_bf16 v[100:103], v[32:35], v[64:67], v[100:103]\n"
"ds_read_b64_tr_b16 v[28:29], v18 offset:49248\n"
"ds_read_b64_tr_b16 v[30:31], v18 offset:51296\n"
"v_mfma_f32_16x16x32_bf16 v[132:135], v[32:35], v[68:71], v[132:135]\n"
"buffer_load_dwordx4 v24, s[48:51], s65 offen nt lds\n"
"v_mfma_f32_16x16x32_bf16 v[164:167], v[32:35], v[72:75], v[164:167]\n"
"v_mfma_f32_16x16x32_bf16 v[196:199], v[32:35], v[76:79], v[196:199]\n"
"buffer_load_dwordx4 v25, s[48:51], s65 offen offset:1024 nt lds\n"
"v_mfma_f32_16x16x32_bf16 v[228:231], v[32:35], v[80:83], v[228:231]\n"
"s_waitcnt lgkmcnt(4)\n"
"v_mfma_f32_16x16x32_bf16 v[104:107], v[36:39], v[64:67], v[104:107]\n"
"ds_read_b64_tr_b16 v[32:33], v18 offset:49256\n"
"ds_read_b64_tr_b16 v[34:35], v18 offset:51304\n"
"v_mfma_f32_16x16x32_bf16 v[136:139], v[36:39], v[68:71], v[136:139]\n"
"buffer_load_dwordx4 v93, s[48:51], s65 offen offset:2048 nt lds\n"
"v_mfma_f32_16x16x32_bf16 v[168:171], v[36:39], v[72:75], v[168:171]\n"
"v_mfma_f32_16x16x32_bf16 v[200:203], v[36:39], v[76:79], v[200:203]\n"
"buffer_load_dwordx4 v94, s[48:51], s65 offen offset:3072 nt lds\n"
"v_mfma_f32_16x16x32_bf16 v[232:235], v[36:39], v[80:83], v[232:235]\n"
"s_waitcnt lgkmcnt(4)\n"
"v_mfma_f32_16x16x32_bf16 v[108:111], v[40:43], v[64:67], v[108:111]\n"
"ds_read_b64_tr_b16 v[36:37], v19 offset:49248\n"
"ds_read_b64_tr_b16 v[38:39], v19 offset:51296\n"
"v_mfma_f32_16x16x32_bf16 v[140:143], v[40:43], v[68:71], v[140:143]\n"
"v_mfma_f32_16x16x32_bf16 v[172:175], v[40:43], v[72:75], v[172:175]\n"
"v_mfma_f32_16x16x32_bf16 v[204:207], v[40:43], v[76:79], v[204:207]\n"
"v_mfma_f32_16x16x32_bf16 v[236:239], v[40:43], v[80:83], v[236:239]\n"
"s_waitcnt lgkmcnt(4)\n"
"v_mfma_f32_16x16x32_bf16 v[112:115], v[28:31], v[64:67], v[112:115]\n"
"ds_read_b64_tr_b16 v[40:41], v19 offset:49256\n"
"ds_read_b64_tr_b16 v[42:43], v19 offset:51304\n"
"v_mfma_f32_16x16x32_bf16 v[144:147], v[28:31], v[68:71], v[144:147]\n"
"v_mfma_f32_16x16x32_bf16 v[176:179], v[28:31], v[72:75], v[176:179]\n"
"s_waitcnt vmcnt(8)\n"
"s_barrier\n"
"v_mfma_f32_16x16x32_bf16 v[208:211], v[28:31], v[76:79], v[208:211]\n"
"s_waitcnt lgkmcnt(4)\n"
"ds_read_b128 v[44:47], v20 offset:0\n"
"v_mfma_f32_16x16x32_bf16 v[116:119], v[32:35], v[64:67], v[116:119]\n"
"ds_read_b64_tr_b16 v[28:29], v16 offset:0\n"
"ds_read_b64_tr_b16 v[30:31], v16 offset:2048\n"
"v_mfma_f32_16x16x32_bf16 v[148:151], v[32:35], v[68:71], v[148:151]\n"
"ds_read_b128 v[48:51], v20 offset:2048\n"
"v_mfma_f32_16x16x32_bf16 v[180:183], v[32:35], v[72:75], v[180:183]\n"
"s_add_u32 m0, s64, 114752\n"
"v_mfma_f32_16x16x32_bf16 v[212:215], v[32:35], v[76:79], v[212:215]\n"
"s_waitcnt lgkmcnt(6)\n"
"ds_read_b128 v[52:55], v20 offset:4096\n"
"v_mfma_f32_16x16x32_bf16 v[120:123], v[36:39], v[64:67], v[120:123]\n"
"ds_read_b64_tr_b16 v[32:33], v16 offset:8\n"
"ds_read_b64_tr_b16 v[34:35], v16 offset:2056\n"
"v_mfma_f32_16x16x32_bf16 v[152:155], v[36:39], v[68:71], v[152:155]\n"
"ds_read_b128 v[56:59], v20 offset:6144\n"
"v_mfma_f32_16x16x32_bf16 v[184:187], v[36:39], v[72:75], v[184:187]\n"
"buffer_load_dwordx4 v84, s[44:47], s73 offen lds\n"
"v_mfma_f32_16x16x32_bf16 v[216:219], v[36:39], v[76:79], v[216:219]\n"
"s_waitcnt lgkmcnt(8)\n"
"ds_read_b128 v[60:63], v22 offset:0\n"
"v_mfma_f32_16x16x32_bf16 v[124:127], v[40:43], v[64:67], v[124:127]\n"
"ds_read_b64_tr_b16 v[36:37], v17 offset:0\n"
"ds_read_b64_tr_b16 v[38:39], v17 offset:2048\n"
"v_mfma_f32_16x16x32_bf16 v[156:159], v[40:43], v[68:71], v[156:159]\n"
"buffer_load_dwordx4 v85, s[44:47], s73 offen offset:1024 lds\n"
"v_mfma_f32_16x16x32_bf16 v[188:191], v[40:43], v[72:75], v[188:191]\n"
"v_mfma_f32_16x16x32_bf16 v[220:223], v[40:43], v[76:79], v[220:223]\n"
"buffer_load_dwordx4 v86, s[44:47], s73 offen offset:2048 lds\n"
"s_waitcnt lgkmcnt(2)\n"
"v_mfma_f32_16x16x32_bf16 v[96:99], v[28:31], v[44:47], v[96:99]\n"
"ds_read_b64_tr_b16 v[40:41], v17 offset:8\n"
"ds_read_b64_tr_b16 v[42:43], v17 offset:2056\n"
"v_mfma_f32_16x16x32_bf16 v[128:131], v[28:31], v[48:51], v[128:131]\n"
"buffer_load_dwordx4 v87, s[44:47], s73 offen offset:3072 lds\n"
"v_mfma_f32_16x16x32_bf16 v[160:163], v[28:31], v[52:55], v[160:163]\n"
"v_mfma_f32_16x16x32_bf16 v[192:195], v[28:31], v[56:59], v[192:195]\n"
"s_add_u32 s73, s73, 262144\n"
"s_cmp_eq_u32 s73, 4718592\n"
"s_cselect_b32 s73, 0, s73\n"
"s_add_u32 m0, s61, 49152\n"
"v_mfma_f32_16x16x32_bf16 v[224:227], v[28:31], v[60:63], v[224:227]\n"
"v_mfma_f32_16x16x32_bf16 v[100:103], v[32:35], v[44:47], v[100:103]\n"
"ds_read_b64_tr_b16 v[28:29], v18 offset:0\n"
"ds_read_b64_tr_b16 v[30:31], v18 offset:2048\n"
"v_mfma_f32_16x16x32_bf16 v[132:135], v[32:35], v[48:51], v[132:135]\n"
"buffer_load_dwordx4 v88, s[40:43], s70 offen lds\n"
"v_mfma_f32_16x16x32_bf16 v[164:167], v[32:35], v[52:55], v[164:167]\n"
"v_mfma_f32_16x16x32_bf16 v[196:199], v[32:35], v[56:59], v[196:199]\n"
"buffer_load_dwordx4 v89, s[40:43], s70 offen offset:1024 lds\n"
"v_mfma_f32_16x16x32_bf16 v[228:231], v[32:35], v[60:63], v[228:231]\n"
"s_waitcnt lgkmcnt(4)\n"
"v_mfma_f32_16x16x32_bf16 v[104:107], v[36:39], v[44:47], v[104:107]\n"
"ds_read_b64_tr_b16 v[32:33], v18 offset:8\n"
"ds_read_b64_tr_b16 v[34:35], v18 offset:2056\n"
"v_mfma_f32_16x16x32_bf16 v[136:139], v[36:39], v[48:51], v[136:139]\n"
"buffer_load_dwordx4 v90, s[40:43], s70 offen offset:2048 lds\n"
"v_mfma_f32_16x16x32_bf16 v[168:171], v[36:39], v[52:55], v[168:171]\n"
"v_mfma_f32_16x16x32_bf16 v[200:203], v[36:39], v[56:59], v[200:203]\n"
"buffer_load_dwordx4 v91, s[40:43], s70 offen offset:3072 lds\n"
"v_mfma_f32_16x16x32_bf16 v[232:235], v[36:39], v[60:63], v[232:235]\n"
"s_waitcnt lgkmcnt(4)\n"
"v_mfma_f32_16x16x32_bf16 v[108:111], v[40:43], v[44:47], v[108:111]\n"
"ds_read_b64_tr_b16 v[36:37], v19 offset:0\n"
"ds_read_b64_tr_b16 v[38:39], v19 offset:2048\n"
"v_mfma_f32_16x16x32_bf16 v[140:143], v[40:43], v[48:51], v[140:143]\n"
"s_add_u32 m0, s63, 149760\n"
"v_mfma_f32_16x16x32_bf16 v[172:175], v[40:43], v[52:55], v[172:175]\n"
"buffer_load_dwordx4 v92, s[40:43], s70 offen lds\n"
"v_mfma_f32_16x16x32_bf16 v[204:207], v[40:43], v[56:59], v[204:207]\n"
"v_mfma_f32_16x16x32_bf16 v[236:239], v[40:43], v[60:63], v[236:239]\n"
"s_waitcnt lgkmcnt(4)\n"
"s_add_u32 s70, s70, 128\n"
"s_cmp_eq_u32 s70, 1152\n"
"s_cselect_b32 s70, 0, s70\n"
"v_mfma_f32_16x16x32_bf16 v[112:115], v[28:31], v[44:47], v[112:115]\n"
"ds_read_b64_tr_b16 v[40:41], v19 offset:8\n"
"ds_read_b64_tr_b16 v[42:43], v19 offset:2056\n"
"v_mfma_f32_16x16x32_bf16 v[144:147], v[28:31], v[48:51], v[144:147]\n"
"v_mfma_f32_16x16x32_bf16 v[176:179], v[28:31], v[52:55], v[176:179]\n"
"s_waitcnt vmcnt(13)\n"
"s_barrier\n"
"v_mfma_f32_16x16x32_bf16 v[208:211], v[28:31], v[56:59], v[208:211]\n"
"s_waitcnt lgkmcnt(4)\n"
"ds_read_b128 v[64:67], v21 offset:0\n"
"v_mfma_f32_16x16x32_bf16 v[116:119], v[32:35], v[44:47], v[116:119]\n"
"ds_read_b64_tr_b16 v[28:29], v16 offset:16416\n"
"ds_read_b64_tr_b16 v[30:31], v16 offset:18464\n"
"v_mfma_f32_16x16x32_bf16 v[148:151], v[32:35], v[48:51], v[148:151]\n"
"ds_read_b128 v[68:71], v21 offset:2048\n"
"v_mfma_f32_16x16x32_bf16 v[180:183], v[32:35], v[52:55], v[180:183]\n"
"s_add_u32 m0, s64, 131168\n"
"v_mfma_f32_16x16x32_bf16 v[212:215], v[32:35], v[56:59], v[212:215]\n"
"s_waitcnt lgkmcnt(6)\n"
"ds_read_b128 v[72:75], v21 offset:4096\n"
"v_mfma_f32_16x16x32_bf16 v[120:123], v[36:39], v[44:47], v[120:123]\n"
"ds_read_b64_tr_b16 v[32:33], v16 offset:16424\n"
"ds_read_b64_tr_b16 v[34:35], v16 offset:18472\n"
"v_mfma_f32_16x16x32_bf16 v[152:155], v[36:39], v[48:51], v[152:155]\n"
"ds_read_b128 v[76:79], v21 offset:6144\n"
"v_mfma_f32_16x16x32_bf16 v[184:187], v[36:39], v[52:55], v[184:187]\n"
"buffer_load_dwordx4 v84, s[44:47], s73 offen lds\n"
"v_mfma_f32_16x16x32_bf16 v[216:219], v[36:39], v[56:59], v[216:219]\n"
"s_waitcnt lgkmcnt(8)\n"
"ds_read_b128 v[80:83], v23 offset:0\n"
"v_mfma_f32_16x16x32_bf16 v[124:127], v[40:43], v[44:47], v[124:127]\n"
"ds_read_b64_tr_b16 v[36:37], v17 offset:16416\n"
"ds_read_b64_tr_b16 v[38:39], v17 offset:18464\n"
"v_mfma_f32_16x16x32_bf16 v[156:159], v[40:43], v[48:51], v[156:159]\n"
"buffer_load_dwordx4 v85, s[44:47], s73 offen offset:1024 lds\n"
"v_mfma_f32_16x16x32_bf16 v[188:191], v[40:43], v[52:55], v[188:191]\n"
"v_mfma_f32_16x16x32_bf16 v[220:223], v[40:43], v[56:59], v[220:223]\n"
"buffer_load_dwordx4 v86, s[44:47], s73 offen offset:2048 lds\n"
"s_waitcnt lgkmcnt(2)\n"
"v_mfma_f32_16x16x32_bf16 v[96:99], v[28:31], v[64:67], v[96:99]\n"
"ds_read_b64_tr_b16 v[40:41], v17 offset:16424\n"
"ds_read_b64_tr_b16 v[42:43], v17 offset:18472\n"
"v_mfma_f32_16x16x32_bf16 v[128:131], v[28:31], v[68:71], v[128:131]\n"
"buffer_load_dwordx4 v87, s[44:47], s73 offen offset:3072 lds\n"
"v_mfma_f32_16x16x32_bf16 v[160:163], v[28:31], v[72:75], v[160:163]\n"
"v_mfma_f32_16x16x32_bf16 v[192:195], v[28:31], v[76:79], v[192:195]\n"
"s_add_u32 s73, s73, 262144\n"
"s_cmp_eq_u32 s73, 4718592\n"
"s_cselect_b32 s73, 0, s73\n"
"s_add_u32 m0, s61, 65536\n"
"s_mov_b32 s66, 0\n"
"v_mfma_f32_16x16x32_bf16 v[224:227], v[28:31], v[80:83], v[224:227]\n"
"v_mfma_f32_16x16x32_bf16 v[100:103], v[32:35], v[64:67], v[100:103]\n"
"ds_read_b64_tr_b16 v[28:29], v18 offset:16416\n"
"ds_read_b64_tr_b16 v[30:31], v18 offset:18464\n"
"v_mfma_f32_16x16x32_bf16 v[132:135], v[32:35], v[68:71], v[132:135]\n"
"buffer_load_dwordx4 v95, s[52:55], s66 offen nt lds\n"
"v_mfma_f32_16x16x32_bf16 v[164:167], v[32:35], v[72:75], v[164:167]\n"
"v_mfma_f32_16x16x32_bf16 v[196:199], v[32:35], v[76:79], v[196:199]\n"
"buffer_load_dwordx4 v27, s[52:55], s66 offen offset:1024 nt lds\n"
"v_mfma_f32_16x16x32_bf16 v[228:231], v[32:35], v[80:83], v[228:231]\n"
"s_waitcnt lgkmcnt(4)\n"
"v_mfma_f32_16x16x32_bf16 v[104:107], v[36:39], v[64:67], v[104:107]\n"
"ds_read_b64_tr_b16 v[32:33], v18 offset:16424\n"
"ds_read_b64_tr_b16 v[34:35], v18 offset:18472\n"
"v_mfma_f32_16x16x32_bf16 v[136:139], v[36:39], v[68:71], v[136:139]\n"
"buffer_load_dwordx4 v95, s[52:55], s66 offen offset:2048 nt lds\n"
"v_mfma_f32_16x16x32_bf16 v[168:171], v[36:39], v[72:75], v[168:171]\n"
"v_mfma_f32_16x16x32_bf16 v[200:203], v[36:39], v[76:79], v[200:203]\n"
"buffer_load_dwordx4 v27, s[52:55], s66 offen offset:3072 nt lds\n"
"v_mfma_f32_16x16x32_bf16 v[232:235], v[36:39], v[80:83], v[232:235]\n"
"s_waitcnt lgkmcnt(4)\n"
"v_mfma_f32_16x16x32_bf16 v[108:111], v[40:43], v[64:67], v[108:111]\n"
"ds_read_b64_tr_b16 v[36:37], v19 offset:16416\n"
"ds_read_b64_tr_b16 v[38:39], v19 offset:18464\n"
"v_mfma_f32_16x16x32_bf16 v[140:143], v[40:43], v[68:71], v[140:143]\n"
"v_mfma_f32_16x16x32_bf16 v[172:175], v[40:43], v[72:75], v[172:175]\n"
"v_mfma_f32_16x16x32_bf16 v[204:207], v[40:43], v[76:79], v[204:207]\n"
"v_mfma_f32_16x16x32_bf16 v[236:239], v[40:43], v[80:83], v[236:239]\n"
"s_waitcnt lgkmcnt(4)\n"
"v_mfma_f32_16x16x32_bf16 v[112:115], v[28:31], v[64:67], v[112:115]\n"
"ds_read_b64_tr_b16 v[40:41], v19 offset:16424\n"
"ds_read_b64_tr_b16 v[42:43], v19 offset:18472\n"
"v_mfma_f32_16x16x32_bf16 v[144:147], v[28:31], v[68:71], v[144:147]\n"
"v_mfma_f32_16x16x32_bf16 v[176:179], v[28:31], v[72:75], v[176:179]\n"
"s_waitcnt vmcnt(8)\n"
"s_barrier\n"
"v_mfma_f32_16x16x32_bf16 v[208:211], v[28:31], v[76:79], v[208:211]\n"
"s_waitcnt lgkmcnt(4)\n"
"ds_read_b128 v[44:47], v20 offset:16384\n"
"v_mfma_f32_16x16x32_bf16 v[116:119], v[32:35], v[64:67], v[116:119]\n"
"ds_read_b64_tr_b16 v[28:29], v16 offset:32832\n"
"ds_read_b64_tr_b16 v[30:31], v16 offset:34880\n"
"v_mfma_f32_16x16x32_bf16 v[148:151], v[32:35], v[68:71], v[148:151]\n"
"ds_read_b128 v[48:51], v20 offset:18432\n"
"v_mfma_f32_16x16x32_bf16 v[180:183], v[32:35], v[72:75], v[180:183]\n"
"s_add_u32 m0, s64, 81920\n"
"v_mfma_f32_16x16x32_bf16 v[212:215], v[32:35], v[76:79], v[212:215]\n"
"s_waitcnt lgkmcnt(6)\n"
"ds_read_b128 v[52:55], v20 offset:20480\n"
"v_mfma_f32_16x16x32_bf16 v[120:123], v[36:39], v[64:67], v[120:123]\n"
"ds_read_b64_tr_b16 v[32:33], v16 offset:32840\n"
"ds_read_b64_tr_b16 v[34:35], v16 offset:34888\n"
"v_mfma_f32_16x16x32_bf16 v[152:155], v[36:39], v[68:71], v[152:155]\n"
"ds_read_b128 v[56:59], v20 offset:22528\n"
"v_mfma_f32_16x16x32_bf16 v[184:187], v[36:39], v[72:75], v[184:187]\n"
"buffer_load_dwordx4 v84, s[44:47], s73 offen lds\n"
"v_mfma_f32_16x16x32_bf16 v[216:219], v[36:39], v[76:79], v[216:219]\n"
"s_waitcnt lgkmcnt(8)\n"
"ds_read_b128 v[60:63], v22 offset:2048\n"
"v_mfma_f32_16x16x32_bf16 v[124:127], v[40:43], v[64:67], v[124:127]\n"
"ds_read_b64_tr_b16 v[36:37], v17 offset:32832\n"
"ds_read_b64_tr_b16 v[38:39], v17 offset:34880\n"
"v_mfma_f32_16x16x32_bf16 v[156:159], v[40:43], v[68:71], v[156:159]\n"
"buffer_load_dwordx4 v85, s[44:47], s73 offen offset:1024 lds\n"
"v_mfma_f32_16x16x32_bf16 v[188:191], v[40:43], v[72:75], v[188:191]\n"
"v_mfma_f32_16x16x32_bf16 v[220:223], v[40:43], v[76:79], v[220:223]\n"
"buffer_load_dwordx4 v86, s[44:47], s73 offen offset:2048 lds\n"
"s_waitcnt lgkmcnt(2)\n"
"v_mfma_f32_16x16x32_bf16 v[96:99], v[28:31], v[44:47], v[96:99]\n"
"ds_read_b64_tr_b16 v[40:41], v17 offset:32840\n"
"ds_read_b64_tr_b16 v[42:43], v17 offset:34888\n"
"v_mfma_f32_16x16x32_bf16 v[128:131], v[28:31], v[48:51], v[128:131]\n"
"buffer_load_dwordx4 v87, s[44:47], s73 offen offset:3072 lds\n"
"v_mfma_f32_16x16x32_bf16 v[160:163], v[28:31], v[52:55], v[160:163]\n"
"v_mfma_f32_16x16x32_bf16 v[192:195], v[28:31], v[56:59], v[192:195]\n"
"s_add_u32 s73, s73, 262144\n"
"s_cmp_eq_u32 s73, 4718592\n"
"s_cselect_b32 s73, 0, s73\n"
"s_add_u32 m0, s61, 32768\n"
"v_mfma_f32_16x16x32_bf16 v[224:227], v[28:31], v[60:63], v[224:227]\n"
"v_mfma_f32_16x16x32_bf16 v[100:103], v[32:35], v[44:47], v[100:103]\n"
"ds_read_b64_tr_b16 v[28:29], v18 offset:32832\n"
"ds_read_b64_tr_b16 v[30:31], v18 offset:34880\n"
"v_mfma_f32_16x16x32_bf16 v[132:135], v[32:35], v[48:51], v[132:135]\n"
"buffer_load_dwordx4 v88, s[40:43], s70 offen lds\n"
"v_mfma_f32_16x16x32_bf16 v[164:167], v[32:35], v[52:55], v[164:167]\n"
"v_mfma_f32_16x16x32_bf16 v[196:199], v[32:35], v[56:59], v[196:199]\n"
"buffer_load_dwordx4 v89, s[40:43], s70 offen offset:1024 lds\n"
"v_mfma_f32_16x16x32_bf16 v[228:231], v[32:35], v[60:63], v[228:231]\n"
"s_waitcnt lgkmcnt(4)\n"
"v_mfma_f32_16x16x32_bf16 v[104:107], v[36:39], v[44:47], v[104:107]\n"
"ds_read_b64_tr_b16 v[32:33], v18 offset:32840\n"
"ds_read_b64_tr_b16 v[34:35], v18 offset:34888\n"
"v_mfma_f32_16x16x32_bf16 v[136:139], v[36:39], v[48:51], v[136:139]\n"
"buffer_load_dwordx4 v90, s[40:43], s70 offen offset:2048 lds\n"
"v_mfma_f32_16x16x32_bf16 v[168:171], v[36:39], v[52:55], v[168:171]\n"
"v_mfma_f32_16x16x32_bf16 v[200:203], v[36:39], v[56:59], v[200:203]\n"
"buffer_load_dwordx4 v91, s[40:43], s70 offen offset:3072 lds\n"
"v_mfma_f32_16x16x32_bf16 v[232:235], v[36:39], v[60:63], v[232:235]\n"
"s_waitcnt lgkmcnt(4)\n"
"v_mfma_f32_16x16x32_bf16 v[108:111], v[40:43], v[44:47], v[108:111]\n"
"ds_read_b64_tr_b16 v[36:37], v19 offset:32832\n"
"ds_read_b64_tr_b16 v[38:39], v19 offset:34880\n"
"v_mfma_f32_16x16x32_bf16 v[140:143], v[40:43], v[48:51], v[140:143]\n"
"s_add_u32 m0, s63, 147712\n"
"v_mfma_f32_16x16x32_bf16 v[172:175], v[40:43], v[52:55], v[172:175]\n"
"buffer_load_dwordx4 v92, s[40:43], s70 offen lds\n"
"v_mfma_f32_16x16x32_bf16 v[204:207], v[40:43], v[56:59], v[204:207]\n"
"v_mfma_f32_16x16x32_bf16 v[236:239], v[40:43], v[60:63], v[236:239]\n"
"s_waitcnt lgkmcnt(4)\n"
"s_add_u32 s70, s70, 128\n"
"s_cmp_eq_u32 s70, 1152\n"
"s_cselect_b32 s70, 0, s70\n"
"v_mfma_f32_16x16x32_bf16 v[112:115], v[28:31], v[44:47], v[112:115]\n"
"ds_read_b64_tr_b16 v[40:41], v19 offset:32840\n"
"ds_read_b64_tr_b16 v[42:43], v19 offset:34888\n"
"v_mfma_f32_16x16x32_bf16 v[144:147], v[28:31], v[48:51], v[144:147]\n"
"v_mfma_f32_16x16x32_bf16 v[176:179], v[28:31], v[52:55], v[176:179]\n"
"s_waitcnt vmcnt(13)\n"
"s_barrier\n"
"v_mfma_f32_16x16x32_bf16 v[208:211], v[28:31], v[56:59], v[208:211]\n"
"s_waitcnt lgkmcnt(4)\n"
"ds_read_b128 v[64:67], v21 offset:16384\n"
"v_mfma_f32_16x16x32_bf16 v[116:119], v[32:35], v[44:47], v[116:119]\n"
"ds_read_b64_tr_b16 v[28:29], v16 offset:49248\n"
"ds_read_b64_tr_b16 v[30:31], v16 offset:51296\n"
"v_mfma_f32_16x16x32_bf16 v[148:151], v[32:35], v[48:51], v[148:151]\n"
"ds_read_b128 v[68:71], v21 offset:18432\n"
"v_mfma_f32_16x16x32_bf16 v[180:183], v[32:35], v[52:55], v[180:183]\n"
"s_add_u32 m0, s64, 98336\n"
"v_mfma_f32_16x16x32_bf16 v[212:215], v[32:35], v[56:59], v[212:215]\n"
"s_waitcnt lgkmcnt(6)\n"
"ds_read_b128 v[72:75], v21 offset:20480\n"
"v_mfma_f32_16x16x32_bf16 v[120:123], v[36:39], v[44:47], v[120:123]\n"
"ds_read_b64_tr_b16 v[32:33], v16 offset:49256\n"
"ds_read_b64_tr_b16 v[34:35], v16 offset:51304\n"
"v_mfma_f32_16x16x32_bf16 v[152:155], v[36:39], v[48:51], v[152:155]\n"
"ds_read_b128 v[76:79], v21 offset:22528\n"
"v_mfma_f32_16x16x32_bf16 v[184:187], v[36:39], v[52:55], v[184:187]\n"
"buffer_load_dwordx4 v84, s[44:47], s73 offen lds\n"
"v_mfma_f32_16x16x32_bf16 v[216:219], v[36:39], v[56:59], v[216:219]\n"
"s_waitcnt lgkmcnt(8)\n"
"ds_read_b128 v[80:83], v23 offset:2048\n"
"v_mfma_f32_16x16x32_bf16 v[124:127], v[40:43], v[44:47], v[124:127]\n"
"ds_read_b64_tr_b16 v[36:37], v17 offset:49248\n"
"ds_read_b64_tr_b16 v[38:39], v17 offset:51296\n"
"v_mfma_f32_16x16x32_bf16 v[156:159], v[40:43], v[48:51], v[156:159]\n"
"buffer_load_dwordx4 v85, s[44:47], s73 offen offset:1024 lds\n"
"v_mfma_f32_16x16x32_bf16 v[188:191], v[40:43], v[52:55], v[188:191]\n"
"v_mfma_f32_16x16x32_bf16 v[220:223], v[40:43], v[56:59], v[220:223]\n"
"buffer_load_dwordx4 v86, s[44:47], s73 offen offset:2048 lds\n"
"s_waitcnt lgkmcnt(2)\n"
"v_mfma_f32_16x16x32_bf16 v[96:99], v[28:31], v[64:67], v[96:99]\n"
"ds_read_b64_tr_b16 v[40:41], v17 offset:49256\n"
"ds_read_b64_tr_b16 v[42:43], v17 offset:51304\n"
"v_mfma_f32_16x16x32_bf16 v[128:131], v[28:31], v[68:71], v[128:131]\n"
"buffer_load_dwordx4 v87, s[44:47], s73 offen offset:3072 lds\n"
"v_mfma_f32_16x16x32_bf16 v[160:163], v[28:31], v[72:75], v[160:163]\n"
"v_mfma_f32_16x16x32_bf16 v[192:195], v[28:31], v[76:79], v[192:195]\n"
"s_add_u32 s73, s73, 262144\n"
"s_cmp_eq_u32 s73, 4718592\n"
"s_cselect_b32 s73, 0, s73\n"
"s_add_u32 m0, s61, 16384\n"
"s_mov_b32 s65, 16384\n"
"v_mfma_f32_16x16x32_bf16 v[224:227], v[28:31], v[80:83], v[224:227]\n"
"v_mfma_f32_16x16x32_bf16 v[100:103], v[32:35], v[64:67], v[100:103]\n"
"ds_read_b64_tr_b16 v[28:29], v18 offset:49248\n"
"ds_read_b64_tr_b16 v[30:31], v18 offset:51296\n"
"v_mfma_f32_16x16x32_bf16 v[132:135], v[32:35], v[68:71], v[132:135]\n"
"buffer_load_dwordx4 v24, s[48:51], s65 offen nt lds\n"
"v_mfma_f32_16x16x32_bf16 v[164:167], v[32:35], v[72:75], v[164:167]\n"
"v_mfma_f32_16x16x32_bf16 v[196:199], v[32:35], v[76:79], v[196:199]\n"
"buffer_load_dwordx4 v25, s[48:51], s65 offen offset:1024 nt lds\n"
"v_mfma_f32_16x16x32_bf16 v[228:231], v[32:35], v[80:83], v[228:231]\n"
"s_waitcnt lgkmcnt(4)\n"
"v_mfma_f32_16x16x32_bf16 v[104:107], v[36:39], v[64:67], v[104:107]\n"
"ds_read_b64_tr_b16 v[32:33], v18 offset:49256\n"
"ds_read_b64_tr_b16 v[34:35], v18 offset:51304\n"
"v_mfma_f32_16x16x32_bf16 v[136:139], v[36:39], v[68:71], v[136:139]\n"
"buffer_load_dwordx4 v93, s[48:51], s65 offen offset:2048 nt lds\n"
"v_mfma_f32_16x16x32_bf16 v[168:171], v[36:39], v[72:75], v[168:171]\n"
"v_mfma_f32_16x16x32_bf16 v[200:203], v[36:39], v[76:79], v[200:203]\n"
"buffer_load_dwordx4 v94, s[48:51], s65 offen offset:3072 nt lds\n"
"v_mfma_f32_16x16x32_bf16 v[232:235], v[36:39], v[80:83], v[232:235]\n"
"s_waitcnt lgkmcnt(4)\n"
"v_mfma_f32_16x16x32_bf16 v[108:111], v[40:43], v[64:67], v[108:111]\n"
"ds_read_b64_tr_b16 v[36:37], v19 offset:49248\n"
"ds_read_b64_tr_b16 v[38:39], v19 offset:51296\n"
"v_mfma_f32_16x16x32_bf16 v[140:143], v[40:43], v[68:71], v[140:143]\n"
"v_mfma_f32_16x16x32_bf16 v[172:175], v[40:43], v[72:75], v[172:175]\n"
"v_mfma_f32_16x16x32_bf16 v[204:207], v[40:43], v[76:79], v[204:207]\n"
"v_mfma_f32_16x16x32_bf16 v[236:239], v[40:43], v[80:83], v[236:239]\n"
"s_waitcnt lgkmcnt(4)\n"
"v_mfma_f32_16x16x32_bf16 v[112:115], v[28:31], v[64:67], v[112:115]\n"
"ds_read_b64_tr_b16 v[40:41], v19 offset:49256\n"
"ds_read_b64_tr_b16 v[42:43], v19 offset:51304\n"
"v_mfma_f32_16x16x32_bf16 v[144:147], v[28:31], v[68:71], v[144:147]\n"
"v_mfma_f32_16x16x32_bf16 v[176:179], v[28:31], v[72:75], v[176:179]\n"
"s_waitcnt vmcnt(8)\n"
"s_barrier\n"
"v_mfma_f32_16x16x32_bf16 v[208:211], v[28:31], v[76:79], v[208:211]\n"
"s_waitcnt lgkmcnt(4)\n"
"ds_read_b128 v[44:47], v20 offset:0\n"
"v_mfma_f32_16x16x32_bf16 v[116:119], v[32:35], v[64:67], v[116:119]\n"
"ds_read_b64_tr_b16 v[28:29], v16 offset:0\n"
"ds_read_b64_tr_b16 v[30:31], v16 offset:2048\n"
"v_mfma_f32_16x16x32_bf16 v[148:151], v[32:35], v[68:71], v[148:151]\n"
"ds_read_b128 v[48:51], v20 offset:2048\n"
"v_mfma_f32_16x16x32_bf16 v[180:183], v[32:35], v[72:75], v[180:183]\n"
"s_add_u32 m0, s64, 114752\n"
"v_mfma_f32_16x16x32_bf16 v[212:215], v[32:35], v[76:79], v[212:215]\n"
"s_waitcnt lgkmcnt(6)\n"
"ds_read_b128 v[52:55], v20 offset:4096\n"
"v_mfma_f32_16x16x32_bf16 v[120:123], v[36:39], v[64:67], v[120:123]\n"
"ds_read_b64_tr_b16 v[32:33], v16 offset:8\n"
"ds_read_b64_tr_b16 v[34:35], v16 offset:2056\n"
"v_mfma_f32_16x16x32_bf16 v[152:155], v[36:39], v[68:71], v[152:155]\n"
"ds_read_b128 v[56:59], v20 offset:6144\n"
"v_mfma_f32_16x16x32_bf16 v[184:187], v[36:39], v[72:75], v[184:187]\n"
"buffer_load_dwordx4 v84, s[44:47], s73 offen lds\n"
"v_mfma_f32_16x16x32_bf16 v[216:219], v[36:39], v[76:79], v[216:219]\n"
"s_waitcnt lgkmcnt(8)\n"
"ds_read_b128 v[60:63], v22 offset:0\n"
"v_mfma_f32_16x16x32_bf16 v[124:127], v[40:43], v[64:67], v[124:127]\n"
"ds_read_b64_tr_b16 v[36:37], v17 offset:0\n"
"ds_read_b64_tr_b16 v[38:39], v17 offset:2048\n"
"v_mfma_f32_16x16x32_bf16 v[156:159], v[40:43], v[68:71], v[156:159]\n"
"buffer_load_dwordx4 v85, s[44:47], s73 offen offset:1024 lds\n"
"v_mfma_f32_16x16x32_bf16 v[188:191], v[40:43], v[72:75], v[188:191]\n"
"v_mfma_f32_16x16x32_bf16 v[220:223], v[40:43], v[76:79], v[220:223]\n"
"buffer_load_dwordx4 v86, s[44:47], s73 offen offset:2048 lds\n"
"s_waitcnt lgkmcnt(2)\n"
"v_mfma_f32_16x16x32_bf16 v[96:99], v[28:31], v[44:47], v[96:99]\n"
"ds_read_b64_tr_b16 v[40:41], v17 offset:8\n"
"ds_read_b64_tr_b16 v[42:43], v17 offset:2056\n"
"v_mfma_f32_16x16x32_bf16 v[128:131], v[28:31], v[48:51], v[128:131]\n"
"buffer_load_dwordx4 v87, s[44:47], s73 offen offset:3072 lds\n"
"v_mfma_f32_16x16x32_bf16 v[160:163], v[28:31], v[52:55], v[160:163]\n"
"v_mfma_f32_16x16x32_bf16 v[192:195], v[28:31], v[56:59], v[192:195]\n"
"s_add_u32 s73, s73, 262144\n"
"s_cmp_eq_u32 s73, 4718592\n"
"s_cselect_b32 s73, 0, s73\n"
"s_add_u32 m0, s61, 49152\n"
"v_mfma_f32_16x16x32_bf16 v[224:227], v[28:31], v[60:63], v[224:227]\n"
"v_mfma_f32_16x16x32_bf16 v[100:103], v[32:35], v[44:47], v[100:103]\n"
"ds_read_b64_tr_b16 v[28:29], v18 offset:0\n"
"ds_read_b64_tr_b16 v[30:31], v18 offset:2048\n"
"v_mfma_f32_16x16x32_bf16 v[132:135], v[32:35], v[48:51], v[132:135]\n"
"buffer_load_dwordx4 v88, s[40:43], s70 offen lds\n"
"v_mfma_f32_16x16x32_bf16 v[164:167], v[32:35], v[52:55], v[164:167]\n"
"v_mfma_f32_16x16x32_bf16 v[196:199], v[32:35], v[56:59], v[196:199]\n"
"buffer_load_dwordx4 v89, s[40:43], s70 offen offset:1024 lds\n"
"v_mfma_f32_16x16x32_bf16 v[228:231], v[32:35], v[60:63], v[228:231]\n"
"s_waitcnt lgkmcnt(4)\n"
"v_mfma_f32_16x16x32_bf16 v[104:107], v[36:39], v[44:47], v[104:107]\n"
"ds_read_b64_tr_b16 v[32:33], v18 offset:8\n"
"ds_read_b64_tr_b16 v[34:35], v18 offset:2056\n"
"v_mfma_f32_16x16x32_bf16 v[136:139], v[36:39], v[48:51], v[136:139]\n"
"buffer_load_dwordx4 v90, s[40:43], s70 offen offset:2048 lds\n"
"v_mfma_f32_16x16x32_bf16 v[168:171], v[36:39], v[52:55], v[168:171]\n"
"v_mfma_f32_16x16x32_bf16 v[200:203], v[36:39], v[56:59], v[200:203]\n"
"buffer_load_dwordx4 v91, s[40:43], s70 offen offset:3072 lds\n"
"v_mfma_f32_16x16x32_bf16 v[232:235], v[36:39], v[60:63], v[232:235]\n"
"s_waitcnt lgkmcnt(4)\n"
"v_mfma_f32_16x16x32_bf16 v[108:111], v[40:43], v[44:47], v[108:111]\n"
"ds_read_b64_tr_b16 v[36:37], v19 offset:0\n"
"ds_read_b64_tr_b16 v[38:39], v19 offset:2048\n"
"v_mfma_f32_16x16x32_bf16 v[140:143], v[40:43], v[48:51], v[140:143]\n"
"s_add_u32 m0, s63, 149760\n"
"v_mfma_f32_16x16x32_bf16 v[172:175], v[40:43], v[52:55], v[172:175]\n"
"buffer_load_dwordx4 v92, s[40:43], s70 offen lds\n"
"v_mfma_f32_16x16x32_bf16 v[204:207], v[40:43], v[56:59], v[204:207]\n"
"v_mfma_f32_16x16x32_bf16 v[236:239], v[40:43], v[60:63], v[236:239]\n"
"s_waitcnt lgkmcnt(4)\n"
"s_add_u32 s70, s70, 128\n"
"s_cmp_eq_u32 s70, 1152\n"
"s_cselect_b32 s70, 0, s70\n"
"v_mfma_f32_16x16x32_bf16 v[112:115], v[28:31], v[44:47], v[112:115]\n"
"ds_read_b64_tr_b16 v[40:41], v19 offset:8\n"
"ds_read_b64_tr_b16 v[42:43], v19 offset:2056\n"
"v_mfma_f32_16x16x32_bf16 v[144:147], v[28:31], v[48:51], v[144:147]\n"
"v_mfma_f32_16x16x32_bf16 v[176:179], v[28:31], v[52:55], v[176:179]\n"
"s_waitcnt vmcnt(13)\n"
"s_barrier\n"
"v_mfma_f32_16x16x32_bf16 v[208:211], v[28:31], v[56:59], v[208:211]\n"
"s_waitcnt lgkmcnt(4)\n"
"ds_read_b128 v[64:67], v21 offset:0\n"
"v_mfma_f32_16x16x32_bf16 v[116:119], v[32:35], v[44:47], v[116:119]\n"
"ds_read_b64_tr_b16 v[28:29], v16 offset:16416\n"
"ds_read_b64_tr_b16 v[30:31], v16 offset:18464\n"
"v_mfma_f32_16x16x32_bf16 v[148:151], v[32:35], v[48:51], v[148:151]\n"
"ds_read_b128 v[68:71], v21 offset:2048\n"
"v_mfma_f32_16x16x32_bf16 v[180:183], v[32:35], v[52:55], v[180:183]\n"
"s_add_u32 m0, s64, 131168\n"
"v_mfma_f32_16x16x32_bf16 v[212:215], v[32:35], v[56:59], v[212:215]\n"
"s_waitcnt lgkmcnt(6)\n"
"ds_read_b128 v[72:75], v21 offset:4096\n"
"v_mfma_f32_16x16x32_bf16 v[120:123], v[36:39], v[44:47], v[120:123]\n"
"ds_read_b64_tr_b16 v[32:33], v16 offset:16424\n"
"ds_read_b64_tr_b16 v[34:35], v16 offset:18472\n"
"v_mfma_f32_16x16x32_bf16 v[152:155], v[36:39], v[48:51], v[152:155]\n"
"ds_read_b128 v[76:79], v21 offset:6144\n"
"v_mfma_f32_16x16x32_bf16 v[184:187], v[36:39], v[52:55], v[184:187]\n"
"buffer_load_dwordx4 v84, s[44:47], s73 offen lds\n"
"v_mfma_f32_16x16x32_bf16 v[216:219], v[36:39], v[56:59], v[216:219]\n"
"s_waitcnt lgkmcnt(8)\n"
"ds_read_b128 v[80:83], v23 offset:0\n"
"v_mfma_f32_16x16x32_bf16 v[124:127], v[40:43], v[44:47], v[124:127]\n"
"ds_read_b64_tr_b16 v[36:37], v17 offset:16416\n"
"ds_read_b64_tr_b16 v[38:39], v17 offset:18464\n"
"v_mfma_f32_16x16x32_bf16 v[156:159], v[40:43], v[48:51], v[156:159]\n"
"buffer_load_dwordx4 v85, s[44:47], s73 offen offset:1024 lds\n"
"v_mfma_f32_16x16x32_bf16 v[188:191], v[40:43], v[52:55], v[188:191]\n"
"v_mfma_f32_16x16x32_bf16 v[220:223], v[40:43], v[56:59], v[220:223]\n"
"buffer_load_dwordx4 v86, s[44:47], s73 offen offset:2048 lds\n"
"s_waitcnt lgkmcnt(2)\n"
"v_mfma_f32_16x16x32_bf16 v[96:99], v[28:31], v[64:67], v[96:99]\n"
"ds_read_b64_tr_b16 v[40:41], v17 offset:16424\n"
"ds_read_b64_tr_b16 v[42:43], v17 offset:18472\n"
"v_mfma_f32_16x16x32_bf16 v[128:131], v[28:31], v[68:71], v[128:131]\n"
"buffer_load_dwordx4 v87, s[44:47], s73 offen offset:3072 lds\n"
"v_mfma_f32_16x16x32_bf16 v[160:163], v[28:31], v[72:75], v[160:163]\n"
"v_mfma_f32_16x16x32_bf16 v[192:195], v[28:31], v[76:79], v[192:195]\n"
"s_add_u32 s73, s73, 262144\n"
"s_cmp_eq_u32 s73, 4718592\n"
"s_cselect_b32 s73, 0, s73\n"
"v_mfma_f32_16x16x32_bf16 v[224:227], v[28:31], v[80:83], v[224:227]\n"
"v_mfma_f32_16x16x32_bf16 v[100:103], v[32:35], v[64:67], v[100:103]\n"
"ds_read_b64_tr_b16 v[28:29], v18 offset:16416\n"
"ds_read_b64_tr_b16 v[30:31], v18 offset:18464\n"
"v_mfma_f32_16x16x32_bf16 v[132:135], v[32:35], v[68:71], v[132:135]\n"
"v_mfma_f32_16x16x32_bf16 v[164:167], v[32:35], v[72:75], v[164:167]\n"
"v_mfma_f32_16x16x32_bf16 v[196:199], v[32:35], v[76:79], v[196:199]\n"
"v_mfma_f32_16x16x32_bf16 v[228:231], v[32:35], v[80:83], v[228:231]\n"
"s_waitcnt lgkmcnt(4)\n"
"v_mfma_f32_16x16x32_bf16 v[104:107], v[36:39], v[64:67], v[104:107]\n"
"ds_read_b64_tr_b16 v[32:33], v18 offset:16424\n"
"ds_read_b64_tr_b16 v[34:35], v18 offset:18472\n"
"v_mfma_f32_16x16x32_bf16 v[136:139], v[36:39], v[68:71], v[136:139]\n"
"v_mfma_f32_16x16x32_bf16 v[168:171], v[36:39], v[72:75], v[168:171]\n"
"v_mfma_f32_16x16x32_bf16 v[200:203], v[36:39], v[76:79], v[200:203]\n"
"v_mfma_f32_16x16x32_bf16 v[232:235], v[36:39], v[80:83], v[232:235]\n"
"s_waitcnt lgkmcnt(4)\n"
"v_mfma_f32_16x16x32_bf16 v[108:111], v[40:43], v[64:67], v[108:111]\n"
"ds_read_b64_tr_b16 v[36:37], v19 offset:16416\n"
"ds_read_b64_tr_b16 v[38:39], v19 offset:18464\n"
"v_mfma_f32_16x16x32_bf16 v[140:143], v[40:43], v[68:71], v[140:143]\n"
"v_mfma_f32_16x16x32_bf16 v[172:175], v[40:43], v[72:75], v[172:175]\n"
"v_mfma_f32_16x16x32_bf16 v[204:207], v[40:43], v[76:79], v[204:207]\n"
"v_mfma_f32_16x16x32_bf16 v[236:239], v[40:43], v[80:83], v[236:239]\n"
"s_waitcnt lgkmcnt(4)\n"
"v_mfma_f32_16x16x32_bf16 v[112:115], v[28:31], v[64:67], v[112:115]\n"
"ds_read_b64_tr_b16 v[40:41], v19 offset:16424\n"
"ds_read_b64_tr_b16 v[42:43], v19 offset:18472\n"
"v_mfma_f32_16x16x32_bf16 v[144:147], v[28:31], v[68:71], v[144:147]\n"
"v_mfma_f32_16x16x32_bf16 v[176:179], v[28:31], v[72:75], v[176:179]\n"
"s_waitcnt vmcnt(4)\n"
"s_barrier\n"
"v_mfma_f32_16x16x32_bf16 v[208:211], v[28:31], v[76:79], v[208:211]\n"
"s_waitcnt lgkmcnt(4)\n"
"ds_read_b128 v[44:47], v20 offset:16384\n"
"v_mfma_f32_16x16x32_bf16 v[116:119], v[32:35], v[64:67], v[116:119]\n"
"ds_read_b64_tr_b16 v[28:29], v16 offset:32832\n"
"ds_read_b64_tr_b16 v[30:31], v16 offset:34880\n"
"v_mfma_f32_16x16x32_bf16 v[148:151], v[32:35], v[68:71], v[148:151]\n"
"ds_read_b128 v[48:51], v20 offset:18432\n"
"v_mfma_f32_16x16x32_bf16 v[180:183], v[32:35], v[72:75], v[180:183]\n"
"s_add_u32 m0, s64, 81920\n"
"v_mfma_f32_16x16x32_bf16 v[212:215], v[32:35], v[76:79], v[212:215]\n"
"s_waitcnt lgkmcnt(6)\n"
"ds_read_b128 v[52:55], v20 offset:20480\n"
"v_mfma_f32_16x16x32_bf16 v[120:123], v[36:39], v[64:67], v[120:123]\n"
"ds_read_b64_tr_b16 v[32:33], v16 offset:32840\n"
"ds_read_b64_tr_b16 v[34:35], v16 offset:34888\n"
"v_mfma_f32_16x16x32_bf16 v[152:155], v[36:39], v[68:71], v[152:155]\n"
"ds_read_b128 v[56:59], v20 offset:22528\n"
"v_mfma_f32_16x16x32_bf16 v[184:187], v[36:39], v[72:75], v[184:187]\n"
"buffer_load_dwordx4 v84, s[44:47], s73 offen lds\n"
"v_mfma_f32_16x16x32_bf16 v[216:219], v[36:39], v[76:79], v[216:219]\n"
"s_waitcnt lgkmcnt(8)\n"
"ds_read_b128 v[60:63], v22 offset:2048\n"
"v_mfma_f32_16x16x32_bf16 v[124:127], v[40:43], v[64:67], v[124:127]\n"
"ds_read_b64_tr_b16 v[36:37], v17 offset:32832\n"
"ds_read_b64_tr_b16 v[38:39], v17 offset:34880\n"
"v_mfma_f32_16x16x32_bf16 v[156:159], v[40:43], v[68:71], v[156:159]\n"
"buffer_load_dwordx4 v85, s[44:47], s73 offen offset:1024 lds\n"
"v_mfma_f32_16x16x32_bf16 v[188:191], v[40:43], v[72:75], v[188:191]\n"
"v_mfma_f32_16x16x32_bf16 v[220:223], v[40:43], v[76:79], v[220:223]\n"
"buffer_load_dwordx4 v86, s[44:47], s73 offen offset:2048 lds\n"
"s_waitcnt lgkmcnt(2)\n"
"v_mfma_f32_16x16x32_bf16 v[96:99], v[28:31], v[44:47], v[96:99]\n"
"ds_read_b64_tr_b16 v[40:41], v17 offset:32840\n"
"ds_read_b64_tr_b16 v[42:43], v17 offset:34888\n"
"v_mfma_f32_16x16x32_bf16 v[128:131], v[28:31], v[48:51], v[128:131]\n"
"buffer_load_dwordx4 v87, s[44:47], s73 offen offset:3072 lds\n"
"v_mfma_f32_16x16x32_bf16 v[160:163], v[28:31], v[52:55], v[160:163]\n"
"v_mfma_f32_16x16x32_bf16 v[192:195], v[28:31], v[56:59], v[192:195]\n"
"s_add_u32 s73, s73, 262144\n"
"s_cmp_eq_u32 s73, 4718592\n"
"s_cselect_b32 s73, 0, s73\n"
"s_add_u32 m0, s61, 32768\n"
"v_mfma_f32_16x16x32_bf16 v[224:227], v[28:31], v[60:63], v[224:227]\n"
"v_mfma_f32_16x16x32_bf16 v[100:103], v[32:35], v[44:47], v[100:103]\n"
"ds_read_b64_tr_b16 v[28:29], v18 offset:32832\n"
"ds_read_b64_tr_b16 v[30:31], v18 offset:34880\n"
"v_mfma_f32_16x16x32_bf16 v[132:135], v[32:35], v[48:51], v[132:135]\n"
"buffer_load_dwordx4 v88, s[40:43], s70 offen lds\n"
"v_mfma_f32_16x16x32_bf16 v[164:167], v[32:35], v[52:55], v[164:167]\n"
"v_mfma_f32_16x16x32_bf16 v[196:199], v[32:35], v[56:59], v[196:199]\n"
"buffer_load_dwordx4 v89, s[40:43], s70 offen offset:1024 lds\n"
"v_mfma_f32_16x16x32_bf16 v[228:231], v[32:35], v[60:63], v[228:231]\n"
"s_waitcnt lgkmcnt(4)\n"
"v_mfma_f32_16x16x32_bf16 v[104:107], v[36:39], v[44:47], v[104:107]\n"
"ds_read_b64_tr_b16 v[32:33], v18 offset:32840\n"
"ds_read_b64_tr_b16 v[34:35], v18 offset:34888\n"
"v_mfma_f32_16x16x32_bf16 v[136:139], v[36:39], v[48:51], v[136:139]\n"
"buffer_load_dwordx4 v90, s[40:43], s70 offen offset:2048 lds\n"
"v_mfma_f32_16x16x32_bf16 v[168:171], v[36:39], v[52:55], v[168:171]\n"
"v_mfma_f32_16x16x32_bf16 v[200:203], v[36:39], v[56:59], v[200:203]\n"
"buffer_load_dwordx4 v91, s[40:43], s70 offen offset:3072 lds\n"
"v_mfma_f32_16x16x32_bf16 v[232:235], v[36:39], v[60:63], v[232:235]\n"
"s_waitcnt lgkmcnt(4)\n"
"v_mfma_f32_16x16x32_bf16 v[108:111], v[40:43], v[44:47], v[108:111]\n"
"ds_read_b64_tr_b16 v[36:37], v19 offset:32832\n"
"ds_read_b64_tr_b16 v[38:39], v19 offset:34880\n"
"v_mfma_f32_16x16x32_bf16 v[140:143], v[40:43], v[48:51], v[140:143]\n"
"s_add_u32 m0, s63, 147712\n"
"v_mfma_f32_16x16x32_bf16 v[172:175], v[40:43], v[52:55], v[172:175]\n"
"buffer_load_dwordx4 v92, s[40:43], s70 offen lds\n"
"v_mfma_f32_16x16x32_bf16 v[204:207], v[40:43], v[56:59], v[204:207]\n"
"v_mfma_f32_16x16x32_bf16 v[236:239], v[40:43], v[60:63], v[236:239]\n"
"s_waitcnt lgkmcnt(4)\n"
"s_add_u32 s70, s70, 128\n"
"s_cmp_eq_u32 s70, 1152\n"
"s_cselect_b32 s70, 0, s70\n"
"v_mfma_f32_16x16x32_bf16 v[112:115], v[28:31], v[44:47], v[112:115]\n"
"ds_read_b64_tr_b16 v[40:41], v19 offset:32840\n"
"ds_read_b64_tr_b16 v[42:43], v19 offset:34888\n"
"v_mfma_f32_16x16x32_bf16 v[144:147], v[28:31], v[48:51], v[144:147]\n"
"v_mfma_f32_16x16x32_bf16 v[176:179], v[28:31], v[52:55], v[176:179]\n"
"s_waitcnt vmcnt(9)\n"
"s_barrier\n"
"v_mfma_f32_16x16x32_bf16 v[208:211], v[28:31], v[56:59], v[208:211]\n"
"s_waitcnt lgkmcnt(4)\n"
"ds_read_b128 v[64:67], v21 offset:16384\n"
"v_mfma_f32_16x16x32_bf16 v[116:119], v[32:35], v[44:47], v[116:119]\n"
"ds_read_b64_tr_b16 v[28:29], v16 offset:49248\n"
"ds_read_b64_tr_b16 v[30:31], v16 offset:51296\n"
"v_mfma_f32_16x16x32_bf16 v[148:151], v[32:35], v[48:51], v[148:151]\n"
"ds_read_b128 v[68:71], v21 offset:18432\n"
"v_mfma_f32_16x16x32_bf16 v[180:183], v[32:35], v[52:55], v[180:183]\n"
"s_add_u32 m0, s64, 98336\n"
"v_mfma_f32_16x16x32_bf16 v[212:215], v[32:35], v[56:59], v[212:215]\n"
"s_waitcnt lgkmcnt(6)\n"
"ds_read_b128 v[72:75], v21 offset:20480\n"
"v_mfma_f32_16x16x32_bf16 v[120:123], v[36:39], v[44:47], v[120:123]\n"
"ds_read_b64_tr_b16 v[32:33], v16 offset:49256\n"
"ds_read_b64_tr_b16 v[34:35], v16 offset:51304\n"
"v_mfma_f32_16x16x32_bf16 v[152:155], v[36:39], v[48:51], v[152:155]\n"
"ds_read_b128 v[76:79], v21 offset:22528\n"
"v_mfma_f32_16x16x32_bf16 v[184:187], v[36:39], v[52:55], v[184:187]\n"
"buffer_load_dwordx4 v84, s[44:47], s73 offen lds\n"
"v_mfma_f32_16x16x32_bf16 v[216:219], v[36:39], v[56:59], v[216:219]\n"
"s_waitcnt lgkmcnt(8)\n"
"ds_read_b128 v[80:83], v23 offset:2048\n"
"v_mfma_f32_16x16x32_bf16 v[124:127], v[40:43], v[44:47], v[124:127]\n"
"ds_read_b64_tr_b16 v[36:37], v17 offset:49248\n"
"ds_read_b64_tr_b16 v[38:39], v17 offset:51296\n"
"v_mfma_f32_16x16x32_bf16 v[156:159], v[40:43], v[48:51], v[156:159]\n"
"buffer_load_dwordx4 v85, s[44:47], s73 offen offset:1024 lds\n"
"v_mfma_f32_16x16x32_bf16 v[188:191], v[40:43], v[52:55], v[188:191]\n"
"v_mfma_f32_16x16x32_bf16 v[220:223], v[40:43], v[56:59], v[220:223]\n"
"buffer_load_dwordx4 v86, s[44:47], s73 offen offset:2048 lds\n"
"s_waitcnt lgkmcnt(2)\n"
"v_mfma_f32_16x16x32_bf16 v[96:99], v[28:31], v[64:67], v[96:99]\n"
"ds_read_b64_tr_b16 v[40:41], v17 offset:49256\n"
"ds_read_b64_tr_b16 v[42:43], v17 offset:51304\n"
"v_mfma_f32_16x16x32_bf16 v[128:131], v[28:31], v[68:71], v[128:131]\n"
"buffer_load_dwordx4 v87, s[44:47], s73 offen offset:3072 lds\n"
"v_mfma_f32_16x16x32_bf16 v[160:163], v[28:31], v[72:75], v[160:163]\n"
"v_mfma_f32_16x16x32_bf16 v[192:195], v[28:31], v[76:79], v[192:195]\n"
"s_add_u32 s73, s73, 262144\n"
"s_cmp_eq_u32 s73, 4718592\n"
"s_cselect_b32 s73, 0, s73\n"
"v_mfma_f32_16x16x32_bf16 v[224:227], v[28:31], v[80:83], v[224:227]\n"
"v_mfma_f32_16x16x32_bf16 v[100:103], v[32:35], v[64:67], v[100:103]\n"
"ds_read_b64_tr_b16 v[28:29], v18 offset:49248\n"
"ds_read_b64_tr_b16 v[30:31], v18 offset:51296\n"
"v_mfma_f32_16x16x32_bf16 v[132:135], v[32:35], v[68:71], v[132:135]\n"
"v_mfma_f32_16x16x32_bf16 v[164:167], v[32:35], v[72:75], v[164:167]\n"
"v_mfma_f32_16x16x32_bf16 v[196:199], v[32:35], v[76:79], v[196:199]\n"
"v_mfma_f32_16x16x32_bf16 v[228:231], v[32:35], v[80:83], v[228:231]\n"
"s_waitcnt lgkmcnt(4)\n"
"v_mfma_f32_16x16x32_bf16 v[104:107], v[36:39], v[64:67], v[104:107]\n"
"ds_read_b64_tr_b16 v[32:33], v18 offset:49256\n"
"ds_read_b64_tr_b16 v[34:35], v18 offset:51304\n"
"v_mfma_f32_16x16x32_bf16 v[136:139], v[36:39], v[68:71], v[136:139]\n"
"v_mfma_f32_16x16x32_bf16 v[168:171], v[36:39], v[72:75], v[168:171]\n"
"v_mfma_f32_16x16x32_bf16 v[200:203], v[36:39], v[76:79], v[200:203]\n"
"v_mfma_f32_16x16x32_bf16 v[232:235], v[36:39], v[80:83], v[232:235]\n"
"s_waitcnt lgkmcnt(4)\n"
"v_mfma_f32_16x16x32_bf16 v[108:111], v[40:43], v[64:67], v[108:111]\n"
"ds_read_b64_tr_b16 v[36:37], v19 offset:49248\n"
"ds_read_b64_tr_b16 v[38:39], v19 offset:51296\n"
"v_mfma_f32_16x16x32_bf16 v[140:143], v[40:43], v[68:71], v[140:143]\n"
"v_mfma_f32_16x16x32_bf16 v[172:175], v[40:43], v[72:75], v[172:175]\n"
"v_mfma_f32_16x16x32_bf16 v[204:207], v[40:43], v[76:79], v[204:207]\n"
"v_mfma_f32_16x16x32_bf16 v[236:239], v[40:43], v[80:83], v[236:239]\n"
"s_waitcnt lgkmcnt(4)\n"
"v_mfma_f32_16x16x32_bf16 v[112:115], v[28:31], v[64:67], v[112:115]\n"
"ds_read_b64_tr_b16 v[40:41], v19 offset:49256\n"
"ds_read_b64_tr_b16 v[42:43], v19 offset:51304\n"
"v_mfma_f32_16x16x32_bf16 v[144:147], v[28:31], v[68:71], v[144:147]\n"
"v_mfma_f32_16x16x32_bf16 v[176:179], v[28:31], v[72:75], v[176:179]\n"
"s_waitcnt vmcnt(4)\n"
"s_barrier\n"
"v_mfma_f32_16x16x32_bf16 v[208:211], v[28:31], v[76:79], v[208:211]\n"
"s_waitcnt lgkmcnt(4)\n"
"ds_read_b128 v[44:47], v20 offset:0\n"
"v_mfma_f32_16x16x32_bf16 v[116:119], v[32:35], v[64:67], v[116:119]\n"
"ds_read_b64_tr_b16 v[28:29], v16 offset:0\n"
"ds_read_b64_tr_b16 v[30:31], v16 offset:2048\n"
"v_mfma_f32_16x16x32_bf16 v[148:151], v[32:35], v[68:71], v[148:151]\n"
"ds_read_b128 v[48:51], v20 offset:2048\n"
"v_mfma_f32_16x16x32_bf16 v[180:183], v[32:35], v[72:75], v[180:183]\n"
"s_add_u32 m0, s64, 114752\n"
"v_mfma_f32_16x16x32_bf16 v[212:215], v[32:35], v[76:79], v[212:215]\n"
"s_waitcnt lgkmcnt(6)\n"
"ds_read_b128 v[52:55], v20 offset:4096\n"
"v_mfma_f32_16x16x32_bf16 v[120:123], v[36:39], v[64:67], v[120:123]\n"
"ds_read_b64_tr_b16 v[32:33], v16 offset:8\n"
"ds_read_b64_tr_b16 v[34:35], v16 offset:2056\n"
"v_mfma_f32_16x16x32_bf16 v[152:155], v[36:39], v[68:71], v[152:155]\n"
"ds_read_b128 v[56:59], v20 offset:6144\n"
"v_mfma_f32_16x16x32_bf16 v[184:187], v[36:39], v[72:75], v[184:187]\n"
"buffer_load_dwordx4 v84, s[44:47], s73 offen lds\n"
"v_mfma_f32_16x16x32_bf16 v[216:219], v[36:39], v[76:79], v[216:219]\n"
"s_waitcnt lgkmcnt(8)\n"
"ds_read_b128 v[60:63], v22 offset:0\n"
"v_mfma_f32_16x16x32_bf16 v[124:127], v[40:43], v[64:67], v[124:127]\n"
"ds_read_b64_tr_b16 v[36:37], v17 offset:0\n"
"ds_read_b64_tr_b16 v[38:39], v17 offset:2048\n"
"v_mfma_f32_16x16x32_bf16 v[156:159], v[40:43], v[68:71], v[156:159]\n"
"buffer_load_dwordx4 v85, s[44:47], s73 offen offset:1024 lds\n"
"v_mfma_f32_16x16x32_bf16 v[188:191], v[40:43], v[72:75], v[188:191]\n"
"v_mfma_f32_16x16x32_bf16 v[220:223], v[40:43], v[76:79], v[220:223]\n"
"buffer_load_dwordx4 v86, s[44:47], s73 offen offset:2048 lds\n"
"s_waitcnt lgkmcnt(2)\n"
"v_mfma_f32_16x16x32_bf16 v[96:99], v[28:31], v[44:47], v[96:99]\n"
"ds_read_b64_tr_b16 v[40:41], v17 offset:8\n"
"ds_read_b64_tr_b16 v[42:43], v17 offset:2056\n"
"v_mfma_f32_16x16x32_bf16 v[128:131], v[28:31], v[48:51], v[128:131]\n"
"buffer_load_dwordx4 v87, s[44:47], s73 offen offset:3072 lds\n"
"v_mfma_f32_16x16x32_bf16 v[160:163], v[28:31], v[52:55], v[160:163]\n"
"v_mfma_f32_16x16x32_bf16 v[192:195], v[28:31], v[56:59], v[192:195]\n"
"s_add_u32 s73, s73, 262144\n"
"s_cmp_eq_u32 s73, 4718592\n"
"s_cselect_b32 s73, 0, s73\n"
"s_add_u32 m0, s61, 49152\n"
"v_mfma_f32_16x16x32_bf16 v[224:227], v[28:31], v[60:63], v[224:227]\n"
"v_mfma_f32_16x16x32_bf16 v[100:103], v[32:35], v[44:47], v[100:103]\n"
"ds_read_b64_tr_b16 v[28:29], v18 offset:0\n"
"ds_read_b64_tr_b16 v[30:31], v18 offset:2048\n"
"v_mfma_f32_16x16x32_bf16 v[132:135], v[32:35], v[48:51], v[132:135]\n"
"buffer_load_dwordx4 v88, s[40:43], s70 offen lds\n"
"v_mfma_f32_16x16x32_bf16 v[164:167], v[32:35], v[52:55], v[164:167]\n"
"v_mfma_f32_16x16x32_bf16 v[196:199], v[32:35], v[56:59], v[196:199]\n"
"buffer_load_dwordx4 v89, s[40:43], s70 offen offset:1024 lds\n"
"v_mfma_f32_16x16x32_bf16 v[228:231], v[32:35], v[60:63], v[228:231]\n"
"s_waitcnt lgkmcnt(4)\n"
"v_mfma_f32_16x16x32_bf16 v[104:107], v[36:39], v[44:47], v[104:107]\n"
"ds_read_b64_tr_b16 v[32:33], v18 offset:8\n"
"ds_read_b64_tr_b16 v[34:35], v18 offset:2056\n"
"v_mfma_f32_16x16x32_bf16 v[136:139], v[36:39], v[48:51], v[136:139]\n"
"buffer_load_dwordx4 v90, s[40:43], s70 offen offset:2048 lds\n"
"v_mfma_f32_16x16x32_bf16 v[168:171], v[36:39], v[52:55], v[168:171]\n"
"v_mfma_f32_16x16x32_bf16 v[200:203], v[36:39], v[56:59], v[200:203]\n"
"buffer_load_dwordx4 v91, s[40:43], s70 offen offset:3072 lds\n"
"v_mfma_f32_16x16x32_bf16 v[232:235], v[36:39], v[60:63], v[232:235]\n"
"s_waitcnt lgkmcnt(4)\n"
"v_mfma_f32_16x16x32_bf16 v[108:111], v[40:43], v[44:47], v[108:111]\n"
"ds_read_b64_tr_b16 v[36:37], v19 offset:0\n"
"ds_read_b64_tr_b16 v[38:39], v19 offset:2048\n"
"v_mfma_f32_16x16x32_bf16 v[140:143], v[40:43], v[48:51], v[140:143]\n"
"s_add_u32 m0, s63, 149760\n"
"v_mfma_f32_16x16x32_bf16 v[172:175], v[40:43], v[52:55], v[172:175]\n"
"buffer_load_dwordx4 v92, s[40:43], s70 offen lds\n"
"v_mfma_f32_16x16x32_bf16 v[204:207], v[40:43], v[56:59], v[204:207]\n"
"v_mfma_f32_16x16x32_bf16 v[236:239], v[40:43], v[60:63], v[236:239]\n"
"s_waitcnt lgkmcnt(4)\n"
"s_add_u32 s70, s70, 128\n"
"s_cmp_eq_u32 s70, 1152\n"
"s_cselect_b32 s70, 0, s70\n"
"v_mfma_f32_16x16x32_bf16 v[112:115], v[28:31], v[44:47], v[112:115]\n"
"ds_read_b64_tr_b16 v[40:41], v19 offset:8\n"
"ds_read_b64_tr_b16 v[42:43], v19 offset:2056\n"
"v_mfma_f32_16x16x32_bf16 v[144:147], v[28:31], v[48:51], v[144:147]\n"
"v_mfma_f32_16x16x32_bf16 v[176:179], v[28:31], v[52:55], v[176:179]\n"
"s_waitcnt vmcnt(9)\n"
"s_barrier\n"
"v_mfma_f32_16x16x32_bf16 v[208:211], v[28:31], v[56:59], v[208:211]\n"
"s_waitcnt lgkmcnt(4)\n"
"ds_read_b128 v[64:67], v21 offset:0\n"
"v_mfma_f32_16x16x32_bf16 v[116:119], v[32:35], v[44:47], v[116:119]\n"
"ds_read_b64_tr_b16 v[28:29], v16 offset:16416\n"
"ds_read_b64_tr_b16 v[30:31], v16 offset:18464\n"
"v_mfma_f32_16x16x32_bf16 v[148:151], v[32:35], v[48:51], v[148:151]\n"
"ds_read_b128 v[68:71], v21 offset:2048\n"
"v_mfma_f32_16x16x32_bf16 v[180:183], v[32:35], v[52:55], v[180:183]\n"
"s_add_u32 m0, s64, 131168\n"
"v_mfma_f32_16x16x32_bf16 v[212:215], v[32:35], v[56:59], v[212:215]\n"
"s_waitcnt lgkmcnt(6)\n"
"ds_read_b128 v[72:75], v21 offset:4096\n"
"v_mfma_f32_16x16x32_bf16 v[120:123], v[36:39], v[44:47], v[120:123]\n"
"ds_read_b64_tr_b16 v[32:33], v16 offset:16424\n"
"ds_read_b64_tr_b16 v[34:35], v16 offset:18472\n"
"v_mfma_f32_16x16x32_bf16 v[152:155], v[36:39], v[48:51], v[152:155]\n"
"ds_read_b128 v[76:79], v21 offset:6144\n"
"v_mfma_f32_16x16x32_bf16 v[184:187], v[36:39], v[52:55], v[184:187]\n"
"buffer_load_dwordx4 v84, s[44:47], s73 offen lds\n"
"v_mfma_f32_16x16x32_bf16 v[216:219], v[36:39], v[56:59], v[216:219]\n"
"s_waitcnt lgkmcnt(8)\n"
"ds_read_b128 v[80:83], v23 offset:0\n"
"v_mfma_f32_16x16x32_bf16 v[124:127], v[40:43], v[44:47], v[124:127]\n"
"ds_read_b64_tr_b16 v[36:37], v17 offset:16416\n"
"ds_read_b64_tr_b16 v[38:39], v17 offset:18464\n"
"v_mfma_f32_16x16x32_bf16 v[156:159], v[40:43], v[48:51], v[156:159]\n"
"buffer_load_dwordx4 v85, s[44:47], s73 offen offset:1024 lds\n"
"v_mfma_f32_16x16x32_bf16 v[188:191], v[40:43], v[52:55], v[188:191]\n"
"v_mfma_f32_16x16x32_bf16 v[220:223], v[40:43], v[56:59], v[220:223]\n"
"buffer_load_dwordx4 v86, s[44:47], s73 offen offset:2048 lds\n"
"s_waitcnt lgkmcnt(2)\n"
"v_mfma_f32_16x16x32_bf16 v[96:99], v[28:31], v[64:67], v[96:99]\n"
"ds_read_b64_tr_b16 v[40:41], v17 offset:16424\n"
"ds_read_b64_tr_b16 v[42:43], v17 offset:18472\n"
"v_mfma_f32_16x16x32_bf16 v[128:131], v[28:31], v[68:71], v[128:131]\n"
"buffer_load_dwordx4 v87, s[44:47], s73 offen offset:3072 lds\n"
"v_mfma_f32_16x16x32_bf16 v[160:163], v[28:31], v[72:75], v[160:163]\n"
"v_mfma_f32_16x16x32_bf16 v[192:195], v[28:31], v[76:79], v[192:195]\n"
"s_add_u32 s73, s73, 262144\n"
"s_cmp_eq_u32 s73, 4718592\n"
"s_cselect_b32 s73, 0, s73\n"
"v_mfma_f32_16x16x32_bf16 v[224:227], v[28:31], v[80:83], v[224:227]\n"
"v_mfma_f32_16x16x32_bf16 v[100:103], v[32:35], v[64:67], v[100:103]\n"
"ds_read_b64_tr_b16 v[28:29], v18 offset:16416\n"
"ds_read_b64_tr_b16 v[30:31], v18 offset:18464\n"
"v_mfma_f32_16x16x32_bf16 v[132:135], v[32:35], v[68:71], v[132:135]\n"
"v_mfma_f32_16x16x32_bf16 v[164:167], v[32:35], v[72:75], v[164:167]\n"
"v_mfma_f32_16x16x32_bf16 v[196:199], v[32:35], v[76:79], v[196:199]\n"
"v_mfma_f32_16x16x32_bf16 v[228:231], v[32:35], v[80:83], v[228:231]\n"
"s_waitcnt lgkmcnt(4)\n"
"v_mfma_f32_16x16x32_bf16 v[104:107], v[36:39], v[64:67], v[104:107]\n"
"ds_read_b64_tr_b16 v[32:33], v18 offset:16424\n"
"ds_read_b64_tr_b16 v[34:35], v18 offset:18472\n"
"v_mfma_f32_16x16x32_bf16 v[136:139], v[36:39], v[68:71], v[136:139]\n"
"v_mfma_f32_16x16x32_bf16 v[168:171], v[36:39], v[72:75], v[168:171]\n"
"v_mfma_f32_16x16x32_bf16 v[200:203], v[36:39], v[76:79], v[200:203]\n"
"v_mfma_f32_16x16x32_bf16 v[232:235], v[36:39], v[80:83], v[232:235]\n"
"s_waitcnt lgkmcnt(4)\n"
"v_mfma_f32_16x16x32_bf16 v[108:111], v[40:43], v[64:67], v[108:111]\n"
"ds_read_b64_tr_b16 v[36:37], v19 offset:16416\n"
"ds_read_b64_tr_b16 v[38:39], v19 offset:18464\n"
"v_mfma_f32_16x16x32_bf16 v[140:143], v[40:43], v[68:71], v[140:143]\n"
"v_mfma_f32_16x16x32_bf16 v[172:175], v[40:43], v[72:75], v[172:175]\n"
"v_mfma_f32_16x16x32_bf16 v[204:207], v[40:43], v[76:79], v[204:207]\n"
"v_mfma_f32_16x16x32_bf16 v[236:239], v[40:43], v[80:83], v[236:239]\n"
"s_waitcnt lgkmcnt(4)\n"
"v_mfma_f32_16x16x32_bf16 v[112:115], v[28:31], v[64:67], v[112:115]\n"
"ds_read_b64_tr_b16 v[40:41], v19 offset:16424\n"
"ds_read_b64_tr_b16 v[42:43], v19 offset:18472\n"
"v_mfma_f32_16x16x32_bf16 v[144:147], v[28:31], v[68:71], v[144:147]\n"
"v_mfma_f32_16x16x32_bf16 v[176:179], v[28:31], v[72:75], v[176:179]\n"
"s_waitcnt vmcnt(4)\n"
"s_barrier\n"
"v_mfma_f32_16x16x32_bf16 v[208:211], v[28:31], v[76:79], v[208:211]\n"
"s_waitcnt lgkmcnt(4)\n"
"ds_read_b128 v[44:47], v20 offset:16384\n"
"v_mfma_f32_16x16x32_bf16 v[116:119], v[32:35], v[64:67], v[116:119]\n"
"ds_read_b64_tr_b16 v[28:29], v16 offset:32832\n"
"ds_read_b64_tr_b16 v[30:31], v16 offset:34880\n"
"v_mfma_f32_16x16x32_bf16 v[148:151], v[32:35], v[68:71], v[148:151]\n"
"ds_read_b128 v[48:51], v20 offset:18432\n"
"v_mfma_f32_16x16x32_bf16 v[180:183], v[32:35], v[72:75], v[180:183]\n"
"s_add_u32 m0, s61, 81920\n"
"s_mov_b32 s66, 16384\n"
"v_mfma_f32_16x16x32_bf16 v[212:215], v[32:35], v[76:79], v[212:215]\n"
"s_waitcnt lgkmcnt(6)\n"
"ds_read_b128 v[52:55], v20 offset:20480\n"
"v_mfma_f32_16x16x32_bf16 v[120:123], v[36:39], v[64:67], v[120:123]\n"
"ds_read_b64_tr_b16 v[32:33], v16 offset:32840\n"
"ds_read_b64_tr_b16 v[34:35], v16 offset:34888\n"
"v_mfma_f32_16x16x32_bf16 v[152:155], v[36:39], v[68:71], v[152:155]\n"
"ds_read_b128 v[56:59], v20 offset:22528\n"
"v_mfma_f32_16x16x32_bf16 v[184:187], v[36:39], v[72:75], v[184:187]\n"
"buffer_load_dwordx4 v95, s[52:55], s66 offen nt lds\n"
"v_mfma_f32_16x16x32_bf16 v[216:219], v[36:39], v[76:79], v[216:219]\n"
"s_waitcnt lgkmcnt(8)\n"
"ds_read_b128 v[60:63], v22 offset:2048\n"
"v_mfma_f32_16x16x32_bf16 v[124:127], v[40:43], v[64:67], v[124:127]\n"
"ds_read_b64_tr_b16 v[36:37], v17 offset:32832\n"
"ds_read_b64_tr_b16 v[38:39], v17 offset:34880\n"
"v_mfma_f32_16x16x32_bf16 v[156:159], v[40:43], v[68:71], v[156:159]\n"
"buffer_load_dwordx4 v27, s[52:55], s66 offen offset:1024 nt lds\n"
"v_mfma_f32_16x16x32_bf16 v[188:191], v[40:43], v[72:75], v[188:191]\n"
"v_mfma_f32_16x16x32_bf16 v[220:223], v[40:43], v[76:79], v[220:223]\n"
"buffer_load_dwordx4 v95, s[52:55], s66 offen offset:2048 nt lds\n"
"s_waitcnt lgkmcnt(2)\n"
"v_mfma_f32_16x16x32_bf16 v[96:99], v[28:31], v[44:47], v[96:99]\n"
"ds_read_b64_tr_b16 v[40:41], v17 offset:32840\n"
"ds_read_b64_tr_b16 v[42:43], v17 offset:34888\n"
"v_mfma_f32_16x16x32_bf16 v[128:131], v[28:31], v[48:51], v[128:131]\n"
"buffer_load_dwordx4 v27, s[52:55], s66 offen offset:3072 nt lds\n"
"v_mfma_f32_16x16x32_bf16 v[160:163], v[28:31], v[52:55], v[160:163]\n"
"v_mfma_f32_16x16x32_bf16 v[192:195], v[28:31], v[56:59], v[192:195]\n"
"s_add_u32 m0, s61, 32768\n"
"s_mov_b32 s65, 32768\n"
"v_mfma_f32_16x16x32_bf16 v[224:227], v[28:31], v[60:63], v[224:227]\n"
"v_mfma_f32_16x16x32_bf16 v[100:103], v[32:35], v[44:47], v[100:103]\n"
"ds_read_b64_tr_b16 v[28:29], v18 offset:32832\n"
"ds_read_b64_tr_b16 v[30:31], v18 offset:34880\n"
"v_mfma_f32_16x16x32_bf16 v[132:135], v[32:35], v[48:51], v[132:135]\n"
"buffer_load_dwordx4 v24, s[48:51], s65 offen nt lds\n"
"v_mfma_f32_16x16x32_bf16 v[164:167], v[32:35], v[52:55], v[164:167]\n"
"v_mfma_f32_16x16x32_bf16 v[196:199], v[32:35], v[56:59], v[196:199]\n"
"buffer_load_dwordx4 v25, s[48:51], s65 offen offset:1024 nt lds\n"
"v_mfma_f32_16x16x32_bf16 v[228:231], v[32:35], v[60:63], v[228:231]\n"
"s_waitcnt lgkmcnt(4)\n"
"v_mfma_f32_16x16x32_bf16 v[104:107], v[36:39], v[44:47], v[104:107]\n"
"ds_read_b64_tr_b16 v[32:33], v18 offset:32840\n"
"ds_read_b64_tr_b16 v[34:35], v18 offset:34888\n"
"v_mfma_f32_16x16x32_bf16 v[136:139], v[36:39], v[48:51], v[136:139]\n"
"buffer_load_dwordx4 v93, s[48:51], s65 offen offset:2048 nt lds\n"
"v_mfma_f32_16x16x32_bf16 v[168:171], v[36:39], v[52:55], v[168:171]\n"
"v_mfma_f32_16x16x32_bf16 v[200:203], v[36:39], v[56:59], v[200:203]\n"
"buffer_load_dwordx4 v94, s[48:51], s65 offen offset:3072 nt lds\n"
"v_mfma_f32_16x16x32_bf16 v[232:235], v[36:39], v[60:63], v[232:235]\n"
"s_waitcnt lgkmcnt(4)\n"
"v_mfma_f32_16x16x32_bf16 v[108:111], v[40:43], v[44:47], v[108:111]\n"
"ds_read_b64_tr_b16 v[36:37], v19 offset:32832\n"
"ds_read_b64_tr_b16 v[38:39], v19 offset:34880\n"
"v_mfma_f32_16x16x32_bf16 v[140:143], v[40:43], v[48:51], v[140:143]\n"
"v_mfma_f32_16x16x32_bf16 v[172:175], v[40:43], v[52:55], v[172:175]\n"
"v_mfma_f32_16x16x32_bf16 v[204:207], v[40:43], v[56:59], v[204:207]\n"
"v_mfma_f32_16x16x32_bf16 v[236:239], v[40:43], v[60:63], v[236:239]\n"
"s_waitcnt lgkmcnt(4)\n"
"v_mfma_f32_16x16x32_bf16 v[112:115], v[28:31], v[44:47], v[112:115]\n"
"ds_read_b64_tr_b16 v[40:41], v19 offset:32840\n"
"ds_read_b64_tr_b16 v[42:43], v19 offset:34888\n"
"v_mfma_f32_16x16x32_bf16 v[144:147], v[28:31], v[48:51], v[144:147]\n"
"v_mfma_f32_16x16x32_bf16 v[176:179], v[28:31], v[52:55], v[176:179]\n"
"s_waitcnt vmcnt(8)\n"
"s_barrier\n"
"v_mfma_f32_16x16x32_bf16 v[208:211], v[28:31], v[56:59], v[208:211]\n"
"s_waitcnt lgkmcnt(4)\n"
"ds_read_b128 v[64:67], v21 offset:16384\n"
"v_mfma_f32_16x16x32_bf16 v[116:119], v[32:35], v[44:47], v[116:119]\n"
"ds_read_b64_tr_b16 v[28:29], v16 offset:49248\n"
"ds_read_b64_tr_b16 v[30:31], v16 offset:51296\n"
"v_mfma_f32_16x16x32_bf16 v[148:151], v[32:35], v[48:51], v[148:151]\n"
"ds_read_b128 v[68:71], v21 offset:18432\n"
"v_mfma_f32_16x16x32_bf16 v[180:183], v[32:35], v[52:55], v[180:183]\n"
"s_add_u32 m0, s61, 98304\n"
"s_mov_b32 s66, 32768\n"
"v_mfma_f32_16x16x32_bf16 v[212:215], v[32:35], v[56:59], v[212:215]\n"
"s_waitcnt lgkmcnt(6)\n"
"ds_read_b128 v[72:75], v21 offset:20480\n"
"v_mfma_f32_16x16x32_bf16 v[120:123], v[36:39], v[44:47], v[120:123]\n"
"ds_read_b64_tr_b16 v[32:33], v16 offset:49256\n"
"ds_read_b64_tr_b16 v[34:35], v16 offset:51304\n"
"v_mfma_f32_16x16x32_bf16 v[152:155], v[36:39], v[48:51], v[152:155]\n"
"ds_read_b128 v[76:79], v21 offset:22528\n"
"v_mfma_f32_16x16x32_bf16 v[184:187], v[36:39], v[52:55], v[184:187]\n"
"buffer_load_dwordx4 v95, s[52:55], s66 offen nt lds\n"
"v_mfma_f32_16x16x32_bf16 v[216:219], v[36:39], v[56:59], v[216:219]\n"
"s_waitcnt lgkmcnt(8)\n"
"ds_read_b128 v[80:83], v23 offset:2048\n"
"v_mfma_f32_16x16x32_bf16 v[124:127], v[40:43], v[44:47], v[124:127]\n"
"ds_read_b64_tr_b16 v[36:37], v17 offset:49248\n"
"ds_read_b64_tr_b16 v[38:39], v17 offset:51296\n"
"v_mfma_f32_16x16x32_bf16 v[156:159], v[40:43], v[48:51], v[156:159]\n"
"buffer_load_dwordx4 v27, s[52:55], s66 offen offset:1024 nt lds\n"
"v_mfma_f32_16x16x32_bf16 v[188:191], v[40:43], v[52:55], v[188:191]\n"
"v_mfma_f32_16x16x32_bf16 v[220:223], v[40:43], v[56:59], v[220:223]\n"
"buffer_load_dwordx4 v95, s[52:55], s66 offen offset:2048 nt lds\n"
"s_waitcnt lgkmcnt(2)\n"
"v_mfma_f32_16x16x32_bf16 v[224:227], v[28:31], v[80:83], v[224:227]\n"
"ds_read_b64_tr_b16 v[40:41], v17 offset:49256\n"
"ds_read_b64_tr_b16 v[42:43], v17 offset:51304\n"
"v_mfma_f32_16x16x32_bf16 v[228:231], v[32:35], v[80:83], v[228:231]\n"
"ds_read_b64_tr_b16 v[44:45], v18 offset:49248\n"
"ds_read_b64_tr_b16 v[46:47], v18 offset:51296\n"
"s_waitcnt lgkmcnt(4)\n"
"v_mfma_f32_16x16x32_bf16 v[232:235], v[36:39], v[80:83], v[232:235]\n"
"ds_read_b64_tr_b16 v[48:49], v18 offset:49256\n"
"ds_read_b64_tr_b16 v[50:51], v18 offset:51304\n"
"s_waitcnt lgkmcnt(4)\n"
"v_mfma_f32_16x16x32_bf16 v[236:239], v[40:43], v[80:83], v[236:239]\n"
"ds_read_b64_tr_b16 v[52:53], v19 offset:49248\n"
"ds_read_b64_tr_b16 v[54:55], v19 offset:51296\n"
"v_mfma_f32_16x16x32_bf16 v[96:99], v[28:31], v[64:67], v[96:99]\n"
"ds_read_b64_tr_b16 v[56:57], v19 offset:49256\n"
"ds_read_b64_tr_b16 v[58:59], v19 offset:51304\n"
"v_mfma_f32_16x16x32_bf16 v[100:103], v[32:35], v[64:67], v[100:103]\n"
"buffer_load_dwordx4 v27, s[52:55], s66 offen offset:3072 nt lds\n"
"v_mfma_f32_16x16x32_bf16 v[104:107], v[36:39], v[64:67], v[104:107]\n"
"v_mfma_f32_16x16x32_bf16 v[108:111], v[40:43], v[64:67], v[108:111]\n"
"s_waitcnt lgkmcnt(6)\n"
"v_mfma_f32_16x16x32_bf16 v[112:115], v[44:47], v[64:67], v[112:115]\n"
"s_waitcnt lgkmcnt(4)\n"
"v_mfma_f32_16x16x32_bf16 v[116:119], v[48:51], v[64:67], v[116:119]\n"
"s_waitcnt lgkmcnt(2)\n"
"v_mfma_f32_16x16x32_bf16 v[120:123], v[52:55], v[64:67], v[120:123]\n"
"s_waitcnt lgkmcnt(0)\n"
"v_mfma_f32_16x16x32_bf16 v[124:127], v[56:59], v[64:67], v[124:127]\n"
"v_mfma_f32_16x16x32_bf16 v[128:131], v[28:31], v[68:71], v[128:131]\n"
"v_pk_mul_f32 v[224:225], v[224:225], s[68:69]\n"
"v_pk_mul_f32 v[226:227], v[226:227], s[68:69]\n"
"v_mfma_f32_16x16x32_bf16 v[132:135], v[32:35], v[68:71], v[132:135]\n"
"v_pk_mul_f32 v[228:229], v[228:229], s[68:69]\n"
"v_pk_mul_f32 v[230:231], v[230:231], s[68:69]\n"
"v_mfma_f32_16x16x32_bf16 v[136:139], v[36:39], v[68:71], v[136:139]\n"
"v_cvt_pk_bf16_f32 v224, v224, v225\n"
"v_cvt_pk_bf16_f32 v225, v226, v227\n"
"v_mfma_f32_16x16x32_bf16 v[140:143], v[40:43], v[68:71], v[140:143]\n"
"v_cvt_pk_bf16_f32 v226, v228, v229\n"
"v_cvt_pk_bf16_f32 v227, v230, v231\n"
"v_mfma_f32_16x16x32_bf16 v[144:147], v[44:47], v[68:71], v[144:147]\n"
"v_pk_mul_f32 v[232:233], v[232:233], s[68:69]\n"
"v_pk_mul_f32 v[234:235], v[234:235], s[68:69]\n"
"v_mfma_f32_16x16x32_bf16 v[148:151], v[48:51], v[68:71], v[148:151]\n"
"v_pk_mul_f32 v[236:237], v[236:237], s[68:69]\n"
"v_pk_mul_f32 v[238:239], v[238:239], s[68:69]\n"
"v_mfma_f32_16x16x32_bf16 v[152:155], v[52:55], v[68:71], v[152:155]\n"
"v_cvt_pk_bf16_f32 v232, v232, v233\n"
"v_cvt_pk_bf16_f32 v233, v234, v235\n"
"v_mfma_f32_16x16x32_bf16 v[156:159], v[56:59], v[68:71], v[156:159]\n"
"v_cvt_pk_bf16_f32 v234, v236, v237\n"
"v_cvt_pk_bf16_f32 v235, v238, v239\n"
"v_mfma_f32_16x16x32_bf16 v[160:163], v[28:31], v[72:75], v[160:163]\n"
"v_pk_mul_f32 v[96:97], v[96:97], s[68:69]\n"
"v_pk_mul_f32 v[98:99], v[98:99], s[68:69]\n"
"v_mfma_f32_16x16x32_bf16 v[164:167], v[32:35], v[72:75], v[164:167]\n"
"v_pk_mul_f32 v[100:101], v[100:101], s[68:69]\n"
"v_pk_mul_f32 v[102:103], v[102:103], s[68:69]\n"
"v_mfma_f32_16x16x32_bf16 v[168:171], v[36:39], v[72:75], v[168:171]\n"
"s_barrier\n"
"v_cvt_pk_bf16_f32 v96, v96, v97\n"
"v_cvt_pk_bf16_f32 v97, v98, v99\n"
"v_mfma_f32_16x16x32_bf16 v[172:175], v[40:43], v[72:75], v[172:175]\n"
"s_add_u32 m0, s61, 49152\n"
"s_mov_b32 s65, 49152\n"
"v_cvt_pk_bf16_f32 v98, v100, v101\n"
"v_cvt_pk_bf16_f32 v99, v102, v103\n"
"v_mfma_f32_16x16x32_bf16 v[176:179], v[44:47], v[72:75], v[176:179]\n"
"buffer_load_dwordx4 v24, s[48:51], s65 offen nt lds\n"
"v_pk_mul_f32 v[104:105], v[104:105], s[68:69]\n"
"v_pk_mul_f32 v[106:107], v[106:107], s[68:69]\n"
"v_mfma_f32_16x16x32_bf16 v[180:183], v[48:51], v[72:75], v[180:183]\n"
"v_pk_mul_f32 v[108:109], v[108:109], s[68:69]\n"
"v_pk_mul_f32 v[110:111], v[110:111], s[68:69]\n"
"v_mfma_f32_16x16x32_bf16 v[184:187], v[52:55], v[72:75], v[184:187]\n"
"buffer_load_dwordx4 v25, s[48:51], s65 offen offset:1024 nt lds\n"
"v_cvt_pk_bf16_f32 v104, v104, v105\n"
"v_cvt_pk_bf16_f32 v105, v106, v107\n"
"v_mfma_f32_16x16x32_bf16 v[188:191], v[56:59], v[72:75], v[188:191]\n"
"v_cvt_pk_bf16_f32 v106, v108, v109\n"
"v_cvt_pk_bf16_f32 v107, v110, v111\n"
"v_mfma_f32_16x16x32_bf16 v[192:195], v[28:31], v[76:79], v[192:195]\n"
"buffer_load_dwordx4 v93, s[48:51], s65 offen offset:2048 nt lds\n"
"v_pk_mul_f32 v[112:113], v[112:113], s[68:69]\n"
"v_pk_mul_f32 v[114:115], v[114:115], s[68:69]\n"
"v_mfma_f32_16x16x32_bf16 v[196:199], v[32:35], v[76:79], v[196:199]\n"
"v_pk_mul_f32 v[116:117], v[116:117], s[68:69]\n"
"v_pk_mul_f32 v[118:119], v[118:119], s[68:69]\n"
"v_mfma_f32_16x16x32_bf16 v[200:203], v[36:39], v[76:79], v[200:203]\n"
"buffer_load_dwordx4 v94, s[48:51], s65 offen offset:3072 nt lds\n"
"v_cvt_pk_bf16_f32 v112, v112, v113\n"
"v_cvt_pk_bf16_f32 v113, v114, v115\n"
"v_mfma_f32_16x16x32_bf16 v[204:207], v[40:43], v[76:79], v[204:207]\n"
"v_cvt_pk_bf16_f32 v114, v116, v117\n"
"v_cvt_pk_bf16_f32 v115, v118, v119\n"
"v_mfma_f32_16x16x32_bf16 v[208:211], v[44:47], v[76:79], v[208:211]\n"
"v_pk_mul_f32 v[120:121], v[120:121], s[68:69]\n"
"v_pk_mul_f32 v[122:123], v[122:123], s[68:69]\n"
"v_mfma_f32_16x16x32_bf16 v[212:215], v[48:51], v[76:79], v[212:215]\n"
"v_pk_mul_f32 v[124:125], v[124:125], s[68:69]\n"
"v_pk_mul_f32 v[126:127], v[126:127], s[68:69]\n"
"v_mfma_f32_16x16x32_bf16 v[216:219], v[52:55], v[76:79], v[216:219]\n"
"v_cvt_pk_bf16_f32 v120, v120, v121\n"
"v_cvt_pk_bf16_f32 v121, v122, v123\n"
"v_mfma_f32_16x16x32_bf16 v[220:223], v[56:59], v[76:79], v[220:223]\n"
"v_cvt_pk_bf16_f32 v122, v124, v125\n"
"v_cvt_pk_bf16_f32 v123, v126, v127\n"
"v_add_u32 v84, s67, %16\n"
"s_nop 1\n"
"ds_write_b128 v84, v[224:227] offset:0\n"
"ds_write_b128 v84, v[232:235] offset:1024\n"
"s_nop 5\n"
"v_pk_mul_f32 v[128:129], v[128:129], s[68:69]\n"
"v_pk_mul_f32 v[130:131], v[130:131], s[68:69]\n"
"v_pk_mul_f32 v[132:133], v[132:133], s[68:69]\n"
"v_pk_mul_f32 v[134:135], v[134:135], s[68:69]\n"
"v_cvt_pk_bf16_f32 v128, v128, v129\n"
"v_cvt_pk_bf16_f32 v129, v130, v131\n"
"v_cvt_pk_bf16_f32 v130, v132, v133\n"
"v_cvt_pk_bf16_f32 v131, v134, v135\n"
"v_pk_mul_f32 v[136:137], v[136:137], s[68:69]\n"
"v_pk_mul_f32 v[138:139], v[138:139], s[68:69]\n"
"v_pk_mul_f32 v[140:141], v[140:141], s[68:69]\n"
"v_pk_mul_f32 v[142:143], v[142:143], s[68:69]\n"
"v_cvt_pk_bf16_f32 v136, v136, v137\n"
"v_cvt_pk_bf16_f32 v137, v138, v139\n"
"v_cvt_pk_bf16_f32 v138, v140, v141\n"
"v_cvt_pk_bf16_f32 v139, v142, v143\n"
"v_pk_mul_f32 v[144:145], v[144:145], s[68:69]\n"
"v_pk_mul_f32 v[146:147], v[146:147], s[68:69]\n"
"v_pk_mul_f32 v[148:149], v[148:149], s[68:69]\n"
"v_pk_mul_f32 v[150:151], v[150:151], s[68:69]\n"
"v_cvt_pk_bf16_f32 v144, v144, v145\n"
"v_cvt_pk_bf16_f32 v145, v146, v147\n"
"v_cvt_pk_bf16_f32 v146, v148, v149\n"
"v_cvt_pk_bf16_f32 v147, v150, v151\n"
"v_pk_mul_f32 v[152:153], v[152:153], s[68:69]\n"
"v_pk_mul_f32 v[154:155], v[154:155], s[68:69]\n"
"v_pk_mul_f32 v[156:157], v[156:157], s[68:69]\n"
"v_pk_mul_f32 v[158:159], v[158:159], s[68:69]\n"
"v_cvt_pk_bf16_f32 v152, v152, v153\n"
"v_cvt_pk_bf16_f32 v153, v154, v155\n"
"v_cvt_pk_bf16_f32 v154, v156, v157\n"
"v_cvt_pk_bf16_f32 v155, v158, v159\n"
"v_pk_mul_f32 v[160:161], v[160:161], s[68:69]\n"
"v_pk_mul_f32 v[162:163], v[162:163], s[68:69]\n"
"v_pk_mul_f32 v[164:165], v[164:165], s[68:69]\n"
"v_pk_mul_f32 v[166:167], v[166:167], s[68:69]\n"
"v_cvt_pk_bf16_f32 v160, v160, v161\n"
"v_cvt_pk_bf16_f32 v161, v162, v163\n"
"v_cvt_pk_bf16_f32 v162, v164, v165\n"
"v_cvt_pk_bf16_f32 v163, v166, v167\n"
"v_pk_mul_f32 v[168:169], v[168:169], s[68:69]\n"
"v_pk_mul_f32 v[170:171], v[170:171], s[68:69]\n"
"v_pk_mul_f32 v[172:173], v[172:173], s[68:69]\n"
"v_pk_mul_f32 v[174:175], v[174:175], s[68:69]\n"
"v_cvt_pk_bf16_f32 v168, v168, v169\n"
"v_cvt_pk_bf16_f32 v169, v170, v171\n"
"v_cvt_pk_bf16_f32 v170, v172, v173\n"
"v_cvt_pk_bf16_f32 v171, v174, v175\n"
"v_pk_mul_f32 v[176:177], v[176:177], s[68:69]\n"
"v_pk_mul_f32 v[178:179], v[178:179], s[68:69]\n"
"v_pk_mul_f32 v[180:181], v[180:181], s[68:69]\n"
"v_pk_mul_f32 v[182:183], v[182:183], s[68:69]\n"
"v_cvt_pk_bf16_f32 v176, v176, v177\n"
"v_cvt_pk_bf16_f32 v177, v178, v179\n"
"v_cvt_pk_bf16_f32 v178, v180, v181\n"
"v_cvt_pk_bf16_f32 v179, v182, v183\n"
"v_pk_mul_f32 v[184:185], v[184:185], s[68:69]\n"
"v_pk_mul_f32 v[186:187], v[186:187], s[68:69]\n"
"v_pk_mul_f32 v[188:189], v[188:189], s[68:69]\n"
"v_pk_mul_f32 v[190:191], v[190:191], s[68:69]\n"
"v_cvt_pk_bf16_f32 v184, v184, v185\n"
"v_cvt_pk_bf16_f32 v185, v186, v187\n"
"v_cvt_pk_bf16_f32 v186, v188, v189\n"
"v_cvt_pk_bf16_f32 v187, v190, v191\n"
"v_pk_mul_f32 v[192:193], v[192:193], s[68:69]\n"
"v_pk_mul_f32 v[194:195], v[194:195], s[68:69]\n"
"v_pk_mul_f32 v[196:197], v[196:197], s[68:69]\n"
"v_pk_mul_f32 v[198:199], v[198:199], s[68:69]\n"
"v_cvt_pk_bf16_f32 v192, v192, v193\n"
"v_cvt_pk_bf16_f32 v193, v194, v195\n"
"v_cvt_pk_bf16_f32 v194, v196, v197\n"
"v_cvt_pk_bf16_f32 v195, v198, v199\n"
"v_pk_mul_f32 v[200:201], v[200:201], s[68:69]\n"
"v_pk_mul_f32 v[202:203], v[202:203], s[68:69]\n"
"v_pk_mul_f32 v[204:205], v[204:205], s[68:69]\n"
"v_pk_mul_f32 v[206:207], v[206:207], s[68:69]\n"
"v_cvt_pk_bf16_f32 v200, v200, v201\n"
"v_cvt_pk_bf16_f32 v201, v202, v203\n"
"v_cvt_pk_bf16_f32 v202, v204, v205\n"
"v_cvt_pk_bf16_f32 v203, v206, v207\n"
"v_pk_mul_f32 v[208:209], v[208:209], s[68:69]\n"
"v_pk_mul_f32 v[210:211], v[210:211], s[68:69]\n"
"v_pk_mul_f32 v[212:213], v[212:213], s[68:69]\n"
"v_pk_mul_f32 v[214:215], v[214:215], s[68:69]\n"
"v_cvt_pk_bf16_f32 v208, v208, v209\n"
"v_cvt_pk_bf16_f32 v209, v210, v211\n"
"v_cvt_pk_bf16_f32 v210, v212, v213\n"
"v_cvt_pk_bf16_f32 v211, v214, v215\n"
"v_pk_mul_f32 v[216:217], v[216:217], s[68:69]\n"
"v_pk_mul_f32 v[218:219], v[218:219], s[68:69]\n"
"v_pk_mul_f32 v[220:221], v[220:221], s[68:69]\n"
"v_pk_mul_f32 v[222:223], v[222:223], s[68:69]\n"
"v_cvt_pk_bf16_f32 v216, v216, v217\n"
"v_cvt_pk_bf16_f32 v217, v218, v219\n"
"v_cvt_pk_bf16_f32 v218, v220, v221\n"
"v_cvt_pk_bf16_f32 v219, v222, v223\n"
"s_waitcnt lgkmcnt(0)\n"
"s_barrier\n"
"v_xor_b32 v84, 2048, v84\n"
"ds_read_b128 v[240:243], v84 offset:0\n"
"ds_read_b128 v[248:251], v84 offset:1024\n"
"v_xor_b32 v28, 0, %7\n"
"v_xor_b32 v29, 64, %7\n"
"v_xor_b32 v30, 128, %7\n"
"v_xor_b32 v31, 192, %7\n"
"v_xor_b32 v36, 0, %8\n"
"v_xor_b32 v37, 32, %8\n"
"v_xor_b32 v32, 8192, %7\n"
"v_xor_b32 v33, 8256, %7\n"
"v_xor_b32 v34, 8320, %7\n"
"v_xor_b32 v35, 8384, %7\n"
"v_xor_b32 v38, 8192, %8\n"
"v_xor_b32 v39, 8224, %8\n"
"v_mov_b32 v60, 0x3f803f80\n"
"v_mov_b32 v61, 0x3f803f80\n"
"v_mov_b32 v62, 0x3f803f80\n"
"v_mov_b32 v63, 0x3f803f80\n"
"ds_read_b128 a[180:183], v28 offset:0\n"
"ds_read_b128 a[184:187], v29 offset:0\n"
"ds_read_b128 a[188:191], v30 offset:0\n"
"ds_read_b128 a[192:195], v31 offset:0\n"
"ds_read_b128 a[196:199], v28 offset:4096\n"
"ds_read_b128 a[200:203], v29 offset:4096\n"
"s_waitcnt lgkmcnt(5)\n"
"v_mfma_f32_16x16x32_bf16 v[100:103], a[180:183], v[96:99], 0\n"
"v_mfma_f32_16x16x32_bf16 v[116:119], a[180:183], v[128:131], 0\n"
"v_mfma_f32_16x16x32_bf16 v[132:135], a[180:183], v[160:163], 0\n"
"v_mfma_f32_16x16x32_bf16 v[148:151], a[180:183], v[192:195], 0\n"
"v_mfma_f32_16x16x32_bf16 v[164:167], a[180:183], v[224:227], 0\n"
"ds_read_b128 a[180:183], v30 offset:4096\n"
"s_waitcnt lgkmcnt(5)\n"
"v_mfma_f32_16x16x32_bf16 v[100:103], a[184:187], v[104:107], v[100:103]\n"
"v_mfma_f32_16x16x32_bf16 v[116:119], a[184:187], v[136:139], v[116:119]\n"
"v_mfma_f32_16x16x32_bf16 v[132:135], a[184:187], v[168:171], v[132:135]\n"
"v_mfma_f32_16x16x32_bf16 v[148:151], a[184:187], v[200:203], v[148:151]\n"
"v_mfma_f32_16x16x32_bf16 v[164:167], a[184:187], v[232:235], v[164:167]\n"
"ds_read_b128 a[184:187], v31 offset:4096\n"
"s_waitcnt lgkmcnt(5)\n"
"v_mfma_f32_16x16x32_bf16 v[100:103], a[188:191], v[112:115], v[100:103]\n"
"v_mfma_f32_16x16x32_bf16 v[116:119], a[188:191], v[144:147], v[116:119]\n"
"v_mfma_f32_16x16x32_bf16 v[132:135], a[188:191], v[176:179], v[132:135]\n"
"v_mfma_f32_16x16x32_bf16 v[148:151], a[188:191], v[208:211], v[148:151]\n"
"v_mfma_f32_16x16x32_bf16 v[164:167], a[188:191], v[240:243], v[164:167]\n"
"s_waitcnt lgkmcnt(4)\n"
"v_mfma_f32_16x16x32_bf16 v[100:103], a[192:195], v[120:123], v[100:103]\n"
"v_mfma_f32_16x16x32_bf16 v[116:119], a[192:195], v[152:155], v[116:119]\n"
"v_mfma_f32_16x16x32_bf16 v[132:135], a[192:195], v[184:187], v[132:135]\n"
"v_mfma_f32_16x16x32_bf16 v[148:151], a[192:195], v[216:219], v[148:151]\n"
"v_mfma_f32_16x16x32_bf16 v[164:167], a[192:195], v[248:251], v[164:167]\n"
"s_waitcnt lgkmcnt(3)\n"
"v_mfma_f32_16x16x32_bf16 v[108:111], a[196:199], v[96:99], 0\n"
"v_mfma_f32_16x16x32_bf16 v[124:127], a[196:199], v[128:131], 0\n"
"v_mfma_f32_16x16x32_bf16 v[140:143], a[196:199], v[160:163], 0\n"
"v_mfma_f32_16x16x32_bf16 v[156:159], a[196:199], v[192:195], 0\n"
"v_mfma_f32_16x16x32_bf16 v[172:175], a[196:199], v[224:227], 0\n"
"s_waitcnt lgkmcnt(2)\n"
"v_mfma_f32_16x16x32_bf16 v[108:111], a[200:203], v[104:107], v[108:111]\n"
"v_mfma_f32_16x16x32_bf16 v[124:127], a[200:203], v[136:139], v[124:127]\n"
"v_mfma_f32_16x16x32_bf16 v[140:143], a[200:203], v[168:171], v[140:143]\n"
"v_mfma_f32_16x16x32_bf16 v[156:159], a[200:203], v[200:203], v[156:159]\n"
"v_mfma_f32_16x16x32_bf16 v[172:175], a[200:203], v[232:235], v[172:175]\n"
"s_waitcnt lgkmcnt(1)\n"
"v_mfma_f32_16x16x32_bf16 v[108:111], a[180:183], v[112:115], v[108:111]\n"
"v_mfma_f32_16x16x32_bf16 v[124:127], a[180:183], v[144:147], v[124:127]\n"
"v_mfma_f32_16x16x32_bf16 v[140:143], a[180:183], v[176:179], v[140:143]\n"
"v_mfma_f32_16x16x32_bf16 v[156:159], a[180:183], v[208:211], v[156:159]\n"
"v_mfma_f32_16x16x32_bf16 v[172:175], a[180:183], v[240:243], v[172:175]\n"
"s_waitcnt lgkmcnt(0)\n"
"v_mfma_f32_16x16x32_bf16 v[108:111], a[184:187], v[120:123], v[108:111]\n"
"v_mfma_f32_16x16x32_bf16 v[124:127], a[184:187], v[152:155], v[124:127]\n"
"v_mfma_f32_16x16x32_bf16 v[140:143], a[184:187], v[184:187], v[140:143]\n"
"v_mfma_f32_16x16x32_bf16 v[156:159], a[184:187], v[216:219], v[156:159]\n"
"v_mfma_f32_16x16x32_bf16 v[172:175], a[184:187], v[248:251], v[172:175]\n"
"s_nop 7\n"
"v_max3_f32 v64, v100, v101, v102\n"
"v_max3_f32 v64, v64, v103, v108\n"
"v_max3_f32 v64, v64, v109, v110\n"
"v_max_f32 v64, v64, v111\n"
"v_max3_f32 v66, v116, v117, v118\n"
"v_max3_f32 v66, v66, v119, v124\n"
"v_max3_f32 v66, v66, v125, v126\n"
"v_max_f32 v66, v66, v127\n"
"v_max3_f32 v68, v132, v133, v134\n"
"v_max3_f32 v68, v68, v135, v140\n"
"v_max3_f32 v68, v68, v141, v142\n"
"v_max_f32 v68, v68, v143\n"
"v_max3_f32 v70, v148, v149, v150\n"
"v_max3_f32 v70, v70, v151, v156\n"
"v_max3_f32 v70, v70, v157, v158\n"
"v_max_f32 v70, v70, v159\n"
"v_max3_f32 v72, v164, v165, v166\n"
"v_max3_f32 v72, v72, v167, v172\n"
"v_max3_f32 v72, v72, v173, v174\n"
"v_max_f32 v72, v72, v175\n"
"v_mov_b32 v65, v64\n"
"v_mov_b32 v67, v66\n"
"v_mov_b32 v69, v68\n"
"v_mov_b32 v71, v70\n"
"v_mov_b32 v73, v72\n"
"s_nop 1\n"
"v_permlane32_swap_b32 v64, v65\n"
"v_permlane32_swap_b32 v66, v67\n"
"v_permlane32_swap_b32 v68, v69\n"
"v_permlane32_swap_b32 v70, v71\n"
"v_permlane32_swap_b32 v72, v73\n"
"s_nop 1\n"
"v_max_f32 v64, v64, v65\n"
"v_max_f32 v66, v66, v67\n"
"v_max_f32 v68, v68, v69\n"
"v_max_f32 v70, v70, v71\n"
"v_max_f32 v72, v72, v73\n"
"v_mov_b32 v65, v64\n"
"v_mov_b32 v67, v66\n"
"v_mov_b32 v69, v68\n"
"v_mov_b32 v71, v70\n"
"v_mov_b32 v73, v72\n"
"s_nop 1\n"
"v_permlane16_swap_b32 v64, v65\n"
"v_permlane16_swap_b32 v66, v67\n"
"v_permlane16_swap_b32 v68, v69\n"
"v_permlane16_swap_b32 v70, v71\n"
"v_permlane16_swap_b32 v72, v73\n"
"s_nop 1\n"
"v_max_f32 v64, v64, v65\n"
"v_max_f32 v66, v66, v67\n"
"v_max_f32 v68, v68, v69\n"
"v_max_f32 v70, v70, v71\n"
"v_max_f32 v72, v72, v73\n"
"v_sub_f32 v40, 0, v64\n"
"v_sub_f32 v41, 0, v64\n"
"v_sub_f32 v42, 0, v64\n"
"v_sub_f32 v43, 0, v64\n"
"v_sub_f32 v44, 0, v66\n"
"v_sub_f32 v45, 0, v66\n"
"v_sub_f32 v46, 0, v66\n"
"v_sub_f32 v47, 0, v66\n"
"v_sub_f32 v48, 0, v68\n"
"v_sub_f32 v49, 0, v68\n"
"v_sub_f32 v50, 0, v68\n"
"v_sub_f32 v51, 0, v68\n"
"v_sub_f32 v52, 0, v70\n"
"v_sub_f32 v53, 0, v70\n"
"v_sub_f32 v54, 0, v70\n"
"v_sub_f32 v55, 0, v70\n"
"v_sub_f32 v56, 0, v72\n"
"v_sub_f32 v57, 0, v72\n"
"v_sub_f32 v58, 0, v72\n"
"v_sub_f32 v59, 0, v72\n"
"v_sub_f32 v100, v100, v64\n"
"v_sub_f32 v101, v101, v64\n"
"v_sub_f32 v102, v102, v64\n"
"v_sub_f32 v103, v103, v64\n"
"v_sub_f32 v108, v108, v64\n"
"v_sub_f32 v109, v109, v64\n"
"v_sub_f32 v110, v110, v64\n"
"v_sub_f32 v111, v111, v64\n"
"v_sub_f32 v116, v116, v66\n"
"v_sub_f32 v117, v117, v66\n"
"v_sub_f32 v118, v118, v66\n"
"v_sub_f32 v119, v119, v66\n"
"v_sub_f32 v124, v124, v66\n"
"v_sub_f32 v125, v125, v66\n"
"v_sub_f32 v126, v126, v66\n"
"v_sub_f32 v127, v127, v66\n"
"v_sub_f32 v132, v132, v68\n"
"v_sub_f32 v133, v133, v68\n"
"v_sub_f32 v134, v134, v68\n"
"v_sub_f32 v135, v135, v68\n"
"v_sub_f32 v140, v140, v68\n"
"v_sub_f32 v141, v141, v68\n"
"v_sub_f32 v142, v142, v68\n"
"v_sub_f32 v143, v143, v68\n"
"v_sub_f32 v148, v148, v70\n"
"v_sub_f32 v149, v149, v70\n"
"v_sub_f32 v150, v150, v70\n"
"v_sub_f32 v151, v151, v70\n"
"v_sub_f32 v156, v156, v70\n"
"v_sub_f32 v157, v157, v70\n"
"v_sub_f32 v158, v158, v70\n"
"v_sub_f32 v159, v159, v70\n"
"v_sub_f32 v164, v164, v72\n"
"v_sub_f32 v165, v165, v72\n"
"v_sub_f32 v166, v166, v72\n"
"v_sub_f32 v167, v167, v72\n"
"v_sub_f32 v172, v172, v72\n"
"v_sub_f32 v173, v173, v72\n"
"v_sub_f32 v174, v174, v72\n"
"v_sub_f32 v175, v175, v72\n"
"v_exp_f32 v100, v100\n"
"v_exp_f32 v101, v101\n"
"v_exp_f32 v102, v102\n"
"v_exp_f32 v103, v103\n"
"v_cvt_pk_bf16_f32 v180, v100, v101\n"
"v_cvt_pk_bf16_f32 v181, v102, v103\n"
"v_exp_f32 v116, v116\n"
"v_exp_f32 v117, v117\n"
"v_exp_f32 v118, v118\n"
"v_exp_f32 v119, v119\n"
"v_cvt_pk_bf16_f32 v188, v116, v117\n"
"v_cvt_pk_bf16_f32 v189, v118, v119\n"
"v_exp_f32 v132, v132\n"
"v_exp_f32 v133, v133\n"
"v_exp_f32 v134, v134\n"
"v_exp_f32 v135, v135\n"
"v_cvt_pk_bf16_f32 v196, v132, v133\n"
"v_cvt_pk_bf16_f32 v197, v134, v135\n"
"v_exp_f32 v148, v148\n"
"v_exp_f32 v149, v149\n"
"v_exp_f32 v150, v150\n"
"v_exp_f32 v151, v151\n"
"v_cvt_pk_bf16_f32 v204, v148, v149\n"
"v_cvt_pk_bf16_f32 v205, v150, v151\n"
"v_exp_f32 v164, v164\n"
"v_exp_f32 v165, v165\n"
"v_exp_f32 v166, v166\n"
"v_exp_f32 v167, v167\n"
"v_cvt_pk_bf16_f32 v244, v164, v165\n"
"v_cvt_pk_bf16_f32 v245, v166, v167\n"
"v_exp_f32 v108, v108\n"
"v_exp_f32 v109, v109\n"
"v_exp_f32 v110, v110\n"
"v_exp_f32 v111, v111\n"
"v_cvt_pk_bf16_f32 v182, v108, v109\n"
"v_cvt_pk_bf16_f32 v183, v110, v111\n"
"v_exp_f32 v124, v124\n"
"v_exp_f32 v125, v125\n"
"v_exp_f32 v126, v126\n"
"v_exp_f32 v127, v127\n"
"v_cvt_pk_bf16_f32 v190, v124, v125\n"
"v_cvt_pk_bf16_f32 v191, v126, v127\n"
"v_exp_f32 v140, v140\n"
"v_exp_f32 v141, v141\n"
"v_exp_f32 v142, v142\n"
"v_exp_f32 v143, v143\n"
"v_cvt_pk_bf16_f32 v198, v140, v141\n"
"v_cvt_pk_bf16_f32 v199, v142, v143\n"
"v_exp_f32 v156, v156\n"
"v_exp_f32 v157, v157\n"
"v_exp_f32 v158, v158\n"
"v_exp_f32 v159, v159\n"
"v_cvt_pk_bf16_f32 v206, v156, v157\n"
"v_cvt_pk_bf16_f32 v207, v158, v159\n"
"v_exp_f32 v172, v172\n"
"v_exp_f32 v173, v173\n"
"v_exp_f32 v174, v174\n"
"v_exp_f32 v175, v175\n"
"v_cvt_pk_bf16_f32 v246, v172, v173\n"
"v_cvt_pk_bf16_f32 v247, v174, v175\n"
"ds_read_b128 a[188:191], v32 offset:0\n"
"ds_read_b128 a[192:195], v33 offset:0\n"
"ds_read_b128 a[196:199], v34 offset:0\n"
"ds_read_b128 a[200:203], v35 offset:0\n"
"s_waitcnt lgkmcnt(3)\n"
"v_mfma_f32_16x16x32_bf16 v[100:103], a[188:191], v[96:99], v[40:43]\n"
"ds_read_b128 a[180:183], v32 offset:4096\n"
"v_mfma_f32_16x16x32_bf16 v[116:119], a[188:191], v[128:131], v[44:47]\n"
"v_mfma_f32_16x16x32_bf16 v[132:135], a[188:191], v[160:163], v[48:51]\n"
"v_mfma_f32_16x16x32_bf16 v[148:151], a[188:191], v[192:195], v[52:55]\n"
"s_waitcnt lgkmcnt(3)\n"
"v_mfma_f32_16x16x32_bf16 v[100:103], a[192:195], v[104:107], v[100:103]\n"
"ds_read_b128 a[184:187], v33 offset:4096\n"
"v_mfma_f32_16x16x32_bf16 v[116:119], a[192:195], v[136:139], v[116:119]\n"
"v_mfma_f32_16x16x32_bf16 v[132:135], a[192:195], v[168:171], v[132:135]\n"
"v_mfma_f32_16x16x32_bf16 v[148:151], a[192:195], v[200:203], v[148:151]\n"
"s_waitcnt lgkmcnt(3)\n"
"v_mfma_f32_16x16x32_bf16 v[100:103], a[196:199], v[112:115], v[100:103]\n"
"ds_read_b128 a[188:191], v34 offset:4096\n"
"v_mfma_f32_16x16x32_bf16 v[116:119], a[196:199], v[144:147], v[116:119]\n"
"v_mfma_f32_16x16x32_bf16 v[132:135], a[196:199], v[176:179], v[132:135]\n"
"v_mfma_f32_16x16x32_bf16 v[148:151], a[196:199], v[208:211], v[148:151]\n"
"s_waitcnt lgkmcnt(3)\n"
"v_mfma_f32_16x16x32_bf16 v[100:103], a[200:203], v[120:123], v[100:103]\n"
"ds_read_b128 a[192:195], v35 offset:4096\n"
"v_mfma_f32_16x16x32_bf16 v[116:119], a[200:203], v[152:155], v[116:119]\n"
"v_mfma_f32_16x16x32_bf16 v[132:135], a[200:203], v[184:187], v[132:135]\n"
"v_mfma_f32_16x16x32_bf16 v[148:151], a[200:203], v[216:219], v[148:151]\n"
"s_waitcnt lgkmcnt(3)\n"
"v_mfma_f32_16x16x32_bf16 v[108:111], a[180:183], v[96:99], v[40:43]\n"
"v_mfma_f32_16x16x32_bf16 v[124:127], a[180:183], v[128:131], v[44:47]\n"
"ds_read_b64_tr_b16 a[204:205], v36 offset:0\n"
"v_mfma_f32_16x16x32_bf16 v[140:143], a[180:183], v[160:163], v[48:51]\n"
"ds_read_b64_tr_b16 a[206:207], v36 offset:4096\n"
"v_mfma_f32_16x16x32_bf16 v[156:159], a[180:183], v[192:195], v[52:55]\n"
"s_waitcnt lgkmcnt(4)\n"
"v_mfma_f32_16x16x32_bf16 v[108:111], a[184:187], v[104:107], v[108:111]\n"
"v_exp_f32 v100, v100\n"
"v_mfma_f32_16x16x32_bf16 v[124:127], a[184:187], v[136:139], v[124:127]\n"
"ds_read_b64_tr_b16 a[210:211], v37 offset:4096\n"
"v_exp_f32 v101, v101\n"
"v_mfma_f32_16x16x32_bf16 v[140:143], a[184:187], v[168:171], v[140:143]\n"
"ds_read_b64_tr_b16 a[208:209], v37 offset:0\n"
"v_exp_f32 v102, v102\n"
"v_mfma_f32_16x16x32_bf16 v[156:159], a[184:187], v[200:203], v[156:159]\n"
"s_waitcnt lgkmcnt(5)\n"
"v_exp_f32 v103, v103\n"
"v_mfma_f32_16x16x32_bf16 v[108:111], a[188:191], v[112:115], v[108:111]\n"
"v_cvt_pk_bf16_f32 v212, v100, v101\n"
"v_cvt_pk_bf16_f32 v213, v102, v103\n"
"v_mfma_f32_16x16x32_bf16 v[124:127], a[188:191], v[144:147], v[124:127]\n"
"v_exp_f32 v116, v116\n"
"v_mfma_f32_16x16x32_bf16 v[140:143], a[188:191], v[176:179], v[140:143]\n"
"ds_read_b64_tr_b16 a[212:213], v36 offset:512\n"
"v_exp_f32 v117, v117\n"
"v_mfma_f32_16x16x32_bf16 v[156:159], a[188:191], v[208:211], v[156:159]\n"
"s_waitcnt lgkmcnt(5)\n"
"v_exp_f32 v118, v118\n"
"v_mfma_f32_16x16x32_bf16 v[108:111], a[192:195], v[120:123], v[108:111]\n"
"ds_read_b64_tr_b16 a[214:215], v36 offset:4608\n"
"v_exp_f32 v119, v119\n"
"v_mfma_f32_16x16x32_bf16 v[124:127], a[192:195], v[152:155], v[124:127]\n"
"v_cvt_pk_bf16_f32 v220, v116, v117\n"
"v_cvt_pk_bf16_f32 v221, v118, v119\n"
"v_mfma_f32_16x16x32_bf16 v[140:143], a[192:195], v[184:187], v[140:143]\n"
"v_exp_f32 v132, v132\n"
"v_mfma_f32_16x16x32_bf16 v[156:159], a[192:195], v[216:219], v[156:159]\n"
"s_waitcnt vmcnt(12)\n"
"s_barrier\n"
"v_exp_f32 v133, v133\n"
"v_mfma_f32_16x16x32_bf16 a[160:163], v[60:63], v[180:183], 0\n"
"ds_read_b64_tr_b16 a[216:217], v37 offset:512\n"
"v_exp_f32 v134, v134\n"
"v_mfma_f32_16x16x32_bf16 a[164:167], v[60:63], v[188:191], 0\n"
"ds_read_b64_tr_b16 a[218:219], v37 offset:4608\n"
"v_exp_f32 v135, v135\n"
"v_mfma_f32_16x16x32_bf16 a[168:171], v[60:63], v[196:199], 0\n"
"v_cvt_pk_bf16_f32 v228, v132, v133\n"
"v_cvt_pk_bf16_f32 v229, v134, v135\n"
"v_mfma_f32_16x16x32_bf16 a[172:175], v[60:63], v[204:207], 0\n"
"v_exp_f32 v148, v148\n"
"v_mfma_f32_16x16x32_bf16 a[176:179], v[60:63], v[244:247], 0\n"
"s_waitcnt lgkmcnt(6)\n"
"v_exp_f32 v149, v149\n"
"v_mfma_f32_16x16x32_bf16 a[0:3], a[204:207], v[180:183], 0\n"
"ds_read_b64_tr_b16 a[220:221], v36 offset:1024\n"
"v_exp_f32 v150, v150\n"
"v_mfma_f32_16x16x32_bf16 a[32:35], a[204:207], v[188:191], 0\n"
"ds_read_b64_tr_b16 a[222:223], v36 offset:5120\n"
"v_exp_f32 v151, v151\n"
"v_mfma_f32_16x16x32_bf16 a[64:67], a[204:207], v[196:199], 0\n"
"v_cvt_pk_bf16_f32 v236, v148, v149\n"
"v_cvt_pk_bf16_f32 v237, v150, v151\n"
"v_mfma_f32_16x16x32_bf16 a[96:99], a[204:207], v[204:207], 0\n"
"v_exp_f32 v108, v108\n"
"v_mfma_f32_16x16x32_bf16 a[128:131], a[204:207], v[244:247], 0\n"
"s_waitcnt lgkmcnt(6)\n"
"v_exp_f32 v109, v109\n"
"v_mfma_f32_16x16x32_bf16 a[4:7], a[208:211], v[180:183], 0\n"
"ds_read_b64_tr_b16 a[224:225], v37 offset:1024\n"
"v_exp_f32 v110, v110\n"
"v_mfma_f32_16x16x32_bf16 a[36:39], a[208:211], v[188:191], 0\n"
"ds_read_b64_tr_b16 a[226:227], v37 offset:5120\n"
"v_exp_f32 v111, v111\n"
"v_mfma_f32_16x16x32_bf16 a[68:71], a[208:211], v[196:199], 0\n"
"v_cvt_pk_bf16_f32 v214, v108, v109\n"
"v_cvt_pk_bf16_f32 v215, v110, v111\n"
"v_mfma_f32_16x16x32_bf16 a[100:103], a[208:211], v[204:207], 0\n"
"v_exp_f32 v124, v124\n"
"v_mfma_f32_16x16x32_bf16 a[132:135], a[208:211], v[244:247], 0\n"
"s_waitcnt lgkmcnt(6)\n"
"v_exp_f32 v125, v125\n"
"v_mfma_f32_16x16x32_bf16 a[8:11], a[212:215], v[180:183], 0\n"
"ds_read_b64_tr_b16 a[204:205], v36 offset:1536\n"
"v_exp_f32 v126, v126\n"
"v_mfma_f32_16x16x32_bf16 a[40:43], a[212:215], v[188:191], 0\n"
"ds_read_b64_tr_b16 a[206:207], v36 offset:5632\n"
"v_exp_f32 v127, v127\n"
"v_mfma_f32_16x16x32_bf16 a[72:75], a[212:215], v[196:199], 0\n"
"v_cvt_pk_bf16_f32 v222, v124, v125\n"
"v_cvt_pk_bf16_f32 v223, v126, v127\n"
"v_mfma_f32_16x16x32_bf16 a[104:107], a[212:215], v[204:207], 0\n"
"v_exp_f32 v140, v140\n"
"v_mfma_f32_16x16x32_bf16 a[136:139], a[212:215], v[244:247], 0\n"
"s_waitcnt lgkmcnt(6)\n"
"v_exp_f32 v141, v141\n"
"v_mfma_f32_16x16x32_bf16 a[12:15], a[216:219], v[180:183], 0\n"
"ds_read_b64_tr_b16 a[208:209], v37 offset:1536\n"
"v_exp_f32 v142, v142\n"
"v_mfma_f32_16x16x32_bf16 a[44:47], a[216:219], v[188:191], 0\n"
"ds_read_b64_tr_b16 a[210:211], v37 offset:5632\n"
"v_exp_f32 v143, v143\n"
"v_mfma_f32_16x16x32_bf16 a[76:79], a[216:219], v[196:199], 0\n"
"v_cvt_pk_bf16_f32 v230, v140, v141\n"
"v_cvt_pk_bf16_f32 v231, v142, v143\n"
"v_mfma_f32_16x16x32_bf16 a[108:111], a[216:219], v[204:207], 0\n"
"v_exp_f32 v156, v156\n"
"v_mfma_f32_16x16x32_bf16 a[140:143], a[216:219], v[244:247], 0\n"
"s_waitcnt lgkmcnt(6)\n"
"v_exp_f32 v157, v157\n"
"v_mfma_f32_16x16x32_bf16 a[16:19], a[220:223], v[180:183], 0\n"
"ds_read_b128 a[196:199], v28 offset:16384\n"
"v_exp_f32 v158, v158\n"
"v_mfma_f32_16x16x32_bf16 a[48:51], a[220:223], v[188:191], 0\n"
"v_exp_f32 v159, v159\n"
"v_mfma_f32_16x16x32_bf16 a[80:83], a[220:223], v[196:199], 0\n"
"v_cvt_pk_bf16_f32 v238, v156, v157\n"
"v_cvt_pk_bf16_f32 v239, v158, v159\n"
"v_mfma_f32_16x16x32_bf16 a[112:115], a[220:223], v[204:207], 0\n"
"v_mfma_f32_16x16x32_bf16 a[144:147], a[220:223], v[244:247], 0\n"
"s_waitcnt lgkmcnt(5)\n"
"v_mfma_f32_16x16x32_bf16 a[20:23], a[224:227], v[180:183], 0\n"
"ds_read_b128 a[200:203], v29 offset:16384\n"
"v_mfma_f32_16x16x32_bf16 a[52:55], a[224:227], v[188:191], 0\n"
"v_mfma_f32_16x16x32_bf16 a[84:87], a[224:227], v[196:199], 0\n"
"v_mfma_f32_16x16x32_bf16 a[116:119], a[224:227], v[204:207], 0\n"
"v_mfma_f32_16x16x32_bf16 a[148:151], a[224:227], v[244:247], 0\n"
"s_waitcnt lgkmcnt(4)\n"
"v_mfma_f32_16x16x32_bf16 a[24:27], a[204:207], v[180:183], 0\n"
"ds_read_b128 a[180:183], v30 offset:16384\n"
"v_mfma_f32_16x16x32_bf16 a[56:59], a[204:207], v[188:191], 0\n"
"v_mfma_f32_16x16x32_bf16 a[88:91], a[204:207], v[196:199], 0\n"
"v_mfma_f32_16x16x32_bf16 a[120:123], a[204:207], v[204:207], 0\n"
"v_mfma_f32_16x16x32_bf16 a[152:155], a[204:207], v[244:247], 0\n"
"s_waitcnt lgkmcnt(3)\n"
"v_mfma_f32_16x16x32_bf16 a[28:31], a[208:211], v[180:183], 0\n"
"ds_read_b128 a[184:187], v31 offset:16384\n"
"v_mfma_f32_16x16x32_bf16 a[60:63], a[208:211], v[188:191], 0\n"
"v_mfma_f32_16x16x32_bf16 a[92:95], a[208:211], v[196:199], 0\n"
"v_mfma_f32_16x16x32_bf16 a[124:127], a[208:211], v[204:207], 0\n"
"v_mfma_f32_16x16x32_bf16 a[156:159], a[208:211], v[244:247], 0\n"
"s_waitcnt lgkmcnt(3)\n"
"v_mfma_f32_16x16x32_bf16 v[100:103], a[196:199], v[96:99], v[40:43]\n"
"ds_read_b128 a[188:191], v28 offset:20480\n"
"v_mfma_f32_16x16x32_bf16 v[116:119], a[196:199], v[128:131], v[44:47]\n"
"s_add_u32 m0, s61, 114688\n"
"s_mov_b32 s66, 49152\n"
"v_mfma_f32_16x16x32_bf16 v[132:135], a[196:199], v[160:163], v[48:51]\n"
"buffer_load_dwordx4 v95, s[52:55], s66 offen nt lds\n"
"v_mfma_f32_16x16x32_bf16 v[148:151], a[196:199], v[192:195], v[52:55]\n"
"v_mfma_f32_16x16x32_bf16 v[164:167], a[196:199], v[224:227], v[56:59]\n"
"s_waitcnt lgkmcnt(3)\n"
"v_mfma_f32_16x16x32_bf16 v[100:103], a[200:203], v[104:107], v[100:103]\n"
"ds_read_b128 a[192:195], v29 offset:20480\n"
"v_mfma_f32_16x16x32_bf16 v[116:119], a[200:203], v[136:139], v[116:119]\n"
"buffer_load_dwordx4 v27, s[52:55], s66 offen offset:1024 nt lds\n"
"v_mfma_f32_16x16x32_bf16 v[132:135], a[200:203], v[168:171], v[132:135]\n"
"v_mfma_f32_16x16x32_bf16 v[148:151], a[200:203], v[200:203], v[148:151]\n"
"v_mfma_f32_16x16x32_bf16 v[164:167], a[200:203], v[232:235], v[164:167]\n"
"s_waitcnt lgkmcnt(3)\n"
"v_mfma_f32_16x16x32_bf16 v[100:103], a[180:183], v[112:115], v[100:103]\n"
"ds_read_b128 a[196:199], v30 offset:20480\n"
"v_mfma_f32_16x16x32_bf16 v[116:119], a[180:183], v[144:147], v[116:119]\n"
"buffer_load_dwordx4 v95, s[52:55], s66 offen offset:2048 nt lds\n"
"v_mfma_f32_16x16x32_bf16 v[132:135], a[180:183], v[176:179], v[132:135]\n"
"v_mfma_f32_16x16x32_bf16 v[148:151], a[180:183], v[208:211], v[148:151]\n"
"v_mfma_f32_16x16x32_bf16 v[164:167], a[180:183], v[240:243], v[164:167]\n"
"s_waitcnt lgkmcnt(3)\n"
"v_mfma_f32_16x16x32_bf16 v[100:103], a[184:187], v[120:123], v[100:103]\n"
"ds_read_b128 a[200:203], v31 offset:20480\n"
"v_mfma_f32_16x16x32_bf16 v[116:119], a[184:187], v[152:155], v[116:119]\n"
"buffer_load_dwordx4 v27, s[52:55], s66 offen offset:3072 nt lds\n"
"v_mfma_f32_16x16x32_bf16 v[132:135], a[184:187], v[184:187], v[132:135]\n"
"v_mfma_f32_16x16x32_bf16 v[148:151], a[184:187], v[216:219], v[148:151]\n"
"v_mfma_f32_16x16x32_bf16 v[164:167], a[184:187], v[248:251], v[164:167]\n"
"s_waitcnt lgkmcnt(3)\n"
"v_mfma_f32_16x16x32_bf16 v[108:111], a[188:191], v[96:99], v[40:43]\n"
"v_mfma_f32_16x16x32_bf16 v[124:127], a[188:191], v[128:131], v[44:47]\n"
"v_mfma_f32_16x16x32_bf16 v[140:143], a[188:191], v[160:163], v[48:51]\n"
"v_mfma_f32_16x16x32_bf16 v[156:159], a[188:191], v[192:195], v[52:55]\n"
"v_exp_f32 v100, v100\n"
"v_mfma_f32_16x16x32_bf16 v[172:175], a[188:191], v[224:227], v[56:59]\n"
"s_waitcnt lgkmcnt(2)\n"
"v_exp_f32 v101, v101\n"
"v_mfma_f32_16x16x32_bf16 v[108:111], a[192:195], v[104:107], v[108:111]\n"
"v_exp_f32 v102, v102\n"
"v_mfma_f32_16x16x32_bf16 v[124:127], a[192:195], v[136:139], v[124:127]\n"
"v_exp_f32 v103, v103\n"
"v_mfma_f32_16x16x32_bf16 v[140:143], a[192:195], v[168:171], v[140:143]\n"
"ds_read_b64_tr_b16 a[214:215], v38 offset:4096\n"
"v_cvt_pk_bf16_f32 v180, v100, v101\n"
"v_cvt_pk_bf16_f32 v181, v102, v103\n"
"v_mfma_f32_16x16x32_bf16 v[156:159], a[192:195], v[200:203], v[156:159]\n"
"ds_read_b64_tr_b16 a[212:213], v38 offset:0\n"
"v_exp_f32 v116, v116\n"
"v_mfma_f32_16x16x32_bf16 v[172:175], a[192:195], v[232:235], v[172:175]\n"
"s_waitcnt lgkmcnt(3)\n"
"v_exp_f32 v117, v117\n"
"v_mfma_f32_16x16x32_bf16 v[108:111], a[196:199], v[112:115], v[108:111]\n"
"v_exp_f32 v118, v118\n"
"v_mfma_f32_16x16x32_bf16 v[124:127], a[196:199], v[144:147], v[124:127]\n"
"v_exp_f32 v119, v119\n"
"v_mfma_f32_16x16x32_bf16 v[140:143], a[196:199], v[176:179], v[140:143]\n"
"ds_read_b64_tr_b16 a[216:217], v39 offset:0\n"
"v_cvt_pk_bf16_f32 v188, v116, v117\n"
"v_cvt_pk_bf16_f32 v189, v118, v119\n"
"v_mfma_f32_16x16x32_bf16 v[156:159], a[196:199], v[208:211], v[156:159]\n"
"ds_read_b64_tr_b16 a[218:219], v39 offset:4096\n"
"v_exp_f32 v132, v132\n"
"v_mfma_f32_16x16x32_bf16 v[172:175], a[196:199], v[240:243], v[172:175]\n"
"s_waitcnt lgkmcnt(4)\n"
"v_exp_f32 v133, v133\n"
"v_mfma_f32_16x16x32_bf16 v[108:111], a[200:203], v[120:123], v[108:111]\n"
"v_exp_f32 v134, v134\n"
"v_mfma_f32_16x16x32_bf16 v[124:127], a[200:203], v[152:155], v[124:127]\n"
"ds_read_b64_tr_b16 a[220:221], v38 offset:512\n"
"v_exp_f32 v135, v135\n"
"v_mfma_f32_16x16x32_bf16 v[140:143], a[200:203], v[184:187], v[140:143]\n"
"ds_read_b64_tr_b16 a[222:223], v38 offset:4608\n"
"v_cvt_pk_bf16_f32 v196, v132, v133\n"
"v_cvt_pk_bf16_f32 v197, v134, v135\n"
"v_mfma_f32_16x16x32_bf16 v[156:159], a[200:203], v[216:219], v[156:159]\n"
"v_exp_f32 v148, v148\n"
"v_mfma_f32_16x16x32_bf16 v[172:175], a[200:203], v[248:251], v[172:175]\n"
"v_exp_f32 v149, v149\n"
"v_mfma_f32_16x16x32_bf16 a[160:163], v[60:63], v[212:215], a[160:163]\n"
"ds_read_b64_tr_b16 a[224:225], v39 offset:512\n"
"v_exp_f32 v150, v150\n"
"v_mfma_f32_16x16x32_bf16 a[164:167], v[60:63], v[220:223], a[164:167]\n"
"ds_read_b64_tr_b16 a[226:227], v39 offset:4608\n"
"v_exp_f32 v151, v151\n"
"v_mfma_f32_16x16x32_bf16 a[168:171], v[60:63], v[228:231], a[168:171]\n"
"v_cvt_pk_bf16_f32 v204, v148, v149\n"
"v_cvt_pk_bf16_f32 v205, v150, v151\n"
"v_mfma_f32_16x16x32_bf16 a[172:175], v[60:63], v[236:239], a[172:175]\n"
"s_waitcnt lgkmcnt(6)\n"
"v_exp_f32 v164, v164\n"
"v_mfma_f32_16x16x32_bf16 a[0:3], a[212:215], v[212:215], a[0:3]\n"
"ds_read_b64_tr_b16 a[204:205], v38 offset:1024\n"
"v_exp_f32 v165, v165\n"
"v_mfma_f32_16x16x32_bf16 a[32:35], a[212:215], v[220:223], a[32:35]\n"
"ds_read_b64_tr_b16 a[206:207], v38 offset:5120\n"
"v_exp_f32 v166, v166\n"
"v_mfma_f32_16x16x32_bf16 a[64:67], a[212:215], v[228:231], a[64:67]\n"
"v_exp_f32 v167, v167\n"
"v_mfma_f32_16x16x32_bf16 a[96:99], a[212:215], v[236:239], a[96:99]\n"
"s_waitcnt lgkmcnt(6)\n"
"v_cvt_pk_bf16_f32 v244, v164, v165\n"
"v_cvt_pk_bf16_f32 v245, v166, v167\n"
"v_mfma_f32_16x16x32_bf16 a[4:7], a[216:219], v[212:215], a[4:7]\n"
"ds_read_b64_tr_b16 a[208:209], v39 offset:1024\n"
"v_exp_f32 v108, v108\n"
"v_mfma_f32_16x16x32_bf16 a[36:39], a[216:219], v[220:223], a[36:39]\n"
"ds_read_b64_tr_b16 a[210:211], v39 offset:5120\n"
"v_exp_f32 v109, v109\n"
"v_mfma_f32_16x16x32_bf16 a[68:71], a[216:219], v[228:231], a[68:71]\n"
"v_exp_f32 v110, v110\n"
"v_mfma_f32_16x16x32_bf16 a[100:103], a[216:219], v[236:239], a[100:103]\n"
"s_waitcnt lgkmcnt(6)\n"
"v_exp_f32 v111, v111\n"
"v_mfma_f32_16x16x32_bf16 a[8:11], a[220:223], v[212:215], a[8:11]\n"
"ds_read_b64_tr_b16 a[212:213], v38 offset:1536\n"
"v_cvt_pk_bf16_f32 v182, v108, v109\n"
"v_cvt_pk_bf16_f32 v183, v110, v111\n"
"v_mfma_f32_16x16x32_bf16 a[40:43], a[220:223], v[220:223], a[40:43]\n"
"ds_read_b64_tr_b16 a[214:215], v38 offset:5632\n"
"v_exp_f32 v124, v124\n"
"v_mfma_f32_16x16x32_bf16 a[72:75], a[220:223], v[228:231], a[72:75]\n"
"v_exp_f32 v125, v125\n"
"v_mfma_f32_16x16x32_bf16 a[104:107], a[220:223], v[236:239], a[104:107]\n"
"s_waitcnt lgkmcnt(6)\n"
"v_exp_f32 v126, v126\n"
"v_mfma_f32_16x16x32_bf16 a[12:15], a[224:227], v[212:215], a[12:15]\n"
"ds_read_b64_tr_b16 a[216:217], v39 offset:1536\n"
"v_exp_f32 v127, v127\n"
"v_mfma_f32_16x16x32_bf16 a[44:47], a[224:227], v[220:223], a[44:47]\n"
"ds_read_b64_tr_b16 a[218:219], v39 offset:5632\n"
"v_cvt_pk_bf16_f32 v190, v124, v125\n"
"v_cvt_pk_bf16_f32 v191, v126, v127\n"
"v_mfma_f32_16x16x32_bf16 a[76:79], a[224:227], v[228:231], a[76:79]\n"
"v_exp_f32 v140, v140\n"
"v_mfma_f32_16x16x32_bf16 a[108:111], a[224:227], v[236:239], a[108:111]\n"
"s_waitcnt lgkmcnt(6)\n"
"v_exp_f32 v141, v141\n"
"v_mfma_f32_16x16x32_bf16 a[16:19], a[204:207], v[212:215], a[16:19]\n"
"ds_read_b128 a[180:183], v32 offset:16384\n"
"v_exp_f32 v142, v142\n"
"v_mfma_f32_16x16x32_bf16 a[48:51], a[204:207], v[220:223], a[48:51]\n"
"v_exp_f32 v143, v143\n"
"v_mfma_f32_16x16x32_bf16 a[80:83], a[204:207], v[228:231], a[80:83]\n"
"v_cvt_pk_bf16_f32 v198, v140, v141\n"
"v_cvt_pk_bf16_f32 v199, v142, v143\n"
"v_mfma_f32_16x16x32_bf16 a[112:115], a[204:207], v[236:239], a[112:115]\n"
"s_waitcnt lgkmcnt(5)\n"
"v_exp_f32 v156, v156\n"
"v_mfma_f32_16x16x32_bf16 a[20:23], a[208:211], v[212:215], a[20:23]\n"
"ds_read_b128 a[184:187], v33 offset:16384\n"
"v_exp_f32 v157, v157\n"
"v_mfma_f32_16x16x32_bf16 a[52:55], a[208:211], v[220:223], a[52:55]\n"
"v_exp_f32 v158, v158\n"
"v_mfma_f32_16x16x32_bf16 a[84:87], a[208:211], v[228:231], a[84:87]\n"
"v_exp_f32 v159, v159\n"
"v_mfma_f32_16x16x32_bf16 a[116:119], a[208:211], v[236:239], a[116:119]\n"
"s_waitcnt lgkmcnt(4)\n"
"v_cvt_pk_bf16_f32 v206, v156, v157\n"
"v_cvt_pk_bf16_f32 v207, v158, v159\n"
"v_mfma_f32_16x16x32_bf16 a[24:27], a[212:215], v[212:215], a[24:27]\n"
"ds_read_b128 a[188:191], v34 offset:16384\n"
"v_exp_f32 v172, v172\n"
"v_mfma_f32_16x16x32_bf16 a[56:59], a[212:215], v[220:223], a[56:59]\n"
"v_exp_f32 v173, v173\n"
"v_mfma_f32_16x16x32_bf16 a[88:91], a[212:215], v[228:231], a[88:91]\n"
"v_exp_f32 v174, v174\n"
"v_mfma_f32_16x16x32_bf16 a[120:123], a[212:215], v[236:239], a[120:123]\n"
"s_waitcnt lgkmcnt(3)\n"
"v_exp_f32 v175, v175\n"
"v_mfma_f32_16x16x32_bf16 a[28:31], a[216:219], v[212:215], a[28:31]\n"
"ds_read_b128 a[192:195], v35 offset:16384\n"
"v_cvt_pk_bf16_f32 v246, v172, v173\n"
"v_cvt_pk_bf16_f32 v247, v174, v175\n"
"v_mfma_f32_16x16x32_bf16 a[60:63], a[216:219], v[220:223], a[60:63]\n"
"v_mfma_f32_16x16x32_bf16 a[92:95], a[216:219], v[228:231], a[92:95]\n"
"v_mfma_f32_16x16x32_bf16 a[124:127], a[216:219], v[236:239], a[124:127]\n"
"s_waitcnt lgkmcnt(3)\n"
"v_mfma_f32_16x16x32_bf16 v[100:103], a[180:183], v[96:99], v[40:43]\n"
"ds_read_b128 a[196:199], v32 offset:20480\n"
"v_mfma_f32_16x16x32_bf16 v[116:119], a[180:183], v[128:131], v[44:47]\n"
"s_add_u32 m0, s61, 0\n"
"s_mov_b32 s65, 65536\n"
"v_mfma_f32_16x16x32_bf16 v[132:135], a[180:183], v[160:163], v[48:51]\n"
"buffer_load_dwordx4 v24, s[48:51], s65 offen nt lds\n"
"v_mfma_f32_16x16x32_bf16 v[148:151], a[180:183], v[192:195], v[52:55]\n"
"s_waitcnt lgkmcnt(3)\n"
"v_mfma_f32_16x16x32_bf16 v[100:103], a[184:187], v[104:107], v[100:103]\n"
"ds_read_b128 a[200:203], v33 offset:20480\n"
"v_mfma_f32_16x16x32_bf16 v[116:119], a[184:187], v[136:139], v[116:119]\n"
"v_mfma_f32_16x16x32_bf16 v[132:135], a[184:187], v[168:171], v[132:135]\n"
"buffer_load_dwordx4 v25, s[48:51], s65 offen offset:1024 nt lds\n"
"v_mfma_f32_16x16x32_bf16 v[148:151], a[184:187], v[200:203], v[148:151]\n"
"s_waitcnt lgkmcnt(3)\n"
"v_mfma_f32_16x16x32_bf16 v[100:103], a[188:191], v[112:115], v[100:103]\n"
"ds_read_b128 a[180:183], v34 offset:20480\n"
"v_mfma_f32_16x16x32_bf16 v[116:119], a[188:191], v[144:147], v[116:119]\n"
"v_mfma_f32_16x16x32_bf16 v[132:135], a[188:191], v[176:179], v[132:135]\n"
"buffer_load_dwordx4 v93, s[48:51], s65 offen offset:2048 nt lds\n"
"v_mfma_f32_16x16x32_bf16 v[148:151], a[188:191], v[208:211], v[148:151]\n"
"s_waitcnt lgkmcnt(3)\n"
"v_mfma_f32_16x16x32_bf16 v[100:103], a[192:195], v[120:123], v[100:103]\n"
"ds_read_b128 a[184:187], v35 offset:20480\n"
"v_mfma_f32_16x16x32_bf16 v[116:119], a[192:195], v[152:155], v[116:119]\n"
"v_mfma_f32_16x16x32_bf16 v[132:135], a[192:195], v[184:187], v[132:135]\n"
"buffer_load_dwordx4 v94, s[48:51], s65 offen offset:3072 nt lds\n"
"v_mfma_f32_16x16x32_bf16 v[148:151], a[192:195], v[216:219], v[148:151]\n"
"s_waitcnt lgkmcnt(3)\n"
"v_mfma_f32_16x16x32_bf16 v[108:111], a[196:199], v[96:99], v[40:43]\n"
"v_mfma_f32_16x16x32_bf16 v[124:127], a[196:199], v[128:131], v[44:47]\n"
"ds_read_b64_tr_b16 a[220:221], v36 offset:16384\n"
"v_mfma_f32_16x16x32_bf16 v[140:143], a[196:199], v[160:163], v[48:51]\n"
"ds_read_b64_tr_b16 a[222:223], v36 offset:20480\n"
"v_mfma_f32_16x16x32_bf16 v[156:159], a[196:199], v[192:195], v[52:55]\n"
"s_waitcnt lgkmcnt(4)\n"
"v_mfma_f32_16x16x32_bf16 v[108:111], a[200:203], v[104:107], v[108:111]\n"
"v_exp_f32 v100, v100\n"
"v_mfma_f32_16x16x32_bf16 v[124:127], a[200:203], v[136:139], v[124:127]\n"
"ds_read_b64_tr_b16 a[226:227], v37 offset:20480\n"
"v_exp_f32 v101, v101\n"
"v_mfma_f32_16x16x32_bf16 v[140:143], a[200:203], v[168:171], v[140:143]\n"
"ds_read_b64_tr_b16 a[224:225], v37 offset:16384\n"
"v_exp_f32 v102, v102\n"
"v_mfma_f32_16x16x32_bf16 v[156:159], a[200:203], v[200:203], v[156:159]\n"
"s_waitcnt lgkmcnt(5)\n"
"v_exp_f32 v103, v103\n"
"v_mfma_f32_16x16x32_bf16 v[108:111], a[180:183], v[112:115], v[108:111]\n"
"v_cvt_pk_bf16_f32 v212, v100, v101\n"
"v_cvt_pk_bf16_f32 v213, v102, v103\n"
"v_mfma_f32_16x16x32_bf16 v[124:127], a[180:183], v[144:147], v[124:127]\n"
"v_exp_f32 v116, v116\n"
"v_mfma_f32_16x16x32_bf16 v[140:143], a[180:183], v[176:179], v[140:143]\n"
"ds_read_b64_tr_b16 a[204:205], v36 offset:16896\n"
"v_exp_f32 v117, v117\n"
"v_mfma_f32_16x16x32_bf16 v[156:159], a[180:183], v[208:211], v[156:159]\n"
"s_waitcnt lgkmcnt(5)\n"
"v_exp_f32 v118, v118\n"
"v_mfma_f32_16x16x32_bf16 v[108:111], a[184:187], v[120:123], v[108:111]\n"
"ds_read_b64_tr_b16 a[206:207], v36 offset:20992\n"
"v_exp_f32 v119, v119\n"
"v_mfma_f32_16x16x32_bf16 v[124:127], a[184:187], v[152:155], v[124:127]\n"
"v_cvt_pk_bf16_f32 v220, v116, v117\n"
"v_cvt_pk_bf16_f32 v221, v118, v119\n"
"v_mfma_f32_16x16x32_bf16 v[140:143], a[184:187], v[184:187], v[140:143]\n"
"v_exp_f32 v132, v132\n"
"v_mfma_f32_16x16x32_bf16 v[156:159], a[184:187], v[216:219], v[156:159]\n"
"s_waitcnt vmcnt(12)\n"
"s_barrier\n"
"v_exp_f32 v133, v133\n"
"v_mfma_f32_16x16x32_bf16 a[160:163], v[60:63], v[180:183], a[160:163]\n"
"ds_read_b64_tr_b16 a[208:209], v37 offset:16896\n"
"v_exp_f32 v134, v134\n"
"v_mfma_f32_16x16x32_bf16 a[164:167], v[60:63], v[188:191], a[164:167]\n"
"ds_read_b64_tr_b16 a[210:211], v37 offset:20992\n"
"v_exp_f32 v135, v135\n"
"v_mfma_f32_16x16x32_bf16 a[168:171], v[60:63], v[196:199], a[168:171]\n"
"v_cvt_pk_bf16_f32 v228, v132, v133\n"
"v_cvt_pk_bf16_f32 v229, v134, v135\n"
"v_mfma_f32_16x16x32_bf16 a[172:175], v[60:63], v[204:207], a[172:175]\n"
"v_exp_f32 v148, v148\n"
"v_mfma_f32_16x16x32_bf16 a[176:179], v[60:63], v[244:247], a[176:179]\n"
"s_waitcnt lgkmcnt(6)\n"
"v_exp_f32 v149, v149\n"
"v_mfma_f32_16x16x32_bf16 a[0:3], a[220:223], v[180:183], a[0:3]\n"
"ds_read_b64_tr_b16 a[212:213], v36 offset:17408\n"
"v_exp_f32 v150, v150\n"
"v_mfma_f32_16x16x32_bf16 a[32:35], a[220:223], v[188:191], a[32:35]\n"
"ds_read_b64_tr_b16 a[214:215], v36 offset:21504\n"
"v_exp_f32 v151, v151\n"
"v_mfma_f32_16x16x32_bf16 a[64:67], a[220:223], v[196:199], a[64:67]\n"
"v_cvt_pk_bf16_f32 v236, v148, v149\n"
"v_cvt_pk_bf16_f32 v237, v150, v151\n"
"v_mfma_f32_16x16x32_bf16 a[96:99], a[220:223], v[204:207], a[96:99]\n"
"v_exp_f32 v108, v108\n"
"v_mfma_f32_16x16x32_bf16 a[128:131], a[220:223], v[244:247], a[128:131]\n"
"s_waitcnt lgkmcnt(6)\n"
"v_exp_f32 v109, v109\n"
"v_mfma_f32_16x16x32_bf16 a[4:7], a[224:227], v[180:183], a[4:7]\n"
"ds_read_b64_tr_b16 a[216:217], v37 offset:17408\n"
"v_exp_f32 v110, v110\n"
"v_mfma_f32_16x16x32_bf16 a[36:39], a[224:227], v[188:191], a[36:39]\n"
"ds_read_b64_tr_b16 a[218:219], v37 offset:21504\n"
"v_exp_f32 v111, v111\n"
"v_mfma_f32_16x16x32_bf16 a[68:71], a[224:227], v[196:199], a[68:71]\n"
"v_cvt_pk_bf16_f32 v214, v108, v109\n"
"v_cvt_pk_bf16_f32 v215, v110, v111\n"
"v_mfma_f32_16x16x32_bf16 a[100:103], a[224:227], v[204:207], a[100:103]\n"
"v_exp_f32 v124, v124\n"
"v_mfma_f32_16x16x32_bf16 a[132:135], a[224:227], v[244:247], a[132:135]\n"
"s_waitcnt lgkmcnt(6)\n"
"v_exp_f32 v125, v125\n"
"v_mfma_f32_16x16x32_bf16 a[8:11], a[204:207], v[180:183], a[8:11]\n"
"ds_read_b64_tr_b16 a[220:221], v36 offset:17920\n"
"v_exp_f32 v126, v126\n"
"v_mfma_f32_16x16x32_bf16 a[40:43], a[204:207], v[188:191], a[40:43]\n"
"ds_read_b64_tr_b16 a[222:223], v36 offset:22016\n"
"v_exp_f32 v127, v127\n"
"v_mfma_f32_16x16x32_bf16 a[72:75], a[204:207], v[196:199], a[72:75]\n"
"v_cvt_pk_bf16_f32 v222, v124, v125\n"
"v_cvt_pk_bf16_f32 v223, v126, v127\n"
"v_mfma_f32_16x16x32_bf16 a[104:107], a[204:207], v[204:207], a[104:107]\n"
"v_exp_f32 v140, v140\n"
"v_mfma_f32_16x16x32_bf16 a[136:139], a[204:207], v[244:247], a[136:139]\n"
"s_waitcnt lgkmcnt(6)\n"
"v_exp_f32 v141, v141\n"
"v_mfma_f32_16x16x32_bf16 a[12:15], a[208:211], v[180:183], a[12:15]\n"
"ds_read_b64_tr_b16 a[224:225], v37 offset:17920\n"
"v_exp_f32 v142, v142\n"
"v_mfma_f32_16x16x32_bf16 a[44:47], a[208:211], v[188:191], a[44:47]\n"
"ds_read_b64_tr_b16 a[226:227], v37 offset:22016\n"
"v_exp_f32 v143, v143\n"
"v_mfma_f32_16x16x32_bf16 a[76:79], a[208:211], v[196:199], a[76:79]\n"
"v_cvt_pk_bf16_f32 v230, v140, v141\n"
"v_cvt_pk_bf16_f32 v231, v142, v143\n"
"v_mfma_f32_16x16x32_bf16 a[108:111], a[208:211], v[204:207], a[108:111]\n"
"v_exp_f32 v156, v156\n"
"v_mfma_f32_16x16x32_bf16 a[140:143], a[208:211], v[244:247], a[140:143]\n"
"s_waitcnt lgkmcnt(6)\n"
"v_exp_f32 v157, v157\n"
"v_mfma_f32_16x16x32_bf16 a[16:19], a[212:215], v[180:183], a[16:19]\n"
"ds_read_b128 a[188:191], v28 offset:32768\n"
"v_exp_f32 v158, v158\n"
"v_mfma_f32_16x16x32_bf16 a[48:51], a[212:215], v[188:191], a[48:51]\n"
"v_exp_f32 v159, v159\n"
"v_mfma_f32_16x16x32_bf16 a[80:83], a[212:215], v[196:199], a[80:83]\n"
"v_cvt_pk_bf16_f32 v238, v156, v157\n"
"v_cvt_pk_bf16_f32 v239, v158, v159\n"
"v_mfma_f32_16x16x32_bf16 a[112:115], a[212:215], v[204:207], a[112:115]\n"
"v_mfma_f32_16x16x32_bf16 a[144:147], a[212:215], v[244:247], a[144:147]\n"
"s_waitcnt lgkmcnt(5)\n"
"v_mfma_f32_16x16x32_bf16 a[20:23], a[216:219], v[180:183], a[20:23]\n"
"ds_read_b128 a[192:195], v29 offset:32768\n"
"v_mfma_f32_16x16x32_bf16 a[52:55], a[216:219], v[188:191], a[52:55]\n"
"v_mfma_f32_16x16x32_bf16 a[84:87], a[216:219], v[196:199], a[84:87]\n"
"v_mfma_f32_16x16x32_bf16 a[116:119], a[216:219], v[204:207], a[116:119]\n"
"v_mfma_f32_16x16x32_bf16 a[148:151], a[216:219], v[244:247], a[148:151]\n"
"s_waitcnt lgkmcnt(4)\n"
"v_mfma_f32_16x16x32_bf16 a[24:27], a[220:223], v[180:183], a[24:27]\n"
"ds_read_b128 a[196:199], v30 offset:32768\n"
"v_mfma_f32_16x16x32_bf16 a[56:59], a[220:223], v[188:191], a[56:59]\n"
"v_mfma_f32_16x16x32_bf16 a[88:91], a[220:223], v[196:199], a[88:91]\n"
"v_mfma_f32_16x16x32_bf16 a[120:123], a[220:223], v[204:207], a[120:123]\n"
"v_mfma_f32_16x16x32_bf16 a[152:155], a[220:223], v[244:247], a[152:155]\n"
"s_waitcnt lgkmcnt(3)\n"
"v_mfma_f32_16x16x32_bf16 a[28:31], a[224:227], v[180:183], a[28:31]\n"
"ds_read_b128 a[200:203], v31 offset:32768\n"
"v_mfma_f32_16x16x32_bf16 a[60:63], a[224:227], v[188:191], a[60:63]\n"
"v_mfma_f32_16x16x32_bf16 a[92:95], a[224:227], v[196:199], a[92:95]\n"
"v_mfma_f32_16x16x32_bf16 a[124:127], a[224:227], v[204:207], a[124:127]\n"
"v_mfma_f32_16x16x32_bf16 a[156:159], a[224:227], v[244:247], a[156:159]\n"
"s_waitcnt lgkmcnt(3)\n"
"v_mfma_f32_16x16x32_bf16 v[100:103], a[188:191], v[96:99], v[40:43]\n"
"ds_read_b128 a[180:183], v28 offset:36864\n"
"v_mfma_f32_16x16x32_bf16 v[116:119], a[188:191], v[128:131], v[44:47]\n"
"s_add_u32 m0, s61, 65536\n"
"s_mov_b32 s66, 65536\n"
"v_mfma_f32_16x16x32_bf16 v[132:135], a[188:191], v[160:163], v[48:51]\n"
"buffer_load_dwordx4 v95, s[52:55], s66 offen nt lds\n"
"v_mfma_f32_16x16x32_bf16 v[148:151], a[188:191], v[192:195], v[52:55]\n"
"v_mfma_f32_16x16x32_bf16 v[164:167], a[188:191], v[224:227], v[56:59]\n"
"s_waitcnt lgkmcnt(3)\n"
"v_mfma_f32_16x16x32_bf16 v[100:103], a[192:195], v[104:107], v[100:103]\n"
"ds_read_b128 a[184:187], v29 offset:36864\n"
"v_mfma_f32_16x16x32_bf16 v[116:119], a[192:195], v[136:139], v[116:119]\n"
"buffer_load_dwordx4 v27, s[52:55], s66 offen offset:1024 nt lds\n"
"v_mfma_f32_16x16x32_bf16 v[132:135], a[192:195], v[168:171], v[132:135]\n"
"v_mfma_f32_16x16x32_bf16 v[148:151], a[192:195], v[200:203], v[148:151]\n"
"v_mfma_f32_16x16x32_bf16 v[164:167], a[192:195], v[232:235], v[164:167]\n"
"s_waitcnt lgkmcnt(3)\n"
"v_mfma_f32_16x16x32_bf16 v[100:103], a[196:199], v[112:115], v[100:103]\n"
"ds_read_b128 a[188:191], v30 offset:36864\n"
"v_mfma_f32_16x16x32_bf16 v[116:119], a[196:199], v[144:147], v[116:119]\n"
"buffer_load_dwordx4 v95, s[52:55], s66 offen offset:2048 nt lds\n"
"v_mfma_f32_16x16x32_bf16 v[132:135], a[196:199], v[176:179], v[132:135]\n"
"v_mfma_f32_16x16x32_bf16 v[148:151], a[196:199], v[208:211], v[148:151]\n"
"v_mfma_f32_16x16x32_bf16 v[164:167], a[196:199], v[240:243], v[164:167]\n"
"s_waitcnt lgkmcnt(3)\n"
"v_mfma_f32_16x16x32_bf16 v[100:103], a[200:203], v[120:123], v[100:103]\n"
"ds_read_b128 a[192:195], v31 offset:36864\n"
"v_mfma_f32_16x16x32_bf16 v[116:119], a[200:203], v[152:155], v[116:119]\n"
"buffer_load_dwordx4 v27, s[52:55], s66 offen offset:3072 nt lds\n"
"v_mfma_f32_16x16x32_bf16 v[132:135], a[200:203], v[184:187], v[132:135]\n"
"v_mfma_f32_16x16x32_bf16 v[148:151], a[200:203], v[216:219], v[148:151]\n"
"v_mfma_f32_16x16x32_bf16 v[164:167], a[200:203], v[248:251], v[164:167]\n"
"s_waitcnt lgkmcnt(3)\n"
"v_mfma_f32_16x16x32_bf16 v[108:111], a[180:183], v[96:99], v[40:43]\n"
"v_mfma_f32_16x16x32_bf16 v[124:127], a[180:183], v[128:131], v[44:47]\n"
"v_mfma_f32_16x16x32_bf16 v[140:143], a[180:183], v[160:163], v[48:51]\n"
"v_mfma_f32_16x16x32_bf16 v[156:159], a[180:183], v[192:195], v[52:55]\n"
"v_exp_f32 v100, v100\n"
"v_mfma_f32_16x16x32_bf16 v[172:175], a[180:183], v[224:227], v[56:59]\n"
"s_waitcnt lgkmcnt(2)\n"
"v_exp_f32 v101, v101\n"
"v_mfma_f32_16x16x32_bf16 v[108:111], a[184:187], v[104:107], v[108:111]\n"
"v_exp_f32 v102, v102\n"
"v_mfma_f32_16x16x32_bf16 v[124:127], a[184:187], v[136:139], v[124:127]\n"
"v_exp_f32 v103, v103\n"
"v_mfma_f32_16x16x32_bf16 v[140:143], a[184:187], v[168:171], v[140:143]\n"
"ds_read_b64_tr_b16 a[206:207], v38 offset:20480\n"
"v_cvt_pk_bf16_f32 v180, v100, v101\n"
"v_cvt_pk_bf16_f32 v181, v102, v103\n"
"v_mfma_f32_16x16x32_bf16 v[156:159], a[184:187], v[200:203], v[156:159]\n"
"ds_read_b64_tr_b16 a[204:205], v38 offset:16384\n"
"v_exp_f32 v116, v116\n"
"v_mfma_f32_16x16x32_bf16 v[172:175], a[184:187], v[232:235], v[172:175]\n"
"s_waitcnt lgkmcnt(3)\n"
"v_exp_f32 v117, v117\n"
"v_mfma_f32_16x16x32_bf16 v[108:111], a[188:191], v[112:115], v[108:111]\n"
"v_exp_f32 v118, v118\n"
"v_mfma_f32_16x16x32_bf16 v[124:127], a[188:191], v[144:147], v[124:127]\n"
"v_exp_f32 v119, v119\n"
"v_mfma_f32_16x16x32_bf16 v[140:143], a[188:191], v[176:179], v[140:143]\n"
"ds_read_b64_tr_b16 a[208:209], v39 offset:16384\n"
"v_cvt_pk_bf16_f32 v188, v116, v117\n"
"v_cvt_pk_bf16_f32 v189, v118, v119\n"
"v_mfma_f32_16x16x32_bf16 v[156:159], a[188:191], v[208:211], v[156:159]\n"
"ds_read_b64_tr_b16 a[210:211], v39 offset:20480\n"
"v_exp_f32 v132, v132\n"
"v_mfma_f32_16x16x32_bf16 v[172:175], a[188:191], v[240:243], v[172:175]\n"
"s_waitcnt lgkmcnt(4)\n"
"v_exp_f32 v133, v133\n"
"v_mfma_f32_16x16x32_bf16 v[108:111], a[192:195], v[120:123], v[108:111]\n"
"v_exp_f32 v134, v134\n"
"v_mfma_f32_16x16x32_bf16 v[124:127], a[192:195], v[152:155], v[124:127]\n"
"ds_read_b64_tr_b16 a[212:213], v38 offset:16896\n"
"v_exp_f32 v135, v135\n"
"v_mfma_f32_16x16x32_bf16 v[140:143], a[192:195], v[184:187], v[140:143]\n"
"ds_read_b64_tr_b16 a[214:215], v38 offset:20992\n"
"v_cvt_pk_bf16_f32 v196, v132, v133\n"
"v_cvt_pk_bf16_f32 v197, v134, v135\n"
"v_mfma_f32_16x16x32_bf16 v[156:159], a[192:195], v[216:219], v[156:159]\n"
"v_exp_f32 v148, v148\n"
"v_mfma_f32_16x16x32_bf16 v[172:175], a[192:195], v[248:251], v[172:175]\n"
"v_exp_f32 v149, v149\n"
"v_mfma_f32_16x16x32_bf16 a[160:163], v[60:63], v[212:215], a[160:163]\n"
"ds_read_b64_tr_b16 a[216:217], v39 offset:16896\n"
"v_exp_f32 v150, v150\n"
"v_mfma_f32_16x16x32_bf16 a[164:167], v[60:63], v[220:223], a[164:167]\n"
"ds_read_b64_tr_b16 a[218:219], v39 offset:20992\n"
"v_exp_f32 v151, v151\n"
"v_mfma_f32_16x16x32_bf16 a[168:171], v[60:63], v[228:231], a[168:171]\n"
"v_cvt_pk_bf16_f32 v204, v148, v149\n"
"v_cvt_pk_bf16_f32 v205, v150, v151\n"
"v_mfma_f32_16x16x32_bf16 a[172:175], v[60:63], v[236:239], a[172:175]\n"
"s_waitcnt lgkmcnt(6)\n"
"v_exp_f32 v164, v164\n"
"v_mfma_f32_16x16x32_bf16 a[0:3], a[204:207], v[212:215], a[0:3]\n"
"ds_read_b64_tr_b16 a[220:221], v38 offset:17408\n"
"v_exp_f32 v165, v165\n"
"v_mfma_f32_16x16x32_bf16 a[32:35], a[204:207], v[220:223], a[32:35]\n"
"ds_read_b64_tr_b16 a[222:223], v38 offset:21504\n"
"v_exp_f32 v166, v166\n"
"v_mfma_f32_16x16x32_bf16 a[64:67], a[204:207], v[228:231], a[64:67]\n"
"v_exp_f32 v167, v167\n"
"v_mfma_f32_16x16x32_bf16 a[96:99], a[204:207], v[236:239], a[96:99]\n"
"s_waitcnt lgkmcnt(6)\n"
"v_cvt_pk_bf16_f32 v244, v164, v165\n"
"v_cvt_pk_bf16_f32 v245, v166, v167\n"
"v_mfma_f32_16x16x32_bf16 a[4:7], a[208:211], v[212:215], a[4:7]\n"
"ds_read_b64_tr_b16 a[224:225], v39 offset:17408\n"
"v_exp_f32 v108, v108\n"
"v_mfma_f32_16x16x32_bf16 a[36:39], a[208:211], v[220:223], a[36:39]\n"
"ds_read_b64_tr_b16 a[226:227], v39 offset:21504\n"
"v_exp_f32 v109, v109\n"
"v_mfma_f32_16x16x32_bf16 a[68:71], a[208:211], v[228:231], a[68:71]\n"
"v_exp_f32 v110, v110\n"
"v_mfma_f32_16x16x32_bf16 a[100:103], a[208:211], v[236:239], a[100:103]\n"
"s_waitcnt lgkmcnt(6)\n"
"v_exp_f32 v111, v111\n"
"v_mfma_f32_16x16x32_bf16 a[8:11], a[212:215], v[212:215], a[8:11]\n"
"ds_read_b64_tr_b16 a[204:205], v38 offset:17920\n"
"v_cvt_pk_bf16_f32 v182, v108, v109\n"
"v_cvt_pk_bf16_f32 v183, v110, v111\n"
"v_mfma_f32_16x16x32_bf16 a[40:43], a[212:215], v[220:223], a[40:43]\n"
"ds_read_b64_tr_b16 a[206:207], v38 offset:22016\n"
"v_exp_f32 v124, v124\n"
"v_mfma_f32_16x16x32_bf16 a[72:75], a[212:215], v[228:231], a[72:75]\n"
"v_exp_f32 v125, v125\n"
"v_mfma_f32_16x16x32_bf16 a[104:107], a[212:215], v[236:239], a[104:107]\n"
"s_waitcnt lgkmcnt(6)\n"
"v_exp_f32 v126, v126\n"
"v_mfma_f32_16x16x32_bf16 a[12:15], a[216:219], v[212:215], a[12:15]\n"
"ds_read_b64_tr_b16 a[208:209], v39 offset:17920\n"
"v_exp_f32 v127, v127\n"
"v_mfma_f32_16x16x32_bf16 a[44:47], a[216:219], v[220:223], a[44:47]\n"
"ds_read_b64_tr_b16 a[210:211], v39 offset:22016\n"
"v_cvt_pk_bf16_f32 v190, v124, v125\n"
"v_cvt_pk_bf16_f32 v191, v126, v127\n"
"v_mfma_f32_16x16x32_bf16 a[76:79], a[216:219], v[228:231], a[76:79]\n"
"v_exp_f32 v140, v140\n"
"v_mfma_f32_16x16x32_bf16 a[108:111], a[216:219], v[236:239], a[108:111]\n"
"s_waitcnt lgkmcnt(6)\n"
"v_exp_f32 v141, v141\n"
"v_mfma_f32_16x16x32_bf16 a[16:19], a[220:223], v[212:215], a[16:19]\n"
"ds_read_b128 a[196:199], v32 offset:32768\n"
"v_exp_f32 v142, v142\n"
"v_mfma_f32_16x16x32_bf16 a[48:51], a[220:223], v[220:223], a[48:51]\n"
"v_exp_f32 v143, v143\n"
"v_mfma_f32_16x16x32_bf16 a[80:83], a[220:223], v[228:231], a[80:83]\n"
"v_cvt_pk_bf16_f32 v198, v140, v141\n"
"v_cvt_pk_bf16_f32 v199, v142, v143\n"
"v_mfma_f32_16x16x32_bf16 a[112:115], a[220:223], v[236:239], a[112:115]\n"
"s_waitcnt lgkmcnt(5)\n"
"v_exp_f32 v156, v156\n"
"v_mfma_f32_16x16x32_bf16 a[20:23], a[224:227], v[212:215], a[20:23]\n"
"ds_read_b128 a[200:203], v33 offset:32768\n"
"v_exp_f32 v157, v157\n"
"v_mfma_f32_16x16x32_bf16 a[52:55], a[224:227], v[220:223], a[52:55]\n"
"v_exp_f32 v158, v158\n"
"v_mfma_f32_16x16x32_bf16 a[84:87], a[224:227], v[228:231], a[84:87]\n"
"v_exp_f32 v159, v159\n"
"v_mfma_f32_16x16x32_bf16 a[116:119], a[224:227], v[236:239], a[116:119]\n"
"s_waitcnt lgkmcnt(4)\n"
"v_cvt_pk_bf16_f32 v206, v156, v157\n"
"v_cvt_pk_bf16_f32 v207, v158, v159\n"
"v_mfma_f32_16x16x32_bf16 a[24:27], a[204:207], v[212:215], a[24:27]\n"
"ds_read_b128 a[180:183], v34 offset:32768\n"
"v_exp_f32 v172, v172\n"
"v_mfma_f32_16x16x32_bf16 a[56:59], a[204:207], v[220:223], a[56:59]\n"
"v_exp_f32 v173, v173\n"
"v_mfma_f32_16x16x32_bf16 a[88:91], a[204:207], v[228:231], a[88:91]\n"
"v_exp_f32 v174, v174\n"
"v_mfma_f32_16x16x32_bf16 a[120:123], a[204:207], v[236:239], a[120:123]\n"
"s_waitcnt lgkmcnt(3)\n"
"v_exp_f32 v175, v175\n"
"v_mfma_f32_16x16x32_bf16 a[28:31], a[208:211], v[212:215], a[28:31]\n"
"ds_read_b128 a[184:187], v35 offset:32768\n"
"v_cvt_pk_bf16_f32 v246, v172, v173\n"
"v_cvt_pk_bf16_f32 v247, v174, v175\n"
"v_mfma_f32_16x16x32_bf16 a[60:63], a[208:211], v[220:223], a[60:63]\n"
"v_mfma_f32_16x16x32_bf16 a[92:95], a[208:211], v[228:231], a[92:95]\n"
"v_mfma_f32_16x16x32_bf16 a[124:127], a[208:211], v[236:239], a[124:127]\n"
"s_waitcnt lgkmcnt(3)\n"
"v_mfma_f32_16x16x32_bf16 v[100:103], a[196:199], v[96:99], v[40:43]\n"
"ds_read_b128 a[188:191], v32 offset:36864\n"
"v_mfma_f32_16x16x32_bf16 v[116:119], a[196:199], v[128:131], v[44:47]\n"
"s_add_u32 m0, s61, 16384\n"
"s_mov_b32 s65, 81920\n"
"v_mfma_f32_16x16x32_bf16 v[132:135], a[196:199], v[160:163], v[48:51]\n"
"buffer_load_dwordx4 v24, s[48:51], s65 offen nt lds\n"
"v_mfma_f32_16x16x32_bf16 v[148:151], a[196:199], v[192:195], v[52:55]\n"
"s_waitcnt lgkmcnt(3)\n"
"v_mfma_f32_16x16x32_bf16 v[100:103], a[200:203], v[104:107], v[100:103]\n"
"ds_read_b128 a[192:195], v33 offset:36864\n"
"v_mfma_f32_16x16x32_bf16 v[116:119], a[200:203], v[136:139], v[116:119]\n"
"v_mfma_f32_16x16x32_bf16 v[132:135], a[200:203], v[168:171], v[132:135]\n"
"buffer_load_dwordx4 v25, s[48:51], s65 offen offset:1024 nt lds\n"
"v_mfma_f32_16x16x32_bf16 v[148:151], a[200:203], v[200:203], v[148:151]\n"
"s_waitcnt lgkmcnt(3)\n"
"v_mfma_f32_16x16x32_bf16 v[100:103], a[180:183], v[112:115], v[100:103]\n"
"ds_read_b128 a[196:199], v34 offset:36864\n"
"v_mfma_f32_16x16x32_bf16 v[116:119], a[180:183], v[144:147], v[116:119]\n"
"v_mfma_f32_16x16x32_bf16 v[132:135], a[180:183], v[176:179], v[132:135]\n"
"buffer_load_dwordx4 v93, s[48:51], s65 offen offset:2048 nt lds\n"
"v_mfma_f32_16x16x32_bf16 v[148:151], a[180:183], v[208:211], v[148:151]\n"
"s_waitcnt lgkmcnt(3)\n"
"v_mfma_f32_16x16x32_bf16 v[100:103], a[184:187], v[120:123], v[100:103]\n"
"ds_read_b128 a[200:203], v35 offset:36864\n"
"v_mfma_f32_16x16x32_bf16 v[116:119], a[184:187], v[152:155], v[116:119]\n"
"v_mfma_f32_16x16x32_bf16 v[132:135], a[184:187], v[184:187], v[132:135]\n"
"buffer_load_dwordx4 v94, s[48:51], s65 offen offset:3072 nt lds\n"
"v_mfma_f32_16x16x32_bf16 v[148:151], a[184:187], v[216:219], v[148:151]\n"
"s_waitcnt lgkmcnt(3)\n"
"v_mfma_f32_16x16x32_bf16 v[108:111], a[188:191], v[96:99], v[40:43]\n"
"v_mfma_f32_16x16x32_bf16 v[124:127], a[188:191], v[128:131], v[44:47]\n"
"ds_read_b64_tr_b16 a[212:213], v36 offset:32768\n"
"v_mfma_f32_16x16x32_bf16 v[140:143], a[188:191], v[160:163], v[48:51]\n"
"ds_read_b64_tr_b16 a[214:215], v36 offset:36864\n"
"v_mfma_f32_16x16x32_bf16 v[156:159], a[188:191], v[192:195], v[52:55]\n"
"s_waitcnt lgkmcnt(4)\n"
"v_mfma_f32_16x16x32_bf16 v[108:111], a[192:195], v[104:107], v[108:111]\n"
"v_exp_f32 v100, v100\n"
"v_mfma_f32_16x16x32_bf16 v[124:127], a[192:195], v[136:139], v[124:127]\n"
"ds_read_b64_tr_b16 a[218:219], v37 offset:36864\n"
"v_exp_f32 v101, v101\n"
"v_mfma_f32_16x16x32_bf16 v[140:143], a[192:195], v[168:171], v[140:143]\n"
"ds_read_b64_tr_b16 a[216:217], v37 offset:32768\n"
"v_exp_f32 v102, v102\n"
"v_mfma_f32_16x16x32_bf16 v[156:159], a[192:195], v[200:203], v[156:159]\n"
"s_waitcnt lgkmcnt(5)\n"
"v_exp_f32 v103, v103\n"
"v_mfma_f32_16x16x32_bf16 v[108:111], a[196:199], v[112:115], v[108:111]\n"
"v_cvt_pk_bf16_f32 v212, v100, v101\n"
"v_cvt_pk_bf16_f32 v213, v102, v103\n"
"v_mfma_f32_16x16x32_bf16 v[124:127], a[196:199], v[144:147], v[124:127]\n"
"v_exp_f32 v116, v116\n"
"v_mfma_f32_16x16x32_bf16 v[140:143], a[196:199], v[176:179], v[140:143]\n"
"ds_read_b64_tr_b16 a[220:221], v36 offset:33280\n"
"v_exp_f32 v117, v117\n"
"v_mfma_f32_16x16x32_bf16 v[156:159], a[196:199], v[208:211], v[156:159]\n"
"s_waitcnt lgkmcnt(5)\n"
"v_exp_f32 v118, v118\n"
"v_mfma_f32_16x16x32_bf16 v[108:111], a[200:203], v[120:123], v[108:111]\n"
"ds_read_b64_tr_b16 a[222:223], v36 offset:37376\n"
"v_exp_f32 v119, v119\n"
"v_mfma_f32_16x16x32_bf16 v[124:127], a[200:203], v[152:155], v[124:127]\n"
"v_cvt_pk_bf16_f32 v220, v116, v117\n"
"v_cvt_pk_bf16_f32 v221, v118, v119\n"
"v_mfma_f32_16x16x32_bf16 v[140:143], a[200:203], v[184:187], v[140:143]\n"
"v_exp_f32 v132, v132\n"
"v_mfma_f32_16x16x32_bf16 v[156:159], a[200:203], v[216:219], v[156:159]\n"
"s_waitcnt vmcnt(12)\n"
"s_barrier\n"
"v_exp_f32 v133, v133\n"
"v_mfma_f32_16x16x32_bf16 a[160:163], v[60:63], v[180:183], a[160:163]\n"
"ds_read_b64_tr_b16 a[224:225], v37 offset:33280\n"
"v_exp_f32 v134, v134\n"
"v_mfma_f32_16x16x32_bf16 a[164:167], v[60:63], v[188:191], a[164:167]\n"
"ds_read_b64_tr_b16 a[226:227], v37 offset:37376\n"
"v_exp_f32 v135, v135\n"
"v_mfma_f32_16x16x32_bf16 a[168:171], v[60:63], v[196:199], a[168:171]\n"
"v_cvt_pk_bf16_f32 v228, v132, v133\n"
"v_cvt_pk_bf16_f32 v229, v134, v135\n"
"v_mfma_f32_16x16x32_bf16 a[172:175], v[60:63], v[204:207], a[172:175]\n"
"v_exp_f32 v148, v148\n"
"v_mfma_f32_16x16x32_bf16 a[176:179], v[60:63], v[244:247], a[176:179]\n"
"s_waitcnt lgkmcnt(6)\n"
"v_exp_f32 v149, v149\n"
"v_mfma_f32_16x16x32_bf16 a[0:3], a[212:215], v[180:183], a[0:3]\n"
"ds_read_b64_tr_b16 a[204:205], v36 offset:33792\n"
"v_exp_f32 v150, v150\n"
"v_mfma_f32_16x16x32_bf16 a[32:35], a[212:215], v[188:191], a[32:35]\n"
"ds_read_b64_tr_b16 a[206:207], v36 offset:37888\n"
"v_exp_f32 v151, v151\n"
"v_mfma_f32_16x16x32_bf16 a[64:67], a[212:215], v[196:199], a[64:67]\n"
"v_cvt_pk_bf16_f32 v236, v148, v149\n"
"v_cvt_pk_bf16_f32 v237, v150, v151\n"
"v_mfma_f32_16x16x32_bf16 a[96:99], a[212:215], v[204:207], a[96:99]\n"
"v_exp_f32 v108, v108\n"
"v_mfma_f32_16x16x32_bf16 a[128:131], a[212:215], v[244:247], a[128:131]\n"
"s_waitcnt lgkmcnt(6)\n"
"v_exp_f32 v109, v109\n"
"v_mfma_f32_16x16x32_bf16 a[4:7], a[216:219], v[180:183], a[4:7]\n"
"ds_read_b64_tr_b16 a[208:209], v37 offset:33792\n"
"v_exp_f32 v110, v110\n"
"v_mfma_f32_16x16x32_bf16 a[36:39], a[216:219], v[188:191], a[36:39]\n"
"ds_read_b64_tr_b16 a[210:211], v37 offset:37888\n"
"v_exp_f32 v111, v111\n"
"v_mfma_f32_16x16x32_bf16 a[68:71], a[216:219], v[196:199], a[68:71]\n"
"v_cvt_pk_bf16_f32 v214, v108, v109\n"
"v_cvt_pk_bf16_f32 v215, v110, v111\n"
"v_mfma_f32_16x16x32_bf16 a[100:103], a[216:219], v[204:207], a[100:103]\n"
"v_exp_f32 v124, v124\n"
"v_mfma_f32_16x16x32_bf16 a[132:135], a[216:219], v[244:247], a[132:135]\n"
"s_waitcnt lgkmcnt(6)\n"
"v_exp_f32 v125, v125\n"
"v_mfma_f32_16x16x32_bf16 a[8:11], a[220:223], v[180:183], a[8:11]\n"
"ds_read_b64_tr_b16 a[212:213], v36 offset:34304\n"
"v_exp_f32 v126, v126\n"
"v_mfma_f32_16x16x32_bf16 a[40:43], a[220:223], v[188:191], a[40:43]\n"
"ds_read_b64_tr_b16 a[214:215], v36 offset:38400\n"
"v_exp_f32 v127, v127\n"
"v_mfma_f32_16x16x32_bf16 a[72:75], a[220:223], v[196:199], a[72:75]\n"
"v_cvt_pk_bf16_f32 v222, v124, v125\n"
"v_cvt_pk_bf16_f32 v223, v126, v127\n"
"v_mfma_f32_16x16x32_bf16 a[104:107], a[220:223], v[204:207], a[104:107]\n"
"v_exp_f32 v140, v140\n"
"v_mfma_f32_16x16x32_bf16 a[136:139], a[220:223], v[244:247], a[136:139]\n"
"s_waitcnt lgkmcnt(6)\n"
"v_exp_f32 v141, v141\n"
"v_mfma_f32_16x16x32_bf16 a[12:15], a[224:227], v[180:183], a[12:15]\n"
"ds_read_b64_tr_b16 a[216:217], v37 offset:34304\n"
"v_exp_f32 v142, v142\n"
"v_mfma_f32_16x16x32_bf16 a[44:47], a[224:227], v[188:191], a[44:47]\n"
"ds_read_b64_tr_b16 a[218:219], v37 offset:38400\n"
"v_exp_f32 v143, v143\n"
"v_mfma_f32_16x16x32_bf16 a[76:79], a[224:227], v[196:199], a[76:79]\n"
"v_cvt_pk_bf16_f32 v230, v140, v141\n"
"v_cvt_pk_bf16_f32 v231, v142, v143\n"
"v_mfma_f32_16x16x32_bf16 a[108:111], a[224:227], v[204:207], a[108:111]\n"
"v_exp_f32 v156, v156\n"
"v_mfma_f32_16x16x32_bf16 a[140:143], a[224:227], v[244:247], a[140:143]\n"
"s_waitcnt lgkmcnt(6)\n"
"v_exp_f32 v157, v157\n"
"v_mfma_f32_16x16x32_bf16 a[16:19], a[204:207], v[180:183], a[16:19]\n"
"ds_read_b128 a[180:183], v28 offset:49152\n"
"v_exp_f32 v158, v158\n"
"v_mfma_f32_16x16x32_bf16 a[48:51], a[204:207], v[188:191], a[48:51]\n"
"v_exp_f32 v159, v159\n"
"v_mfma_f32_16x16x32_bf16 a[80:83], a[204:207], v[196:199], a[80:83]\n"
"v_cvt_pk_bf16_f32 v238, v156, v157\n"
"v_cvt_pk_bf16_f32 v239, v158, v159\n"
"v_mfma_f32_16x16x32_bf16 a[112:115], a[204:207], v[204:207], a[112:115]\n"
"v_mfma_f32_16x16x32_bf16 a[144:147], a[204:207], v[244:247], a[144:147]\n"
"s_waitcnt lgkmcnt(5)\n"
"v_mfma_f32_16x16x32_bf16 a[20:23], a[208:211], v[180:183], a[20:23]\n"
"ds_read_b128 a[184:187], v29 offset:49152\n"
"v_mfma_f32_16x16x32_bf16 a[52:55], a[208:211], v[188:191], a[52:55]\n"
"v_mfma_f32_16x16x32_bf16 a[84:87], a[208:211], v[196:199], a[84:87]\n"
"v_mfma_f32_16x16x32_bf16 a[116:119], a[208:211], v[204:207], a[116:119]\n"
"v_mfma_f32_16x16x32_bf16 a[148:151], a[208:211], v[244:247], a[148:151]\n"
"s_waitcnt lgkmcnt(4)\n"
"v_mfma_f32_16x16x32_bf16 a[24:27], a[212:215], v[180:183], a[24:27]\n"
"ds_read_b128 a[188:191], v30 offset:49152\n"
"v_mfma_f32_16x16x32_bf16 a[56:59], a[212:215], v[188:191], a[56:59]\n"
"v_mfma_f32_16x16x32_bf16 a[88:91], a[212:215], v[196:199], a[88:91]\n"
"v_mfma_f32_16x16x32_bf16 a[120:123], a[212:215], v[204:207], a[120:123]\n"
"v_mfma_f32_16x16x32_bf16 a[152:155], a[212:215], v[244:247], a[152:155]\n"
"s_waitcnt lgkmcnt(3)\n"
"v_mfma_f32_16x16x32_bf16 a[28:31], a[216:219], v[180:183], a[28:31]\n"
"ds_read_b128 a[192:195], v31 offset:49152\n"
"v_mfma_f32_16x16x32_bf16 a[60:63], a[216:219], v[188:191], a[60:63]\n"
"v_mfma_f32_16x16x32_bf16 a[92:95], a[216:219], v[196:199], a[92:95]\n"
"v_mfma_f32_16x16x32_bf16 a[124:127], a[216:219], v[204:207], a[124:127]\n"
"v_mfma_f32_16x16x32_bf16 a[156:159], a[216:219], v[244:247], a[156:159]\n"
"s_waitcnt lgkmcnt(3)\n"
"v_mfma_f32_16x16x32_bf16 v[100:103], a[180:183], v[96:99], v[40:43]\n"
"ds_read_b128 a[196:199], v28 offset:53248\n"
"v_mfma_f32_16x16x32_bf16 v[116:119], a[180:183], v[128:131], v[44:47]\n"
"s_add_u32 m0, s61, 81920\n"
"s_mov_b32 s66, 81920\n"
"v_mfma_f32_16x16x32_bf16 v[132:135], a[180:183], v[160:163], v[48:51]\n"
"buffer_load_dwordx4 v95, s[52:55], s66 offen nt lds\n"
"v_mfma_f32_16x16x32_bf16 v[148:151], a[180:183], v[192:195], v[52:55]\n"
"v_mfma_f32_16x16x32_bf16 v[164:167], a[180:183], v[224:227], v[56:59]\n"
"s_waitcnt lgkmcnt(3)\n"
"v_mfma_f32_16x16x32_bf16 v[100:103], a[184:187], v[104:107], v[100:103]\n"
"ds_read_b128 a[200:203], v29 offset:53248\n"
"v_mfma_f32_16x16x32_bf16 v[116:119], a[184:187], v[136:139], v[116:119]\n"
"buffer_load_dwordx4 v27, s[52:55], s66 offen offset:1024 nt lds\n"
"v_mfma_f32_16x16x32_bf16 v[132:135], a[184:187], v[168:171], v[132:135]\n"
"v_mfma_f32_16x16x32_bf16 v[148:151], a[184:187], v[200:203], v[148:151]\n"
"v_mfma_f32_16x16x32_bf16 v[164:167], a[184:187], v[232:235], v[164:167]\n"
"s_waitcnt lgkmcnt(3)\n"
"v_mfma_f32_16x16x32_bf16 v[100:103], a[188:191], v[112:115], v[100:103]\n"
"ds_read_b128 a[180:183], v30 offset:53248\n"
"v_mfma_f32_16x16x32_bf16 v[116:119], a[188:191], v[144:147], v[116:119]\n"
"buffer_load_dwordx4 v95, s[52:55], s66 offen offset:2048 nt lds\n"
"v_mfma_f32_16x16x32_bf16 v[132:135], a[188:191], v[176:179], v[132:135]\n"
"v_mfma_f32_16x16x32_bf16 v[148:151], a[188:191], v[208:211], v[148:151]\n"
"v_mfma_f32_16x16x32_bf16 v[164:167], a[188:191], v[240:243], v[164:167]\n"
"s_waitcnt lgkmcnt(3)\n"
"v_mfma_f32_16x16x32_bf16 v[100:103], a[192:195], v[120:123], v[100:103]\n"
"ds_read_b128 a[184:187], v31 offset:53248\n"
"v_mfma_f32_16x16x32_bf16 v[116:119], a[192:195], v[152:155], v[116:119]\n"
"buffer_load_dwordx4 v27, s[52:55], s66 offen offset:3072 nt lds\n"
"v_mfma_f32_16x16x32_bf16 v[132:135], a[192:195], v[184:187], v[132:135]\n"
"v_mfma_f32_16x16x32_bf16 v[148:151], a[192:195], v[216:219], v[148:151]\n"
"v_mfma_f32_16x16x32_bf16 v[164:167], a[192:195], v[248:251], v[164:167]\n"
"s_waitcnt lgkmcnt(3)\n"
"v_mfma_f32_16x16x32_bf16 v[108:111], a[196:199], v[96:99], v[40:43]\n"
"v_mfma_f32_16x16x32_bf16 v[124:127], a[196:199], v[128:131], v[44:47]\n"
"v_mfma_f32_16x16x32_bf16 v[140:143], a[196:199], v[160:163], v[48:51]\n"
"v_mfma_f32_16x16x32_bf16 v[156:159], a[196:199], v[192:195], v[52:55]\n"
"v_exp_f32 v100, v100\n"
"v_mfma_f32_16x16x32_bf16 v[172:175], a[196:199], v[224:227], v[56:59]\n"
"s_waitcnt lgkmcnt(2)\n"
"v_exp_f32 v101, v101\n"
"v_mfma_f32_16x16x32_bf16 v[108:111], a[200:203], v[104:107], v[108:111]\n"
"v_exp_f32 v102, v102\n"
"v_mfma_f32_16x16x32_bf16 v[124:127], a[200:203], v[136:139], v[124:127]\n"
"v_exp_f32 v103, v103\n"
"v_mfma_f32_16x16x32_bf16 v[140:143], a[200:203], v[168:171], v[140:143]\n"
"ds_read_b64_tr_b16 a[222:223], v38 offset:36864\n"
"v_cvt_pk_bf16_f32 v180, v100, v101\n"
"v_cvt_pk_bf16_f32 v181, v102, v103\n"
"v_mfma_f32_16x16x32_bf16 v[156:159], a[200:203], v[200:203], v[156:159]\n"
"ds_read_b64_tr_b16 a[220:221], v38 offset:32768\n"
"v_exp_f32 v116, v116\n"
"v_mfma_f32_16x16x32_bf16 v[172:175], a[200:203], v[232:235], v[172:175]\n"
"s_waitcnt lgkmcnt(3)\n"
"v_exp_f32 v117, v117\n"
"v_mfma_f32_16x16x32_bf16 v[108:111], a[180:183], v[112:115], v[108:111]\n"
"v_exp_f32 v118, v118\n"
"v_mfma_f32_16x16x32_bf16 v[124:127], a[180:183], v[144:147], v[124:127]\n"
"v_exp_f32 v119, v119\n"
"v_mfma_f32_16x16x32_bf16 v[140:143], a[180:183], v[176:179], v[140:143]\n"
"ds_read_b64_tr_b16 a[224:225], v39 offset:32768\n"
"v_cvt_pk_bf16_f32 v188, v116, v117\n"
"v_cvt_pk_bf16_f32 v189, v118, v119\n"
"v_mfma_f32_16x16x32_bf16 v[156:159], a[180:183], v[208:211], v[156:159]\n"
"ds_read_b64_tr_b16 a[226:227], v39 offset:36864\n"
"v_exp_f32 v132, v132\n"
"v_mfma_f32_16x16x32_bf16 v[172:175], a[180:183], v[240:243], v[172:175]\n"
"s_waitcnt lgkmcnt(4)\n"
"v_exp_f32 v133, v133\n"
"v_mfma_f32_16x16x32_bf16 v[108:111], a[184:187], v[120:123], v[108:111]\n"
"v_exp_f32 v134, v134\n"
"v_mfma_f32_16x16x32_bf16 v[124:127], a[184:187], v[152:155], v[124:127]\n"
"ds_read_b64_tr_b16 a[204:205], v38 offset:33280\n"
"v_exp_f32 v135, v135\n"
"v_mfma_f32_16x16x32_bf16 v[140:143], a[184:187], v[184:187], v[140:143]\n"
"ds_read_b64_tr_b16 a[206:207], v38 offset:37376\n"
"v_cvt_pk_bf16_f32 v196, v132, v133\n"
"v_cvt_pk_bf16_f32 v197, v134, v135\n"
"v_mfma_f32_16x16x32_bf16 v[156:159], a[184:187], v[216:219], v[156:159]\n"
"v_exp_f32 v148, v148\n"
"v_mfma_f32_16x16x32_bf16 v[172:175], a[184:187], v[248:251], v[172:175]\n"
"v_exp_f32 v149, v149\n"
"v_mfma_f32_16x16x32_bf16 a[160:163], v[60:63], v[212:215], a[160:163]\n"
"ds_read_b64_tr_b16 a[208:209], v39 offset:33280\n"
"v_exp_f32 v150, v150\n"
"v_mfma_f32_16x16x32_bf16 a[164:167], v[60:63], v[220:223], a[164:167]\n"
"ds_read_b64_tr_b16 a[210:211], v39 offset:37376\n"
"v_exp_f32 v151, v151\n"
"v_mfma_f32_16x16x32_bf16 a[168:171], v[60:63], v[228:231], a[168:171]\n"
"v_cvt_pk_bf16_f32 v204, v148, v149\n"
"v_cvt_pk_bf16_f32 v205, v150, v151\n"
"v_mfma_f32_16x16x32_bf16 a[172:175], v[60:63], v[236:239], a[172:175]\n"
"s_waitcnt lgkmcnt(6)\n"
"v_exp_f32 v164, v164\n"
"v_mfma_f32_16x16x32_bf16 a[0:3], a[220:223], v[212:215], a[0:3]\n"
"ds_read_b64_tr_b16 a[212:213], v38 offset:33792\n"
"v_exp_f32 v165, v165\n"
"v_mfma_f32_16x16x32_bf16 a[32:35], a[220:223], v[220:223], a[32:35]\n"
"ds_read_b64_tr_b16 a[214:215], v38 offset:37888\n"
"v_exp_f32 v166, v166\n"
"v_mfma_f32_16x16x32_bf16 a[64:67], a[220:223], v[228:231], a[64:67]\n"
"v_exp_f32 v167, v167\n"
"v_mfma_f32_16x16x32_bf16 a[96:99], a[220:223], v[236:239], a[96:99]\n"
"s_waitcnt lgkmcnt(6)\n"
"v_cvt_pk_bf16_f32 v244, v164, v165\n"
"v_cvt_pk_bf16_f32 v245, v166, v167\n"
"v_mfma_f32_16x16x32_bf16 a[4:7], a[224:227], v[212:215], a[4:7]\n"
"ds_read_b64_tr_b16 a[216:217], v39 offset:33792\n"
"v_exp_f32 v108, v108\n"
"v_mfma_f32_16x16x32_bf16 a[36:39], a[224:227], v[220:223], a[36:39]\n"
"ds_read_b64_tr_b16 a[218:219], v39 offset:37888\n"
"v_exp_f32 v109, v109\n"
"v_mfma_f32_16x16x32_bf16 a[68:71], a[224:227], v[228:231], a[68:71]\n"
"v_exp_f32 v110, v110\n"
"v_mfma_f32_16x16x32_bf16 a[100:103], a[224:227], v[236:239], a[100:103]\n"
"s_waitcnt lgkmcnt(6)\n"
"v_exp_f32 v111, v111\n"
"v_mfma_f32_16x16x32_bf16 a[8:11], a[204:207], v[212:215], a[8:11]\n"
"ds_read_b64_tr_b16 a[220:221], v38 offset:34304\n"
"v_cvt_pk_bf16_f32 v182, v108, v109\n"
"v_cvt_pk_bf16_f32 v183, v110, v111\n"
"v_mfma_f32_16x16x32_bf16 a[40:43], a[204:207], v[220:223], a[40:43]\n"
"ds_read_b64_tr_b16 a[222:223], v38 offset:38400\n"
"v_exp_f32 v124, v124\n"
"v_mfma_f32_16x16x32_bf16 a[72:75], a[204:207], v[228:231], a[72:75]\n"
"v_exp_f32 v125, v125\n"
"v_mfma_f32_16x16x32_bf16 a[104:107], a[204:207], v[236:239], a[104:107]\n"
"s_waitcnt lgkmcnt(6)\n"
"v_exp_f32 v126, v126\n"
"v_mfma_f32_16x16x32_bf16 a[12:15], a[208:211], v[212:215], a[12:15]\n"
"ds_read_b64_tr_b16 a[224:225], v39 offset:34304\n"
"v_exp_f32 v127, v127\n"
"v_mfma_f32_16x16x32_bf16 a[44:47], a[208:211], v[220:223], a[44:47]\n"
"ds_read_b64_tr_b16 a[226:227], v39 offset:38400\n"
"v_cvt_pk_bf16_f32 v190, v124, v125\n"
"v_cvt_pk_bf16_f32 v191, v126, v127\n"
"v_mfma_f32_16x16x32_bf16 a[76:79], a[208:211], v[228:231], a[76:79]\n"
"v_exp_f32 v140, v140\n"
"v_mfma_f32_16x16x32_bf16 a[108:111], a[208:211], v[236:239], a[108:111]\n"
"s_waitcnt lgkmcnt(6)\n"
"v_exp_f32 v141, v141\n"
"v_mfma_f32_16x16x32_bf16 a[16:19], a[212:215], v[212:215], a[16:19]\n"
"ds_read_b128 a[188:191], v32 offset:49152\n"
"v_exp_f32 v142, v142\n"
"v_mfma_f32_16x16x32_bf16 a[48:51], a[212:215], v[220:223], a[48:51]\n"
"v_exp_f32 v143, v143\n"
"v_mfma_f32_16x16x32_bf16 a[80:83], a[212:215], v[228:231], a[80:83]\n"
"v_cvt_pk_bf16_f32 v198, v140, v141\n"
"v_cvt_pk_bf16_f32 v199, v142, v143\n"
"v_mfma_f32_16x16x32_bf16 a[112:115], a[212:215], v[236:239], a[112:115]\n"
"s_waitcnt lgkmcnt(5)\n"
"v_exp_f32 v156, v156\n"
"v_mfma_f32_16x16x32_bf16 a[20:23], a[216:219], v[212:215], a[20:23]\n"
"ds_read_b128 a[192:195], v33 offset:49152\n"
"v_exp_f32 v157, v157\n"
"v_mfma_f32_16x16x32_bf16 a[52:55], a[216:219], v[220:223], a[52:55]\n"
"v_exp_f32 v158, v158\n"
"v_mfma_f32_16x16x32_bf16 a[84:87], a[216:219], v[228:231], a[84:87]\n"
"v_exp_f32 v159, v159\n"
"v_mfma_f32_16x16x32_bf16 a[116:119], a[216:219], v[236:239], a[116:119]\n"
"s_waitcnt lgkmcnt(4)\n"
"v_cvt_pk_bf16_f32 v206, v156, v157\n"
"v_cvt_pk_bf16_f32 v207, v158, v159\n"
"v_mfma_f32_16x16x32_bf16 a[24:27], a[220:223], v[212:215], a[24:27]\n"
"ds_read_b128 a[196:199], v34 offset:49152\n"
"v_exp_f32 v172, v172\n"
"v_mfma_f32_16x16x32_bf16 a[56:59], a[220:223], v[220:223], a[56:59]\n"
"v_exp_f32 v173, v173\n"
"v_mfma_f32_16x16x32_bf16 a[88:91], a[220:223], v[228:231], a[88:91]\n"
"v_exp_f32 v174, v174\n"
"v_mfma_f32_16x16x32_bf16 a[120:123], a[220:223], v[236:239], a[120:123]\n"
"s_waitcnt lgkmcnt(3)\n"
"v_exp_f32 v175, v175\n"
"v_mfma_f32_16x16x32_bf16 a[28:31], a[224:227], v[212:215], a[28:31]\n"
"ds_read_b128 a[200:203], v35 offset:49152\n"
"v_cvt_pk_bf16_f32 v246, v172, v173\n"
"v_cvt_pk_bf16_f32 v247, v174, v175\n"
"v_mfma_f32_16x16x32_bf16 a[60:63], a[224:227], v[220:223], a[60:63]\n"
"v_mfma_f32_16x16x32_bf16 a[92:95], a[224:227], v[228:231], a[92:95]\n"
"v_mfma_f32_16x16x32_bf16 a[124:127], a[224:227], v[236:239], a[124:127]\n"
"s_waitcnt lgkmcnt(3)\n"
"v_mfma_f32_16x16x32_bf16 v[100:103], a[188:191], v[96:99], v[40:43]\n"
"ds_read_b128 a[180:183], v32 offset:53248\n"
"v_mfma_f32_16x16x32_bf16 v[116:119], a[188:191], v[128:131], v[44:47]\n"
"s_add_u32 m0, s61, 32768\n"
"s_mov_b32 s65, 98304\n"
"v_mfma_f32_16x16x32_bf16 v[132:135], a[188:191], v[160:163], v[48:51]\n"
"buffer_load_dwordx4 v24, s[48:51], s65 offen nt lds\n"
"v_mfma_f32_16x16x32_bf16 v[148:151], a[188:191], v[192:195], v[52:55]\n"
"s_waitcnt lgkmcnt(3)\n"
"v_mfma_f32_16x16x32_bf16 v[100:103], a[192:195], v[104:107], v[100:103]\n"
"ds_read_b128 a[184:187], v33 offset:53248\n"
"v_mfma_f32_16x16x32_bf16 v[116:119], a[192:195], v[136:139], v[116:119]\n"
"v_mfma_f32_16x16x32_bf16 v[132:135], a[192:195], v[168:171], v[132:135]\n"
"buffer_load_dwordx4 v25, s[48:51], s65 offen offset:1024 nt lds\n"
"v_mfma_f32_16x16x32_bf16 v[148:151], a[192:195], v[200:203], v[148:151]\n"
"s_waitcnt lgkmcnt(3)\n"
"v_mfma_f32_16x16x32_bf16 v[100:103], a[196:199], v[112:115], v[100:103]\n"
"ds_read_b128 a[188:191], v34 offset:53248\n"
"v_mfma_f32_16x16x32_bf16 v[116:119], a[196:199], v[144:147], v[116:119]\n"
"v_mfma_f32_16x16x32_bf16 v[132:135], a[196:199], v[176:179], v[132:135]\n"
"buffer_load_dwordx4 v93, s[48:51], s65 offen offset:2048 nt lds\n"
"v_mfma_f32_16x16x32_bf16 v[148:151], a[196:199], v[208:211], v[148:151]\n"
"s_waitcnt lgkmcnt(3)\n"
"v_mfma_f32_16x16x32_bf16 v[100:103], a[200:203], v[120:123], v[100:103]\n"
"ds_read_b128 a[192:195], v35 offset:53248\n"
"v_mfma_f32_16x16x32_bf16 v[116:119], a[200:203], v[152:155], v[116:119]\n"
"v_mfma_f32_16x16x32_bf16 v[132:135], a[200:203], v[184:187], v[132:135]\n"
"buffer_load_dwordx4 v94, s[48:51], s65 offen offset:3072 nt lds\n"
"v_mfma_f32_16x16x32_bf16 v[148:151], a[200:203], v[216:219], v[148:151]\n"
"s_waitcnt lgkmcnt(3)\n"
"v_mfma_f32_16x16x32_bf16 v[108:111], a[180:183], v[96:99], v[40:43]\n"
"v_mfma_f32_16x16x32_bf16 v[124:127], a[180:183], v[128:131], v[44:47]\n"
"ds_read_b64_tr_b16 a[204:205], v36 offset:49152\n"
"v_mfma_f32_16x16x32_bf16 v[140:143], a[180:183], v[160:163], v[48:51]\n"
"ds_read_b64_tr_b16 a[206:207], v36 offset:53248\n"
"v_mfma_f32_16x16x32_bf16 v[156:159], a[180:183], v[192:195], v[52:55]\n"
"s_waitcnt lgkmcnt(4)\n"
"v_mfma_f32_16x16x32_bf16 v[108:111], a[184:187], v[104:107], v[108:111]\n"
"v_exp_f32 v100, v100\n"
"v_mfma_f32_16x16x32_bf16 v[124:127], a[184:187], v[136:139], v[124:127]\n"
"ds_read_b64_tr_b16 a[210:211], v37 offset:53248\n"
"v_exp_f32 v101, v101\n"
"v_mfma_f32_16x16x32_bf16 v[140:143], a[184:187], v[168:171], v[140:143]\n"
"ds_read_b64_tr_b16 a[208:209], v37 offset:49152\n"
"v_exp_f32 v102, v102\n"
"v_mfma_f32_16x16x32_bf16 v[156:159], a[184:187], v[200:203], v[156:159]\n"
"s_waitcnt lgkmcnt(5)\n"
"v_exp_f32 v103, v103\n"
"v_mfma_f32_16x16x32_bf16 v[108:111], a[188:191], v[112:115], v[108:111]\n"
"v_cvt_pk_bf16_f32 v212, v100, v101\n"
"v_cvt_pk_bf16_f32 v213, v102, v103\n"
"v_mfma_f32_16x16x32_bf16 v[124:127], a[188:191], v[144:147], v[124:127]\n"
"v_exp_f32 v116, v116\n"
"v_mfma_f32_16x16x32_bf16 v[140:143], a[188:191], v[176:179], v[140:143]\n"
"ds_read_b64_tr_b16 a[212:213], v36 offset:49664\n"
"v_exp_f32 v117, v117\n"
"v_mfma_f32_16x16x32_bf16 v[156:159], a[188:191], v[208:211], v[156:159]\n"
"s_waitcnt lgkmcnt(5)\n"
"v_exp_f32 v118, v118\n"
"v_mfma_f32_16x16x32_bf16 v[108:111], a[192:195], v[120:123], v[108:111]\n"
"ds_read_b64_tr_b16 a[214:215], v36 offset:53760\n"
"v_exp_f32 v119, v119\n"
"v_mfma_f32_16x16x32_bf16 v[124:127], a[192:195], v[152:155], v[124:127]\n"
"v_cvt_pk_bf16_f32 v220, v116, v117\n"
"v_cvt_pk_bf16_f32 v221, v118, v119\n"
"v_mfma_f32_16x16x32_bf16 v[140:143], a[192:195], v[184:187], v[140:143]\n"
"v_exp_f32 v132, v132\n"
"v_mfma_f32_16x16x32_bf16 v[156:159], a[192:195], v[216:219], v[156:159]\n"
"s_waitcnt vmcnt(12)\n"
"s_barrier\n"
"v_exp_f32 v133, v133\n"
"v_mfma_f32_16x16x32_bf16 a[160:163], v[60:63], v[180:183], a[160:163]\n"
"ds_read_b64_tr_b16 a[216:217], v37 offset:49664\n"
"v_exp_f32 v134, v134\n"
"v_mfma_f32_16x16x32_bf16 a[164:167], v[60:63], v[188:191], a[164:167]\n"
"ds_read_b64_tr_b16 a[218:219], v37 offset:53760\n"
"v_exp_f32 v135, v135\n"
"v_mfma_f32_16x16x32_bf16 a[168:171], v[60:63], v[196:199], a[168:171]\n"
"v_cvt_pk_bf16_f32 v228, v132, v133\n"
"v_cvt_pk_bf16_f32 v229, v134, v135\n"
"v_mfma_f32_16x16x32_bf16 a[172:175], v[60:63], v[204:207], a[172:175]\n"
"v_exp_f32 v148, v148\n"
"v_mfma_f32_16x16x32_bf16 a[176:179], v[60:63], v[244:247], a[176:179]\n"
"s_waitcnt lgkmcnt(6)\n"
"v_exp_f32 v149, v149\n"
"v_mfma_f32_16x16x32_bf16 a[0:3], a[204:207], v[180:183], a[0:3]\n"
"ds_read_b64_tr_b16 a[220:221], v36 offset:50176\n"
"v_exp_f32 v150, v150\n"
"v_mfma_f32_16x16x32_bf16 a[32:35], a[204:207], v[188:191], a[32:35]\n"
"ds_read_b64_tr_b16 a[222:223], v36 offset:54272\n"
"v_exp_f32 v151, v151\n"
"v_mfma_f32_16x16x32_bf16 a[64:67], a[204:207], v[196:199], a[64:67]\n"
"v_cvt_pk_bf16_f32 v236, v148, v149\n"
"v_cvt_pk_bf16_f32 v237, v150, v151\n"
"v_mfma_f32_16x16x32_bf16 a[96:99], a[204:207], v[204:207], a[96:99]\n"
"v_exp_f32 v108, v108\n"
"v_mfma_f32_16x16x32_bf16 a[128:131], a[204:207], v[244:247], a[128:131]\n"
"s_waitcnt lgkmcnt(6)\n"
"v_exp_f32 v109, v109\n"
"v_mfma_f32_16x16x32_bf16 a[4:7], a[208:211], v[180:183], a[4:7]\n"
"ds_read_b64_tr_b16 a[224:225], v37 offset:50176\n"
"v_exp_f32 v110, v110\n"
"v_mfma_f32_16x16x32_bf16 a[36:39], a[208:211], v[188:191], a[36:39]\n"
"ds_read_b64_tr_b16 a[226:227], v37 offset:54272\n"
"v_exp_f32 v111, v111\n"
"v_mfma_f32_16x16x32_bf16 a[68:71], a[208:211], v[196:199], a[68:71]\n"
"v_cvt_pk_bf16_f32 v214, v108, v109\n"
"v_cvt_pk_bf16_f32 v215, v110, v111\n"
"v_mfma_f32_16x16x32_bf16 a[100:103], a[208:211], v[204:207], a[100:103]\n"
"v_exp_f32 v124, v124\n"
"v_mfma_f32_16x16x32_bf16 a[132:135], a[208:211], v[244:247], a[132:135]\n"
"s_waitcnt lgkmcnt(6)\n"
"v_exp_f32 v125, v125\n"
"v_mfma_f32_16x16x32_bf16 a[8:11], a[212:215], v[180:183], a[8:11]\n"
"ds_read_b64_tr_b16 a[204:205], v36 offset:50688\n"
"v_exp_f32 v126, v126\n"
"v_mfma_f32_16x16x32_bf16 a[40:43], a[212:215], v[188:191], a[40:43]\n"
"ds_read_b64_tr_b16 a[206:207], v36 offset:54784\n"
"v_exp_f32 v127, v127\n"
"v_mfma_f32_16x16x32_bf16 a[72:75], a[212:215], v[196:199], a[72:75]\n"
"v_cvt_pk_bf16_f32 v222, v124, v125\n"
"v_cvt_pk_bf16_f32 v223, v126, v127\n"
"v_mfma_f32_16x16x32_bf16 a[104:107], a[212:215], v[204:207], a[104:107]\n"
"v_exp_f32 v140, v140\n"
"v_mfma_f32_16x16x32_bf16 a[136:139], a[212:215], v[244:247], a[136:139]\n"
"s_waitcnt lgkmcnt(6)\n"
"v_exp_f32 v141, v141\n"
"v_mfma_f32_16x16x32_bf16 a[12:15], a[216:219], v[180:183], a[12:15]\n"
"ds_read_b64_tr_b16 a[208:209], v37 offset:50688\n"
"v_exp_f32 v142, v142\n"
"v_mfma_f32_16x16x32_bf16 a[44:47], a[216:219], v[188:191], a[44:47]\n"
"ds_read_b64_tr_b16 a[210:211], v37 offset:54784\n"
"v_exp_f32 v143, v143\n"
"v_mfma_f32_16x16x32_bf16 a[76:79], a[216:219], v[196:199], a[76:79]\n"
"v_cvt_pk_bf16_f32 v230, v140, v141\n"
"v_cvt_pk_bf16_f32 v231, v142, v143\n"
"v_mfma_f32_16x16x32_bf16 a[108:111], a[216:219], v[204:207], a[108:111]\n"
"v_exp_f32 v156, v156\n"
"v_mfma_f32_16x16x32_bf16 a[140:143], a[216:219], v[244:247], a[140:143]\n"
"s_waitcnt lgkmcnt(6)\n"
"v_exp_f32 v157, v157\n"
"v_mfma_f32_16x16x32_bf16 a[16:19], a[220:223], v[180:183], a[16:19]\n"
"ds_read_b128 a[196:199], v28 offset:0\n"
"v_exp_f32 v158, v158\n"
"v_mfma_f32_16x16x32_bf16 a[48:51], a[220:223], v[188:191], a[48:51]\n"
"v_exp_f32 v159, v159\n"
"v_mfma_f32_16x16x32_bf16 a[80:83], a[220:223], v[196:199], a[80:83]\n"
"v_cvt_pk_bf16_f32 v238, v156, v157\n"
"v_cvt_pk_bf16_f32 v239, v158, v159\n"
"v_mfma_f32_16x16x32_bf16 a[112:115], a[220:223], v[204:207], a[112:115]\n"
"v_mfma_f32_16x16x32_bf16 a[144:147], a[220:223], v[244:247], a[144:147]\n"
"s_waitcnt lgkmcnt(5)\n"
"v_mfma_f32_16x16x32_bf16 a[20:23], a[224:227], v[180:183], a[20:23]\n"
"ds_read_b128 a[200:203], v29 offset:0\n"
"v_mfma_f32_16x16x32_bf16 a[52:55], a[224:227], v[188:191], a[52:55]\n"
"v_mfma_f32_16x16x32_bf16 a[84:87], a[224:227], v[196:199], a[84:87]\n"
"v_mfma_f32_16x16x32_bf16 a[116:119], a[224:227], v[204:207], a[116:119]\n"
"v_mfma_f32_16x16x32_bf16 a[148:151], a[224:227], v[244:247], a[148:151]\n"
"s_waitcnt lgkmcnt(4)\n"
"v_mfma_f32_16x16x32_bf16 a[24:27], a[204:207], v[180:183], a[24:27]\n"
"ds_read_b128 a[180:183], v30 offset:0\n"
"v_mfma_f32_16x16x32_bf16 a[56:59], a[204:207], v[188:191], a[56:59]\n"
"v_mfma_f32_16x16x32_bf16 a[88:91], a[204:207], v[196:199], a[88:91]\n"
"v_mfma_f32_16x16x32_bf16 a[120:123], a[204:207], v[204:207], a[120:123]\n"
"v_mfma_f32_16x16x32_bf16 a[152:155], a[204:207], v[244:247], a[152:155]\n"
"s_waitcnt lgkmcnt(3)\n"
"v_mfma_f32_16x16x32_bf16 a[28:31], a[208:211], v[180:183], a[28:31]\n"
"ds_read_b128 a[184:187], v31 offset:0\n"
"v_mfma_f32_16x16x32_bf16 a[60:63], a[208:211], v[188:191], a[60:63]\n"
"v_mfma_f32_16x16x32_bf16 a[92:95], a[208:211], v[196:199], a[92:95]\n"
"v_mfma_f32_16x16x32_bf16 a[124:127], a[208:211], v[204:207], a[124:127]\n"
"v_mfma_f32_16x16x32_bf16 a[156:159], a[208:211], v[244:247], a[156:159]\n"
"s_waitcnt lgkmcnt(3)\n"
"v_mfma_f32_16x16x32_bf16 v[100:103], a[196:199], v[96:99], v[40:43]\n"
"ds_read_b128 a[188:191], v28 offset:4096\n"
"v_mfma_f32_16x16x32_bf16 v[116:119], a[196:199], v[128:131], v[44:47]\n"
"s_add_u32 m0, s61, 98304\n"
"s_mov_b32 s66, 98304\n"
"v_mfma_f32_16x16x32_bf16 v[132:135], a[196:199], v[160:163], v[48:51]\n"
"buffer_load_dwordx4 v95, s[52:55], s66 offen nt lds\n"
"v_mfma_f32_16x16x32_bf16 v[148:151], a[196:199], v[192:195], v[52:55]\n"
"v_mfma_f32_16x16x32_bf16 v[164:167], a[196:199], v[224:227], v[56:59]\n"
"s_waitcnt lgkmcnt(3)\n"
"v_mfma_f32_16x16x32_bf16 v[100:103], a[200:203], v[104:107], v[100:103]\n"
"ds_read_b128 a[192:195], v29 offset:4096\n"
"v_mfma_f32_16x16x32_bf16 v[116:119], a[200:203], v[136:139], v[116:119]\n"
"buffer_load_dwordx4 v27, s[52:55], s66 offen offset:1024 nt lds\n"
"v_mfma_f32_16x16x32_bf16 v[132:135], a[200:203], v[168:171], v[132:135]\n"
"v_mfma_f32_16x16x32_bf16 v[148:151], a[200:203], v[200:203], v[148:151]\n"
"v_mfma_f32_16x16x32_bf16 v[164:167], a[200:203], v[232:235], v[164:167]\n"
"s_waitcnt lgkmcnt(3)\n"
"v_mfma_f32_16x16x32_bf16 v[100:103], a[180:183], v[112:115], v[100:103]\n"
"ds_read_b128 a[196:199], v30 offset:4096\n"
"v_mfma_f32_16x16x32_bf16 v[116:119], a[180:183], v[144:147], v[116:119]\n"
"buffer_load_dwordx4 v95, s[52:55], s66 offen offset:2048 nt lds\n"
"v_mfma_f32_16x16x32_bf16 v[132:135], a[180:183], v[176:179], v[132:135]\n"
"v_mfma_f32_16x16x32_bf16 v[148:151], a[180:183], v[208:211], v[148:151]\n"
"v_mfma_f32_16x16x32_bf16 v[164:167], a[180:183], v[240:243], v[164:167]\n"
"s_waitcnt lgkmcnt(3)\n"
"v_mfma_f32_16x16x32_bf16 v[100:103], a[184:187], v[120:123], v[100:103]\n"
"ds_read_b128 a[200:203], v31 offset:4096\n"
"v_mfma_f32_16x16x32_bf16 v[116:119], a[184:187], v[152:155], v[116:119]\n"
"buffer_load_dwordx4 v27, s[52:55], s66 offen offset:3072 nt lds\n"
"v_mfma_f32_16x16x32_bf16 v[132:135], a[184:187], v[184:187], v[132:135]\n"
"v_mfma_f32_16x16x32_bf16 v[148:151], a[184:187], v[216:219], v[148:151]\n"
"v_mfma_f32_16x16x32_bf16 v[164:167], a[184:187], v[248:251], v[164:167]\n"
"s_waitcnt lgkmcnt(3)\n"
"v_mfma_f32_16x16x32_bf16 v[108:111], a[188:191], v[96:99], v[40:43]\n"
"v_mfma_f32_16x16x32_bf16 v[124:127], a[188:191], v[128:131], v[44:47]\n"
"v_mfma_f32_16x16x32_bf16 v[140:143], a[188:191], v[160:163], v[48:51]\n"
"v_mfma_f32_16x16x32_bf16 v[156:159], a[188:191], v[192:195], v[52:55]\n"
"v_exp_f32 v100, v100\n"
"v_mfma_f32_16x16x32_bf16 v[172:175], a[188:191], v[224:227], v[56:59]\n"
"s_waitcnt lgkmcnt(2)\n"
"v_exp_f32 v101, v101\n"
"v_mfma_f32_16x16x32_bf16 v[108:111], a[192:195], v[104:107], v[108:111]\n"
"v_exp_f32 v102, v102\n"
"v_mfma_f32_16x16x32_bf16 v[124:127], a[192:195], v[136:139], v[124:127]\n"
"v_exp_f32 v103, v103\n"
"v_mfma_f32_16x16x32_bf16 v[140:143], a[192:195], v[168:171], v[140:143]\n"
"ds_read_b64_tr_b16 a[214:215], v38 offset:53248\n"
"v_cvt_pk_bf16_f32 v180, v100, v101\n"
"v_cvt_pk_bf16_f32 v181, v102, v103\n"
"v_mfma_f32_16x16x32_bf16 v[156:159], a[192:195], v[200:203], v[156:159]\n"
"ds_read_b64_tr_b16 a[212:213], v38 offset:49152\n"
"v_exp_f32 v116, v116\n"
"v_mfma_f32_16x16x32_bf16 v[172:175], a[192:195], v[232:235], v[172:175]\n"
"s_waitcnt lgkmcnt(3)\n"
"v_exp_f32 v117, v117\n"
"v_mfma_f32_16x16x32_bf16 v[108:111], a[196:199], v[112:115], v[108:111]\n"
"v_exp_f32 v118, v118\n"
"v_mfma_f32_16x16x32_bf16 v[124:127], a[196:199], v[144:147], v[124:127]\n"
"v_exp_f32 v119, v119\n"
"v_mfma_f32_16x16x32_bf16 v[140:143], a[196:199], v[176:179], v[140:143]\n"
"ds_read_b64_tr_b16 a[216:217], v39 offset:49152\n"
"v_cvt_pk_bf16_f32 v188, v116, v117\n"
"v_cvt_pk_bf16_f32 v189, v118, v119\n"
"v_mfma_f32_16x16x32_bf16 v[156:159], a[196:199], v[208:211], v[156:159]\n"
"ds_read_b64_tr_b16 a[218:219], v39 offset:53248\n"
"v_exp_f32 v132, v132\n"
"v_mfma_f32_16x16x32_bf16 v[172:175], a[196:199], v[240:243], v[172:175]\n"
"s_waitcnt lgkmcnt(4)\n"
"v_exp_f32 v133, v133\n"
"v_mfma_f32_16x16x32_bf16 v[108:111], a[200:203], v[120:123], v[108:111]\n"
"v_exp_f32 v134, v134\n"
"v_mfma_f32_16x16x32_bf16 v[124:127], a[200:203], v[152:155], v[124:127]\n"
"ds_read_b64_tr_b16 a[220:221], v38 offset:49664\n"
"v_exp_f32 v135, v135\n"
"v_mfma_f32_16x16x32_bf16 v[140:143], a[200:203], v[184:187], v[140:143]\n"
"ds_read_b64_tr_b16 a[222:223], v38 offset:53760\n"
"v_cvt_pk_bf16_f32 v196, v132, v133\n"
"v_cvt_pk_bf16_f32 v197, v134, v135\n"
"v_mfma_f32_16x16x32_bf16 v[156:159], a[200:203], v[216:219], v[156:159]\n"
"v_exp_f32 v148, v148\n"
"v_mfma_f32_16x16x32_bf16 v[172:175], a[200:203], v[248:251], v[172:175]\n"
"v_exp_f32 v149, v149\n"
"v_mfma_f32_16x16x32_bf16 a[160:163], v[60:63], v[212:215], a[160:163]\n"
"ds_read_b64_tr_b16 a[224:225], v39 offset:49664\n"
"v_exp_f32 v150, v150\n"
"v_mfma_f32_16x16x32_bf16 a[164:167], v[60:63], v[220:223], a[164:167]\n"
"ds_read_b64_tr_b16 a[226:227], v39 offset:53760\n"
"v_exp_f32 v151, v151\n"
"v_mfma_f32_16x16x32_bf16 a[168:171], v[60:63], v[228:231], a[168:171]\n"
"v_cvt_pk_bf16_f32 v204, v148, v149\n"
"v_cvt_pk_bf16_f32 v205, v150, v151\n"
"v_mfma_f32_16x16x32_bf16 a[172:175], v[60:63], v[236:239], a[172:175]\n"
"s_waitcnt lgkmcnt(6)\n"
"v_exp_f32 v164, v164\n"
"v_mfma_f32_16x16x32_bf16 a[0:3], a[212:215], v[212:215], a[0:3]\n"
"ds_read_b64_tr_b16 a[204:205], v38 offset:50176\n"
"v_exp_f32 v165, v165\n"
"v_mfma_f32_16x16x32_bf16 a[32:35], a[212:215], v[220:223], a[32:35]\n"
"ds_read_b64_tr_b16 a[206:207], v38 offset:54272\n"
"v_exp_f32 v166, v166\n"
"v_mfma_f32_16x16x32_bf16 a[64:67], a[212:215], v[228:231], a[64:67]\n"
"v_exp_f32 v167, v167\n"
"v_mfma_f32_16x16x32_bf16 a[96:99], a[212:215], v[236:239], a[96:99]\n"
"s_waitcnt lgkmcnt(6)\n"
"v_cvt_pk_bf16_f32 v244, v164, v165\n"
"v_cvt_pk_bf16_f32 v245, v166, v167\n"
"v_mfma_f32_16x16x32_bf16 a[4:7], a[216:219], v[212:215], a[4:7]\n"
"ds_read_b64_tr_b16 a[208:209], v39 offset:50176\n"
"v_exp_f32 v108, v108\n"
"v_mfma_f32_16x16x32_bf16 a[36:39], a[216:219], v[220:223], a[36:39]\n"
"ds_read_b64_tr_b16 a[210:211], v39 offset:54272\n"
"v_exp_f32 v109, v109\n"
"v_mfma_f32_16x16x32_bf16 a[68:71], a[216:219], v[228:231], a[68:71]\n"
"v_exp_f32 v110, v110\n"
"v_mfma_f32_16x16x32_bf16 a[100:103], a[216:219], v[236:239], a[100:103]\n"
"s_waitcnt lgkmcnt(6)\n"
"v_exp_f32 v111, v111\n"
"v_mfma_f32_16x16x32_bf16 a[8:11], a[220:223], v[212:215], a[8:11]\n"
"ds_read_b64_tr_b16 a[212:213], v38 offset:50688\n"
"v_cvt_pk_bf16_f32 v182, v108, v109\n"
"v_cvt_pk_bf16_f32 v183, v110, v111\n"
"v_mfma_f32_16x16x32_bf16 a[40:43], a[220:223], v[220:223], a[40:43]\n"
"ds_read_b64_tr_b16 a[214:215], v38 offset:54784\n"
"v_exp_f32 v124, v124\n"
"v_mfma_f32_16x16x32_bf16 a[72:75], a[220:223], v[228:231], a[72:75]\n"
"v_exp_f32 v125, v125\n"
"v_mfma_f32_16x16x32_bf16 a[104:107], a[220:223], v[236:239], a[104:107]\n"
"s_waitcnt lgkmcnt(6)\n"
"v_exp_f32 v126, v126\n"
"v_mfma_f32_16x16x32_bf16 a[12:15], a[224:227], v[212:215], a[12:15]\n"
"ds_read_b64_tr_b16 a[216:217], v39 offset:50688\n"
"v_exp_f32 v127, v127\n"
"v_mfma_f32_16x16x32_bf16 a[44:47], a[224:227], v[220:223], a[44:47]\n"
"ds_read_b64_tr_b16 a[218:219], v39 offset:54784\n"
"v_cvt_pk_bf16_f32 v190, v124, v125\n"
"v_cvt_pk_bf16_f32 v191, v126, v127\n"
"v_mfma_f32_16x16x32_bf16 a[76:79], a[224:227], v[228:231], a[76:79]\n"
"v_exp_f32 v140, v140\n"
"v_mfma_f32_16x16x32_bf16 a[108:111], a[224:227], v[236:239], a[108:111]\n"
"s_waitcnt lgkmcnt(6)\n"
"v_exp_f32 v141, v141\n"
"v_mfma_f32_16x16x32_bf16 a[16:19], a[204:207], v[212:215], a[16:19]\n"
"ds_read_b128 a[180:183], v32 offset:0\n"
"v_exp_f32 v142, v142\n"
"v_mfma_f32_16x16x32_bf16 a[48:51], a[204:207], v[220:223], a[48:51]\n"
"v_exp_f32 v143, v143\n"
"v_mfma_f32_16x16x32_bf16 a[80:83], a[204:207], v[228:231], a[80:83]\n"
"v_cvt_pk_bf16_f32 v198, v140, v141\n"
"v_cvt_pk_bf16_f32 v199, v142, v143\n"
"v_mfma_f32_16x16x32_bf16 a[112:115], a[204:207], v[236:239], a[112:115]\n"
"s_waitcnt lgkmcnt(5)\n"
"v_exp_f32 v156, v156\n"
"v_mfma_f32_16x16x32_bf16 a[20:23], a[208:211], v[212:215], a[20:23]\n"
"ds_read_b128 a[184:187], v33 offset:0\n"
"v_exp_f32 v157, v157\n"
"v_mfma_f32_16x16x32_bf16 a[52:55], a[208:211], v[220:223], a[52:55]\n"
"v_exp_f32 v158, v158\n"
"v_mfma_f32_16x16x32_bf16 a[84:87], a[208:211], v[228:231], a[84:87]\n"
"v_exp_f32 v159, v159\n"
"v_mfma_f32_16x16x32_bf16 a[116:119], a[208:211], v[236:239], a[116:119]\n"
"s_waitcnt lgkmcnt(4)\n"
"v_cvt_pk_bf16_f32 v206, v156, v157\n"
"v_cvt_pk_bf16_f32 v207, v158, v159\n"
"v_mfma_f32_16x16x32_bf16 a[24:27], a[212:215], v[212:215], a[24:27]\n"
"ds_read_b128 a[188:191], v34 offset:0\n"
"v_exp_f32 v172, v172\n"
"v_mfma_f32_16x16x32_bf16 a[56:59], a[212:215], v[220:223], a[56:59]\n"
"v_exp_f32 v173, v173\n"
"v_mfma_f32_16x16x32_bf16 a[88:91], a[212:215], v[228:231], a[88:91]\n"
"v_exp_f32 v174, v174\n"
"v_mfma_f32_16x16x32_bf16 a[120:123], a[212:215], v[236:239], a[120:123]\n"
"s_waitcnt lgkmcnt(3)\n"
"v_exp_f32 v175, v175\n"
"v_mfma_f32_16x16x32_bf16 a[28:31], a[216:219], v[212:215], a[28:31]\n"
"ds_read_b128 a[192:195], v35 offset:0\n"
"v_cvt_pk_bf16_f32 v246, v172, v173\n"
"v_cvt_pk_bf16_f32 v247, v174, v175\n"
"v_mfma_f32_16x16x32_bf16 a[60:63], a[216:219], v[220:223], a[60:63]\n"
"v_mfma_f32_16x16x32_bf16 a[92:95], a[216:219], v[228:231], a[92:95]\n"
"v_mfma_f32_16x16x32_bf16 a[124:127], a[216:219], v[236:239], a[124:127]\n"
"s_waitcnt lgkmcnt(3)\n"
"v_mfma_f32_16x16x32_bf16 v[100:103], a[180:183], v[96:99], v[40:43]\n"
"ds_read_b128 a[196:199], v32 offset:4096\n"
"v_mfma_f32_16x16x32_bf16 v[116:119], a[180:183], v[128:131], v[44:47]\n"
"s_add_u32 m0, s61, 49152\n"
"s_mov_b32 s65, 114688\n"
"v_mfma_f32_16x16x32_bf16 v[132:135], a[180:183], v[160:163], v[48:51]\n"
"buffer_load_dwordx4 v24, s[48:51], s65 offen nt lds\n"
"v_mfma_f32_16x16x32_bf16 v[148:151], a[180:183], v[192:195], v[52:55]\n"
"s_waitcnt lgkmcnt(3)\n"
"v_mfma_f32_16x16x32_bf16 v[100:103], a[184:187], v[104:107], v[100:103]\n"
"ds_read_b128 a[200:203], v33 offset:4096\n"
"v_mfma_f32_16x16x32_bf16 v[116:119], a[184:187], v[136:139], v[116:119]\n"
"v_mfma_f32_16x16x32_bf16 v[132:135], a[184:187], v[168:171], v[132:135]\n"
"buffer_load_dwordx4 v25, s[48:51], s65 offen offset:1024 nt lds\n"
"v_mfma_f32_16x16x32_bf16 v[148:151], a[184:187], v[200:203], v[148:151]\n"
"s_waitcnt lgkmcnt(3)\n"
"v_mfma_f32_16x16x32_bf16 v[100:103], a[188:191], v[112:115], v[100:103]\n"
"ds_read_b128 a[180:183], v34 offset:4096\n"
"v_mfma_f32_16x16x32_bf16 v[116:119], a[188:191], v[144:147], v[116:119]\n"
"v_mfma_f32_16x16x32_bf16 v[132:135], a[188:191], v[176:179], v[132:135]\n"
"buffer_load_dwordx4 v93, s[48:51], s65 offen offset:2048 nt lds\n"
"v_mfma_f32_16x16x32_bf16 v[148:151], a[188:191], v[208:211], v[148:151]\n"
"s_waitcnt lgkmcnt(3)\n"
"v_mfma_f32_16x16x32_bf16 v[100:103], a[192:195], v[120:123], v[100:103]\n"
"ds_read_b128 a[184:187], v35 offset:4096\n"
"v_mfma_f32_16x16x32_bf16 v[116:119], a[192:195], v[152:155], v[116:119]\n"
"v_mfma_f32_16x16x32_bf16 v[132:135], a[192:195], v[184:187], v[132:135]\n"
"buffer_load_dwordx4 v94, s[48:51], s65 offen offset:3072 nt lds\n"
"v_mfma_f32_16x16x32_bf16 v[148:151], a[192:195], v[216:219], v[148:151]\n"
"s_waitcnt lgkmcnt(3)\n"
"v_mfma_f32_16x16x32_bf16 v[108:111], a[196:199], v[96:99], v[40:43]\n"
"v_mfma_f32_16x16x32_bf16 v[124:127], a[196:199], v[128:131], v[44:47]\n"
"ds_read_b64_tr_b16 a[220:221], v36 offset:0\n"
"v_mfma_f32_16x16x32_bf16 v[140:143], a[196:199], v[160:163], v[48:51]\n"
"ds_read_b64_tr_b16 a[222:223], v36 offset:4096\n"
"v_mfma_f32_16x16x32_bf16 v[156:159], a[196:199], v[192:195], v[52:55]\n"
"s_waitcnt lgkmcnt(4)\n"
"v_mfma_f32_16x16x32_bf16 v[108:111], a[200:203], v[104:107], v[108:111]\n"
"v_exp_f32 v100, v100\n"
"v_mfma_f32_16x16x32_bf16 v[124:127], a[200:203], v[136:139], v[124:127]\n"
"ds_read_b64_tr_b16 a[226:227], v37 offset:4096\n"
"v_exp_f32 v101, v101\n"
"v_mfma_f32_16x16x32_bf16 v[140:143], a[200:203], v[168:171], v[140:143]\n"
"ds_read_b64_tr_b16 a[224:225], v37 offset:0\n"
"v_exp_f32 v102, v102\n"
"v_mfma_f32_16x16x32_bf16 v[156:159], a[200:203], v[200:203], v[156:159]\n"
"s_waitcnt lgkmcnt(5)\n"
"v_exp_f32 v103, v103\n"
"v_mfma_f32_16x16x32_bf16 v[108:111], a[180:183], v[112:115], v[108:111]\n"
"v_cvt_pk_bf16_f32 v212, v100, v101\n"
"v_cvt_pk_bf16_f32 v213, v102, v103\n"
"v_mfma_f32_16x16x32_bf16 v[124:127], a[180:183], v[144:147], v[124:127]\n"
"v_exp_f32 v116, v116\n"
"v_mfma_f32_16x16x32_bf16 v[140:143], a[180:183], v[176:179], v[140:143]\n"
"ds_read_b64_tr_b16 a[204:205], v36 offset:512\n"
"v_exp_f32 v117, v117\n"
"v_mfma_f32_16x16x32_bf16 v[156:159], a[180:183], v[208:211], v[156:159]\n"
"s_waitcnt lgkmcnt(5)\n"
"v_exp_f32 v118, v118\n"
"v_mfma_f32_16x16x32_bf16 v[108:111], a[184:187], v[120:123], v[108:111]\n"
"ds_read_b64_tr_b16 a[206:207], v36 offset:4608\n"
"v_exp_f32 v119, v119\n"
"v_mfma_f32_16x16x32_bf16 v[124:127], a[184:187], v[152:155], v[124:127]\n"
"v_cvt_pk_bf16_f32 v220, v116, v117\n"
"v_cvt_pk_bf16_f32 v221, v118, v119\n"
"v_mfma_f32_16x16x32_bf16 v[140:143], a[184:187], v[184:187], v[140:143]\n"
"v_exp_f32 v132, v132\n"
"v_mfma_f32_16x16x32_bf16 v[156:159], a[184:187], v[216:219], v[156:159]\n"
"s_waitcnt vmcnt(12)\n"
"s_barrier\n"
"v_exp_f32 v133, v133\n"
"v_mfma_f32_16x16x32_bf16 a[160:163], v[60:63], v[180:183], a[160:163]\n"
"ds_read_b64_tr_b16 a[208:209], v37 offset:512\n"
"v_exp_f32 v134, v134\n"
"v_mfma_f32_16x16x32_bf16 a[164:167], v[60:63], v[188:191], a[164:167]\n"
"ds_read_b64_tr_b16 a[210:211], v37 offset:4608\n"
"v_exp_f32 v135, v135\n"
"v_mfma_f32_16x16x32_bf16 a[168:171], v[60:63], v[196:199], a[168:171]\n"
"v_cvt_pk_bf16_f32 v228, v132, v133\n"
"v_cvt_pk_bf16_f32 v229, v134, v135\n"
"v_mfma_f32_16x16x32_bf16 a[172:175], v[60:63], v[204:207], a[172:175]\n"
"v_exp_f32 v148, v148\n"
"v_mfma_f32_16x16x32_bf16 a[176:179], v[60:63], v[244:247], a[176:179]\n"
"s_waitcnt lgkmcnt(6)\n"
"v_exp_f32 v149, v149\n"
"v_mfma_f32_16x16x32_bf16 a[0:3], a[220:223], v[180:183], a[0:3]\n"
"ds_read_b64_tr_b16 a[212:213], v36 offset:1024\n"
"v_exp_f32 v150, v150\n"
"v_mfma_f32_16x16x32_bf16 a[32:35], a[220:223], v[188:191], a[32:35]\n"
"ds_read_b64_tr_b16 a[214:215], v36 offset:5120\n"
"v_exp_f32 v151, v151\n"
"v_mfma_f32_16x16x32_bf16 a[64:67], a[220:223], v[196:199], a[64:67]\n"
"v_cvt_pk_bf16_f32 v236, v148, v149\n"
"v_cvt_pk_bf16_f32 v237, v150, v151\n"
"v_mfma_f32_16x16x32_bf16 a[96:99], a[220:223], v[204:207], a[96:99]\n"
"v_exp_f32 v108, v108\n"
"v_mfma_f32_16x16x32_bf16 a[128:131], a[220:223], v[244:247], a[128:131]\n"
"s_waitcnt lgkmcnt(6)\n"
"v_exp_f32 v109, v109\n"
"v_mfma_f32_16x16x32_bf16 a[4:7], a[224:227], v[180:183], a[4:7]\n"
"ds_read_b64_tr_b16 a[216:217], v37 offset:1024\n"
"v_exp_f32 v110, v110\n"
"v_mfma_f32_16x16x32_bf16 a[36:39], a[224:227], v[188:191], a[36:39]\n"
"ds_read_b64_tr_b16 a[218:219], v37 offset:5120\n"
"v_exp_f32 v111, v111\n"
"v_mfma_f32_16x16x32_bf16 a[68:71], a[224:227], v[196:199], a[68:71]\n"
"v_cvt_pk_bf16_f32 v214, v108, v109\n"
"v_cvt_pk_bf16_f32 v215, v110, v111\n"
"v_mfma_f32_16x16x32_bf16 a[100:103], a[224:227], v[204:207], a[100:103]\n"
"v_exp_f32 v124, v124\n"
"v_mfma_f32_16x16x32_bf16 a[132:135], a[224:227], v[244:247], a[132:135]\n"
"s_waitcnt lgkmcnt(6)\n"
"v_exp_f32 v125, v125\n"
"v_mfma_f32_16x16x32_bf16 a[8:11], a[204:207], v[180:183], a[8:11]\n"
"ds_read_b64_tr_b16 a[220:221], v36 offset:1536\n"
"v_exp_f32 v126, v126\n"
"v_mfma_f32_16x16x32_bf16 a[40:43], a[204:207], v[188:191], a[40:43]\n"
"ds_read_b64_tr_b16 a[222:223], v36 offset:5632\n"
"v_exp_f32 v127, v127\n"
"v_mfma_f32_16x16x32_bf16 a[72:75], a[204:207], v[196:199], a[72:75]\n"
"v_cvt_pk_bf16_f32 v222, v124, v125\n"
"v_cvt_pk_bf16_f32 v223, v126, v127\n"
"v_mfma_f32_16x16x32_bf16 a[104:107], a[204:207], v[204:207], a[104:107]\n"
"v_exp_f32 v140, v140\n"
"v_mfma_f32_16x16x32_bf16 a[136:139], a[204:207], v[244:247], a[136:139]\n"
"s_waitcnt lgkmcnt(6)\n"
"v_exp_f32 v141, v141\n"
"v_mfma_f32_16x16x32_bf16 a[12:15], a[208:211], v[180:183], a[12:15]\n"
"ds_read_b64_tr_b16 a[224:225], v37 offset:1536\n"
"v_exp_f32 v142, v142\n"
"v_mfma_f32_16x16x32_bf16 a[44:47], a[208:211], v[188:191], a[44:47]\n"
"ds_read_b64_tr_b16 a[226:227], v37 offset:5632\n"
"v_exp_f32 v143, v143\n"
"v_mfma_f32_16x16x32_bf16 a[76:79], a[208:211], v[196:199], a[76:79]\n"
"v_cvt_pk_bf16_f32 v230, v140, v141\n"
"v_cvt_pk_bf16_f32 v231, v142, v143\n"
"v_mfma_f32_16x16x32_bf16 a[108:111], a[208:211], v[204:207], a[108:111]\n"
"v_exp_f32 v156, v156\n"
"v_mfma_f32_16x16x32_bf16 a[140:143], a[208:211], v[244:247], a[140:143]\n"
"s_waitcnt lgkmcnt(6)\n"
"v_exp_f32 v157, v157\n"
"v_mfma_f32_16x16x32_bf16 a[16:19], a[212:215], v[180:183], a[16:19]\n"
"ds_read_b128 a[188:191], v28 offset:16384\n"
"v_exp_f32 v158, v158\n"
"v_mfma_f32_16x16x32_bf16 a[48:51], a[212:215], v[188:191], a[48:51]\n"
"v_exp_f32 v159, v159\n"
"v_mfma_f32_16x16x32_bf16 a[80:83], a[212:215], v[196:199], a[80:83]\n"
"v_cvt_pk_bf16_f32 v238, v156, v157\n"
"v_cvt_pk_bf16_f32 v239, v158, v159\n"
"v_mfma_f32_16x16x32_bf16 a[112:115], a[212:215], v[204:207], a[112:115]\n"
"v_mfma_f32_16x16x32_bf16 a[144:147], a[212:215], v[244:247], a[144:147]\n"
"s_waitcnt lgkmcnt(5)\n"
"v_mfma_f32_16x16x32_bf16 a[20:23], a[216:219], v[180:183], a[20:23]\n"
"ds_read_b128 a[192:195], v29 offset:16384\n"
"v_mfma_f32_16x16x32_bf16 a[52:55], a[216:219], v[188:191], a[52:55]\n"
"v_mfma_f32_16x16x32_bf16 a[84:87], a[216:219], v[196:199], a[84:87]\n"
"v_mfma_f32_16x16x32_bf16 a[116:119], a[216:219], v[204:207], a[116:119]\n"
"v_mfma_f32_16x16x32_bf16 a[148:151], a[216:219], v[244:247], a[148:151]\n"
"s_waitcnt lgkmcnt(4)\n"
"v_mfma_f32_16x16x32_bf16 a[24:27], a[220:223], v[180:183], a[24:27]\n"
"ds_read_b128 a[196:199], v30 offset:16384\n"
"v_mfma_f32_16x16x32_bf16 a[56:59], a[220:223], v[188:191], a[56:59]\n"
"v_mfma_f32_16x16x32_bf16 a[88:91], a[220:223], v[196:199], a[88:91]\n"
"v_mfma_f32_16x16x32_bf16 a[120:123], a[220:223], v[204:207], a[120:123]\n"
"v_mfma_f32_16x16x32_bf16 a[152:155], a[220:223], v[244:247], a[152:155]\n"
"s_waitcnt lgkmcnt(3)\n"
"v_mfma_f32_16x16x32_bf16 a[28:31], a[224:227], v[180:183], a[28:31]\n"
"ds_read_b128 a[200:203], v31 offset:16384\n"
"v_mfma_f32_16x16x32_bf16 a[60:63], a[224:227], v[188:191], a[60:63]\n"
"v_mfma_f32_16x16x32_bf16 a[92:95], a[224:227], v[196:199], a[92:95]\n"
"v_mfma_f32_16x16x32_bf16 a[124:127], a[224:227], v[204:207], a[124:127]\n"
"v_mfma_f32_16x16x32_bf16 a[156:159], a[224:227], v[244:247], a[156:159]\n"
"s_waitcnt lgkmcnt(3)\n"
"v_mfma_f32_16x16x32_bf16 v[100:103], a[188:191], v[96:99], v[40:43]\n"
"ds_read_b128 a[180:183], v28 offset:20480\n"
"v_mfma_f32_16x16x32_bf16 v[116:119], a[188:191], v[128:131], v[44:47]\n"
"s_add_u32 m0, s61, 114688\n"
"s_mov_b32 s66, 114688\n"
"v_mfma_f32_16x16x32_bf16 v[132:135], a[188:191], v[160:163], v[48:51]\n"
"buffer_load_dwordx4 v95, s[52:55], s66 offen nt lds\n"
"v_mfma_f32_16x16x32_bf16 v[148:151], a[188:191], v[192:195], v[52:55]\n"
"v_mfma_f32_16x16x32_bf16 v[164:167], a[188:191], v[224:227], v[56:59]\n"
"s_waitcnt lgkmcnt(3)\n"
"v_mfma_f32_16x16x32_bf16 v[100:103], a[192:195], v[104:107], v[100:103]\n"
"ds_read_b128 a[184:187], v29 offset:20480\n"
"v_mfma_f32_16x16x32_bf16 v[116:119], a[192:195], v[136:139], v[116:119]\n"
"buffer_load_dwordx4 v27, s[52:55], s66 offen offset:1024 nt lds\n"
"v_mfma_f32_16x16x32_bf16 v[132:135], a[192:195], v[168:171], v[132:135]\n"
"v_mfma_f32_16x16x32_bf16 v[148:151], a[192:195], v[200:203], v[148:151]\n"
"v_mfma_f32_16x16x32_bf16 v[164:167], a[192:195], v[232:235], v[164:167]\n"
"s_waitcnt lgkmcnt(3)\n"
"v_mfma_f32_16x16x32_bf16 v[100:103], a[196:199], v[112:115], v[100:103]\n"
"ds_read_b128 a[188:191], v30 offset:20480\n"
"v_mfma_f32_16x16x32_bf16 v[116:119], a[196:199], v[144:147], v[116:119]\n"
"buffer_load_dwordx4 v95, s[52:55], s66 offen offset:2048 nt lds\n"
"v_mfma_f32_16x16x32_bf16 v[132:135], a[196:199], v[176:179], v[132:135]\n"
"v_mfma_f32_16x16x32_bf16 v[148:151], a[196:199], v[208:211], v[148:151]\n"
"v_mfma_f32_16x16x32_bf16 v[164:167], a[196:199], v[240:243], v[164:167]\n"
"s_waitcnt lgkmcnt(3)\n"
"v_mfma_f32_16x16x32_bf16 v[100:103], a[200:203], v[120:123], v[100:103]\n"
"ds_read_b128 a[192:195], v31 offset:20480\n"
"v_mfma_f32_16x16x32_bf16 v[116:119], a[200:203], v[152:155], v[116:119]\n"
"buffer_load_dwordx4 v27, s[52:55], s66 offen offset:3072 nt lds\n"
"v_mfma_f32_16x16x32_bf16 v[132:135], a[200:203], v[184:187], v[132:135]\n"
"v_mfma_f32_16x16x32_bf16 v[148:151], a[200:203], v[216:219], v[148:151]\n"
"v_mfma_f32_16x16x32_bf16 v[164:167], a[200:203], v[248:251], v[164:167]\n"
"s_waitcnt lgkmcnt(3)\n"
"v_mfma_f32_16x16x32_bf16 v[108:111], a[180:183], v[96:99], v[40:43]\n"
"v_mfma_f32_16x16x32_bf16 v[124:127], a[180:183], v[128:131], v[44:47]\n"
"v_mfma_f32_16x16x32_bf16 v[140:143], a[180:183], v[160:163], v[48:51]\n"
"v_mfma_f32_16x16x32_bf16 v[156:159], a[180:183], v[192:195], v[52:55]\n"
"v_exp_f32 v100, v100\n"
"v_mfma_f32_16x16x32_bf16 v[172:175], a[180:183], v[224:227], v[56:59]\n"
"s_waitcnt lgkmcnt(2)\n"
"v_exp_f32 v101, v101\n"
"v_mfma_f32_16x16x32_bf16 v[108:111], a[184:187], v[104:107], v[108:111]\n"
"v_exp_f32 v102, v102\n"
"v_mfma_f32_16x16x32_bf16 v[124:127], a[184:187], v[136:139], v[124:127]\n"
"v_exp_f32 v103, v103\n"
"v_mfma_f32_16x16x32_bf16 v[140:143], a[184:187], v[168:171], v[140:143]\n"
"ds_read_b64_tr_b16 a[206:207], v38 offset:4096\n"
"v_cvt_pk_bf16_f32 v180, v100, v101\n"
"v_cvt_pk_bf16_f32 v181, v102, v103\n"
"v_mfma_f32_16x16x32_bf16 v[156:159], a[184:187], v[200:203], v[156:159]\n"
"ds_read_b64_tr_b16 a[204:205], v38 offset:0\n"
"v_exp_f32 v116, v116\n"
"v_mfma_f32_16x16x32_bf16 v[172:175], a[184:187], v[232:235], v[172:175]\n"
"s_waitcnt lgkmcnt(3)\n"
"v_exp_f32 v117, v117\n"
"v_mfma_f32_16x16x32_bf16 v[108:111], a[188:191], v[112:115], v[108:111]\n"
"v_exp_f32 v118, v118\n"
"v_mfma_f32_16x16x32_bf16 v[124:127], a[188:191], v[144:147], v[124:127]\n"
"v_exp_f32 v119, v119\n"
"v_mfma_f32_16x16x32_bf16 v[140:143], a[188:191], v[176:179], v[140:143]\n"
"ds_read_b64_tr_b16 a[208:209], v39 offset:0\n"
"v_cvt_pk_bf16_f32 v188, v116, v117\n"
"v_cvt_pk_bf16_f32 v189, v118, v119\n"
"v_mfma_f32_16x16x32_bf16 v[156:159], a[188:191], v[208:211], v[156:159]\n"
"ds_read_b64_tr_b16 a[210:211], v39 offset:4096\n"
"v_exp_f32 v132, v132\n"
"v_mfma_f32_16x16x32_bf16 v[172:175], a[188:191], v[240:243], v[172:175]\n"
"s_waitcnt lgkmcnt(4)\n"
"v_exp_f32 v133, v133\n"
"v_mfma_f32_16x16x32_bf16 v[108:111], a[192:195], v[120:123], v[108:111]\n"
"v_exp_f32 v134, v134\n"
"v_mfma_f32_16x16x32_bf16 v[124:127], a[192:195], v[152:155], v[124:127]\n"
"ds_read_b64_tr_b16 a[212:213], v38 offset:512\n"
"v_exp_f32 v135, v135\n"
"v_mfma_f32_16x16x32_bf16 v[140:143], a[192:195], v[184:187], v[140:143]\n"
"ds_read_b64_tr_b16 a[214:215], v38 offset:4608\n"
"v_cvt_pk_bf16_f32 v196, v132, v133\n"
"v_cvt_pk_bf16_f32 v197, v134, v135\n"
"v_mfma_f32_16x16x32_bf16 v[156:159], a[192:195], v[216:219], v[156:159]\n"
"v_exp_f32 v148, v148\n"
"v_mfma_f32_16x16x32_bf16 v[172:175], a[192:195], v[248:251], v[172:175]\n"
"v_exp_f32 v149, v149\n"
"v_mfma_f32_16x16x32_bf16 a[160:163], v[60:63], v[212:215], a[160:163]\n"
"ds_read_b64_tr_b16 a[216:217], v39 offset:512\n"
"v_exp_f32 v150, v150\n"
"v_mfma_f32_16x16x32_bf16 a[164:167], v[60:63], v[220:223], a[164:167]\n"
"ds_read_b64_tr_b16 a[218:219], v39 offset:4608\n"
"v_exp_f32 v151, v151\n"
"v_mfma_f32_16x16x32_bf16 a[168:171], v[60:63], v[228:231], a[168:171]\n"
"v_cvt_pk_bf16_f32 v204, v148, v149\n"
"v_cvt_pk_bf16_f32 v205, v150, v151\n"
"v_mfma_f32_16x16x32_bf16 a[172:175], v[60:63], v[236:239], a[172:175]\n"
"s_waitcnt lgkmcnt(6)\n"
"v_exp_f32 v164, v164\n"
"v_mfma_f32_16x16x32_bf16 a[0:3], a[204:207], v[212:215], a[0:3]\n"
"ds_read_b64_tr_b16 a[220:221], v38 offset:1024\n"
"v_exp_f32 v165, v165\n"
"v_mfma_f32_16x16x32_bf16 a[32:35], a[204:207], v[220:223], a[32:35]\n"
"ds_read_b64_tr_b16 a[222:223], v38 offset:5120\n"
"v_exp_f32 v166, v166\n"
"v_mfma_f32_16x16x32_bf16 a[64:67], a[204:207], v[228:231], a[64:67]\n"
"v_exp_f32 v167, v167\n"
"v_mfma_f32_16x16x32_bf16 a[96:99], a[204:207], v[236:239], a[96:99]\n"
"s_waitcnt lgkmcnt(6)\n"
"v_cvt_pk_bf16_f32 v244, v164, v165\n"
"v_cvt_pk_bf16_f32 v245, v166, v167\n"
"v_mfma_f32_16x16x32_bf16 a[4:7], a[208:211], v[212:215], a[4:7]\n"
"ds_read_b64_tr_b16 a[224:225], v39 offset:1024\n"
"v_exp_f32 v108, v108\n"
"v_mfma_f32_16x16x32_bf16 a[36:39], a[208:211], v[220:223], a[36:39]\n"
"ds_read_b64_tr_b16 a[226:227], v39 offset:5120\n"
"v_exp_f32 v109, v109\n"
"v_mfma_f32_16x16x32_bf16 a[68:71], a[208:211], v[228:231], a[68:71]\n"
"v_exp_f32 v110, v110\n"
"v_mfma_f32_16x16x32_bf16 a[100:103], a[208:211], v[236:239], a[100:103]\n"
"s_waitcnt lgkmcnt(6)\n"
"v_exp_f32 v111, v111\n"
"v_mfma_f32_16x16x32_bf16 a[8:11], a[212:215], v[212:215], a[8:11]\n"
"ds_read_b64_tr_b16 a[204:205], v38 offset:1536\n"
"v_cvt_pk_bf16_f32 v182, v108, v109\n"
"v_cvt_pk_bf16_f32 v183, v110, v111\n"
"v_mfma_f32_16x16x32_bf16 a[40:43], a[212:215], v[220:223], a[40:43]\n"
"ds_read_b64_tr_b16 a[206:207], v38 offset:5632\n"
"v_exp_f32 v124, v124\n"
"v_mfma_f32_16x16x32_bf16 a[72:75], a[212:215], v[228:231], a[72:75]\n"
"v_exp_f32 v125, v125\n"
"v_mfma_f32_16x16x32_bf16 a[104:107], a[212:215], v[236:239], a[104:107]\n"
"s_waitcnt lgkmcnt(6)\n"
"v_exp_f32 v126, v126\n"
"v_mfma_f32_16x16x32_bf16 a[12:15], a[216:219], v[212:215], a[12:15]\n"
"ds_read_b64_tr_b16 a[208:209], v39 offset:1536\n"
"v_exp_f32 v127, v127\n"
"v_mfma_f32_16x16x32_bf16 a[44:47], a[216:219], v[220:223], a[44:47]\n"
"ds_read_b64_tr_b16 a[210:211], v39 offset:5632\n"
"v_cvt_pk_bf16_f32 v190, v124, v125\n"
"v_cvt_pk_bf16_f32 v191, v126, v127\n"
"v_mfma_f32_16x16x32_bf16 a[76:79], a[216:219], v[228:231], a[76:79]\n"
"v_exp_f32 v140, v140\n"
"v_mfma_f32_16x16x32_bf16 a[108:111], a[216:219], v[236:239], a[108:111]\n"
"s_waitcnt lgkmcnt(6)\n"
"v_exp_f32 v141, v141\n"
"v_mfma_f32_16x16x32_bf16 a[16:19], a[220:223], v[212:215], a[16:19]\n"
"ds_read_b128 a[196:199], v32 offset:16384\n"
"v_exp_f32 v142, v142\n"
"v_mfma_f32_16x16x32_bf16 a[48:51], a[220:223], v[220:223], a[48:51]\n"
"v_exp_f32 v143, v143\n"
"v_mfma_f32_16x16x32_bf16 a[80:83], a[220:223], v[228:231], a[80:83]\n"
"v_cvt_pk_bf16_f32 v198, v140, v141\n"
"v_cvt_pk_bf16_f32 v199, v142, v143\n"
"v_mfma_f32_16x16x32_bf16 a[112:115], a[220:223], v[236:239], a[112:115]\n"
"s_waitcnt lgkmcnt(5)\n"
"v_exp_f32 v156, v156\n"
"v_mfma_f32_16x16x32_bf16 a[20:23], a[224:227], v[212:215], a[20:23]\n"
"ds_read_b128 a[200:203], v33 offset:16384\n"
"v_exp_f32 v157, v157\n"
"v_mfma_f32_16x16x32_bf16 a[52:55], a[224:227], v[220:223], a[52:55]\n"
"v_exp_f32 v158, v158\n"
"v_mfma_f32_16x16x32_bf16 a[84:87], a[224:227], v[228:231], a[84:87]\n"
"v_exp_f32 v159, v159\n"
"v_mfma_f32_16x16x32_bf16 a[116:119], a[224:227], v[236:239], a[116:119]\n"
"s_waitcnt lgkmcnt(4)\n"
"v_cvt_pk_bf16_f32 v206, v156, v157\n"
"v_cvt_pk_bf16_f32 v207, v158, v159\n"
"v_mfma_f32_16x16x32_bf16 a[24:27], a[204:207], v[212:215], a[24:27]\n"
"ds_read_b128 a[180:183], v34 offset:16384\n"
"v_exp_f32 v172, v172\n"
"v_mfma_f32_16x16x32_bf16 a[56:59], a[204:207], v[220:223], a[56:59]\n"
"v_exp_f32 v173, v173\n"
"v_mfma_f32_16x16x32_bf16 a[88:91], a[204:207], v[228:231], a[88:91]\n"
"v_exp_f32 v174, v174\n"
"v_mfma_f32_16x16x32_bf16 a[120:123], a[204:207], v[236:239], a[120:123]\n"
"s_waitcnt lgkmcnt(3)\n"
"v_exp_f32 v175, v175\n"
"v_mfma_f32_16x16x32_bf16 a[28:31], a[208:211], v[212:215], a[28:31]\n"
"ds_read_b128 a[184:187], v35 offset:16384\n"
"v_cvt_pk_bf16_f32 v246, v172, v173\n"
"v_cvt_pk_bf16_f32 v247, v174, v175\n"
"v_mfma_f32_16x16x32_bf16 a[60:63], a[208:211], v[220:223], a[60:63]\n"
"v_mfma_f32_16x16x32_bf16 a[92:95], a[208:211], v[228:231], a[92:95]\n"
"v_mfma_f32_16x16x32_bf16 a[124:127], a[208:211], v[236:239], a[124:127]\n"
"s_waitcnt lgkmcnt(3)\n"
"v_mfma_f32_16x16x32_bf16 v[100:103], a[196:199], v[96:99], v[40:43]\n"
"ds_read_b128 a[188:191], v32 offset:20480\n"
"v_mfma_f32_16x16x32_bf16 v[116:119], a[196:199], v[128:131], v[44:47]\n"
"s_add_u32 m0, s61, 0\n"
"s_mov_b32 s65, 131072\n"
"v_mfma_f32_16x16x32_bf16 v[132:135], a[196:199], v[160:163], v[48:51]\n"
"buffer_load_dwordx4 v24, s[48:51], s65 offen nt lds\n"
"v_mfma_f32_16x16x32_bf16 v[148:151], a[196:199], v[192:195], v[52:55]\n"
"s_waitcnt lgkmcnt(3)\n"
"v_mfma_f32_16x16x32_bf16 v[100:103], a[200:203], v[104:107], v[100:103]\n"
"ds_read_b128 a[192:195], v33 offset:20480\n"
"v_mfma_f32_16x16x32_bf16 v[116:119], a[200:203], v[136:139], v[116:119]\n"
"v_mfma_f32_16x16x32_bf16 v[132:135], a[200:203], v[168:171], v[132:135]\n"
"buffer_load_dwordx4 v25, s[48:51], s65 offen offset:1024 nt lds\n"
"v_mfma_f32_16x16x32_bf16 v[148:151], a[200:203], v[200:203], v[148:151]\n"
"s_waitcnt lgkmcnt(3)\n"
"v_mfma_f32_16x16x32_bf16 v[100:103], a[180:183], v[112:115], v[100:103]\n"
"ds_read_b128 a[196:199], v34 offset:20480\n"
"v_mfma_f32_16x16x32_bf16 v[116:119], a[180:183], v[144:147], v[116:119]\n"
"v_mfma_f32_16x16x32_bf16 v[132:135], a[180:183], v[176:179], v[132:135]\n"
"buffer_load_dwordx4 v93, s[48:51], s65 offen offset:2048 nt lds\n"
"v_mfma_f32_16x16x32_bf16 v[148:151], a[180:183], v[208:211], v[148:151]\n"
"s_waitcnt lgkmcnt(3)\n"
"v_mfma_f32_16x16x32_bf16 v[100:103], a[184:187], v[120:123], v[100:103]\n"
"ds_read_b128 a[200:203], v35 offset:20480\n"
"v_mfma_f32_16x16x32_bf16 v[116:119], a[184:187], v[152:155], v[116:119]\n"
"v_mfma_f32_16x16x32_bf16 v[132:135], a[184:187], v[184:187], v[132:135]\n"
"buffer_load_dwordx4 v94, s[48:51], s65 offen offset:3072 nt lds\n"
"v_mfma_f32_16x16x32_bf16 v[148:151], a[184:187], v[216:219], v[148:151]\n"
"s_waitcnt lgkmcnt(3)\n"
"v_mfma_f32_16x16x32_bf16 v[108:111], a[188:191], v[96:99], v[40:43]\n"
"v_mfma_f32_16x16x32_bf16 v[124:127], a[188:191], v[128:131], v[44:47]\n"
"ds_read_b64_tr_b16 a[212:213], v36 offset:16384\n"
"v_mfma_f32_16x16x32_bf16 v[140:143], a[188:191], v[160:163], v[48:51]\n"
"ds_read_b64_tr_b16 a[214:215], v36 offset:20480\n"
"v_mfma_f32_16x16x32_bf16 v[156:159], a[188:191], v[192:195], v[52:55]\n"
"s_waitcnt lgkmcnt(4)\n"
"v_mfma_f32_16x16x32_bf16 v[108:111], a[192:195], v[104:107], v[108:111]\n"
"v_exp_f32 v100, v100\n"
"v_mfma_f32_16x16x32_bf16 v[124:127], a[192:195], v[136:139], v[124:127]\n"
"ds_read_b64_tr_b16 a[218:219], v37 offset:20480\n"
"v_exp_f32 v101, v101\n"
"v_mfma_f32_16x16x32_bf16 v[140:143], a[192:195], v[168:171], v[140:143]\n"
"ds_read_b64_tr_b16 a[216:217], v37 offset:16384\n"
"v_exp_f32 v102, v102\n"
"v_mfma_f32_16x16x32_bf16 v[156:159], a[192:195], v[200:203], v[156:159]\n"
"s_waitcnt lgkmcnt(5)\n"
"v_exp_f32 v103, v103\n"
"v_mfma_f32_16x16x32_bf16 v[108:111], a[196:199], v[112:115], v[108:111]\n"
"v_cvt_pk_bf16_f32 v212, v100, v101\n"
"v_cvt_pk_bf16_f32 v213, v102, v103\n"
"v_mfma_f32_16x16x32_bf16 v[124:127], a[196:199], v[144:147], v[124:127]\n"
"v_exp_f32 v116, v116\n"
"v_mfma_f32_16x16x32_bf16 v[140:143], a[196:199], v[176:179], v[140:143]\n"
"ds_read_b64_tr_b16 a[220:221], v36 offset:16896\n"
"v_exp_f32 v117, v117\n"
"v_mfma_f32_16x16x32_bf16 v[156:159], a[196:199], v[208:211], v[156:159]\n"
"s_waitcnt lgkmcnt(5)\n"
"v_exp_f32 v118, v118\n"
"v_mfma_f32_16x16x32_bf16 v[108:111], a[200:203], v[120:123], v[108:111]\n"
"ds_read_b64_tr_b16 a[222:223], v36 offset:20992\n"
"v_exp_f32 v119, v119\n"
"v_mfma_f32_16x16x32_bf16 v[124:127], a[200:203], v[152:155], v[124:127]\n"
"v_cvt_pk_bf16_f32 v220, v116, v117\n"
"v_cvt_pk_bf16_f32 v221, v118, v119\n"
"v_mfma_f32_16x16x32_bf16 v[140:143], a[200:203], v[184:187], v[140:143]\n"
"v_exp_f32 v132, v132\n"
"v_mfma_f32_16x16x32_bf16 v[156:159], a[200:203], v[216:219], v[156:159]\n"
"s_waitcnt vmcnt(12)\n"
"s_barrier\n"
"v_exp_f32 v133, v133\n"
"v_mfma_f32_16x16x32_bf16 a[160:163], v[60:63], v[180:183], a[160:163]\n"
"ds_read_b64_tr_b16 a[224:225], v37 offset:16896\n"
"v_exp_f32 v134, v134\n"
"v_mfma_f32_16x16x32_bf16 a[164:167], v[60:63], v[188:191], a[164:167]\n"
"ds_read_b64_tr_b16 a[226:227], v37 offset:20992\n"
"v_exp_f32 v135, v135\n"
"v_mfma_f32_16x16x32_bf16 a[168:171], v[60:63], v[196:199], a[168:171]\n"
"s_mov_b64 s[92:93], %20\n"
"s_and_b32 s93, s93, 0xffff\n"
"v_cvt_pk_bf16_f32 v228, v132, v133\n"
"v_cvt_pk_bf16_f32 v229, v134, v135\n"
"v_mfma_f32_16x16x32_bf16 a[172:175], v[60:63], v[204:207], a[172:175]\n"
"s_mov_b32 s94, 4718592\n"
"s_mov_b32 s95, 0x20000\n"
"v_exp_f32 v148, v148\n"
"v_mfma_f32_16x16x32_bf16 a[176:179], v[60:63], v[244:247], a[176:179]\n"
"s_waitcnt lgkmcnt(6)\n"
"v_exp_f32 v149, v149\n"
"v_mfma_f32_16x16x32_bf16 a[0:3], a[212:215], v[180:183], a[0:3]\n"
"ds_read_b64_tr_b16 a[204:205], v36 offset:17408\n"
"v_exp_f32 v150, v150\n"
"v_mfma_f32_16x16x32_bf16 a[32:35], a[212:215], v[188:191], a[32:35]\n"
"ds_read_b64_tr_b16 a[206:207], v36 offset:21504\n"
"v_exp_f32 v151, v151\n"
"v_mfma_f32_16x16x32_bf16 a[64:67], a[212:215], v[196:199], a[64:67]\n"
"s_mov_b32 s76, %21\n"
"v_cvt_pk_bf16_f32 v236, v148, v149\n"
"v_cvt_pk_bf16_f32 v237, v150, v151\n"
"v_mfma_f32_16x16x32_bf16 a[96:99], a[212:215], v[204:207], a[96:99]\n"
"v_exp_f32 v108, v108\n"
"v_mfma_f32_16x16x32_bf16 a[128:131], a[212:215], v[244:247], a[128:131]\n"
"s_waitcnt lgkmcnt(6)\n"
"v_exp_f32 v109, v109\n"
"v_mfma_f32_16x16x32_bf16 a[4:7], a[216:219], v[180:183], a[4:7]\n"
"ds_read_b64_tr_b16 a[208:209], v37 offset:17408\n"
"v_exp_f32 v110, v110\n"
"v_mfma_f32_16x16x32_bf16 a[36:39], a[216:219], v[188:191], a[36:39]\n"
"ds_read_b64_tr_b16 a[210:211], v37 offset:21504\n"
"v_exp_f32 v111, v111\n"
"v_mfma_f32_16x16x32_bf16 a[68:71], a[216:219], v[196:199], a[68:71]\n"
"v_cvt_pk_bf16_f32 v214, v108, v109\n"
"v_cvt_pk_bf16_f32 v215, v110, v111\n"
"v_mfma_f32_16x16x32_bf16 a[100:103], a[216:219], v[204:207], a[100:103]\n"
"v_exp_f32 v124, v124\n"
"v_mfma_f32_16x16x32_bf16 a[132:135], a[216:219], v[244:247], a[132:135]\n"
"s_waitcnt lgkmcnt(6)\n"
"v_exp_f32 v125, v125\n"
"v_mfma_f32_16x16x32_bf16 a[8:11], a[220:223], v[180:183], a[8:11]\n"
"ds_read_b64_tr_b16 a[212:213], v36 offset:17920\n"
"v_exp_f32 v126, v126\n"
"v_mfma_f32_16x16x32_bf16 a[40:43], a[220:223], v[188:191], a[40:43]\n"
"ds_read_b64_tr_b16 a[214:215], v36 offset:22016\n"
"v_exp_f32 v127, v127\n"
"v_mfma_f32_16x16x32_bf16 a[72:75], a[220:223], v[196:199], a[72:75]\n"
"v_cvt_pk_bf16_f32 v222, v124, v125\n"
"v_cvt_pk_bf16_f32 v223, v126, v127\n"
"v_mfma_f32_16x16x32_bf16 a[104:107], a[220:223], v[204:207], a[104:107]\n"
"v_exp_f32 v140, v140\n"
"v_mfma_f32_16x16x32_bf16 a[136:139], a[220:223], v[244:247], a[136:139]\n"
"s_waitcnt lgkmcnt(6)\n"
"v_exp_f32 v141, v141\n"
"v_mfma_f32_16x16x32_bf16 a[12:15], a[224:227], v[180:183], a[12:15]\n"
"ds_read_b64_tr_b16 a[216:217], v37 offset:17920\n"
"v_exp_f32 v142, v142\n"
"v_mfma_f32_16x16x32_bf16 a[44:47], a[224:227], v[188:191], a[44:47]\n"
"ds_read_b64_tr_b16 a[218:219], v37 offset:22016\n"
"v_exp_f32 v143, v143\n"
"v_mfma_f32_16x16x32_bf16 a[76:79], a[224:227], v[196:199], a[76:79]\n"
"v_cvt_pk_bf16_f32 v230, v140, v141\n"
"v_cvt_pk_bf16_f32 v231, v142, v143\n"
"v_mfma_f32_16x16x32_bf16 a[108:111], a[224:227], v[204:207], a[108:111]\n"
"v_exp_f32 v156, v156\n"
"v_mfma_f32_16x16x32_bf16 a[140:143], a[224:227], v[244:247], a[140:143]\n"
"s_waitcnt lgkmcnt(6)\n"
"v_exp_f32 v157, v157\n"
"v_mfma_f32_16x16x32_bf16 a[16:19], a[204:207], v[180:183], a[16:19]\n"
"ds_read_b128 a[180:183], v28 offset:32768\n"
"v_exp_f32 v158, v158\n"
"v_mfma_f32_16x16x32_bf16 a[48:51], a[204:207], v[188:191], a[48:51]\n"
"v_exp_f32 v159, v159\n"
"v_mfma_f32_16x16x32_bf16 a[80:83], a[204:207], v[196:199], a[80:83]\n"
"v_cvt_pk_bf16_f32 v238, v156, v157\n"
"v_cvt_pk_bf16_f32 v239, v158, v159\n"
"v_mfma_f32_16x16x32_bf16 a[112:115], a[204:207], v[204:207], a[112:115]\n"
"v_mfma_f32_16x16x32_bf16 a[144:147], a[204:207], v[244:247], a[144:147]\n"
"s_waitcnt lgkmcnt(5)\n"
"v_mfma_f32_16x16x32_bf16 a[20:23], a[208:211], v[180:183], a[20:23]\n"
"ds_read_b128 a[184:187], v29 offset:32768\n"
"v_mfma_f32_16x16x32_bf16 a[52:55], a[208:211], v[188:191], a[52:55]\n"
"v_mfma_f32_16x16x32_bf16 a[84:87], a[208:211], v[196:199], a[84:87]\n"
"v_mfma_f32_16x16x32_bf16 a[116:119], a[208:211], v[204:207], a[116:119]\n"
"v_mfma_f32_16x16x32_bf16 a[148:151], a[208:211], v[244:247], a[148:151]\n"
"s_waitcnt lgkmcnt(4)\n"
"v_mfma_f32_16x16x32_bf16 a[24:27], a[212:215], v[180:183], a[24:27]\n"
"ds_read_b128 a[188:191], v30 offset:32768\n"
"v_mfma_f32_16x16x32_bf16 a[56:59], a[212:215], v[188:191], a[56:59]\n"
"v_mfma_f32_16x16x32_bf16 a[88:91], a[212:215], v[196:199], a[88:91]\n"
"v_mfma_f32_16x16x32_bf16 a[120:123], a[212:215], v[204:207], a[120:123]\n"
"v_mfma_f32_16x16x32_bf16 a[152:155], a[212:215], v[244:247], a[152:155]\n"
"s_waitcnt lgkmcnt(3)\n"
"v_mfma_f32_16x16x32_bf16 a[28:31], a[216:219], v[180:183], a[28:31]\n"
"ds_read_b128 a[192:195], v31 offset:32768\n"
"v_mfma_f32_16x16x32_bf16 a[60:63], a[216:219], v[188:191], a[60:63]\n"
"v_mfma_f32_16x16x32_bf16 a[92:95], a[216:219], v[196:199], a[92:95]\n"
"v_mfma_f32_16x16x32_bf16 a[124:127], a[216:219], v[204:207], a[124:127]\n"
"v_mfma_f32_16x16x32_bf16 a[156:159], a[216:219], v[244:247], a[156:159]\n"
"s_waitcnt lgkmcnt(3)\n"
"v_mfma_f32_16x16x32_bf16 v[100:103], a[180:183], v[96:99], v[40:43]\n"
"ds_read_b128 a[196:199], v28 offset:36864\n"
"v_mfma_f32_16x16x32_bf16 v[116:119], a[180:183], v[128:131], v[44:47]\n"
"s_add_u32 m0, s61, 65536\n"
"s_mov_b32 s66, 131072\n"
"v_mfma_f32_16x16x32_bf16 v[132:135], a[180:183], v[160:163], v[48:51]\n"
"buffer_load_dwordx4 v95, s[52:55], s66 offen nt lds\n"
"v_mfma_f32_16x16x32_bf16 v[148:151], a[180:183], v[192:195], v[52:55]\n"
"v_mfma_f32_16x16x32_bf16 v[164:167], a[180:183], v[224:227], v[56:59]\n"
"s_waitcnt lgkmcnt(3)\n"
"v_mfma_f32_16x16x32_bf16 v[100:103], a[184:187], v[104:107], v[100:103]\n"
"ds_read_b128 a[200:203], v29 offset:36864\n"
"v_mfma_f32_16x16x32_bf16 v[116:119], a[184:187], v[136:139], v[116:119]\n"
"buffer_load_dwordx4 v27, s[52:55], s66 offen offset:1024 nt lds\n"
"v_mfma_f32_16x16x32_bf16 v[132:135], a[184:187], v[168:171], v[132:135]\n"
"v_mfma_f32_16x16x32_bf16 v[148:151], a[184:187], v[200:203], v[148:151]\n"
"v_mfma_f32_16x16x32_bf16 v[164:167], a[184:187], v[232:235], v[164:167]\n"
"s_waitcnt lgkmcnt(3)\n"
"v_mfma_f32_16x16x32_bf16 v[100:103], a[188:191], v[112:115], v[100:103]\n"
"ds_read_b128 a[180:183], v30 offset:36864\n"
"v_mfma_f32_16x16x32_bf16 v[116:119], a[188:191], v[144:147], v[116:119]\n"
"buffer_load_dwordx4 v95, s[52:55], s66 offen offset:2048 nt lds\n"
"v_mfma_f32_16x16x32_bf16 v[132:135], a[188:191], v[176:179], v[132:135]\n"
"v_mfma_f32_16x16x32_bf16 v[148:151], a[188:191], v[208:211], v[148:151]\n"
"v_mfma_f32_16x16x32_bf16 v[164:167], a[188:191], v[240:243], v[164:167]\n"
"s_waitcnt lgkmcnt(3)\n"
"v_mfma_f32_16x16x32_bf16 v[100:103], a[192:195], v[120:123], v[100:103]\n"
"ds_read_b128 a[184:187], v31 offset:36864\n"
"v_mfma_f32_16x16x32_bf16 v[116:119], a[192:195], v[152:155], v[116:119]\n"
"buffer_load_dwordx4 v27, s[52:55], s66 offen offset:3072 nt lds\n"
"v_mfma_f32_16x16x32_bf16 v[132:135], a[192:195], v[184:187], v[132:135]\n"
"v_mfma_f32_16x16x32_bf16 v[148:151], a[192:195], v[216:219], v[148:151]\n"
"v_mfma_f32_16x16x32_bf16 v[164:167], a[192:195], v[248:251], v[164:167]\n"
"s_waitcnt lgkmcnt(3)\n"
"v_mfma_f32_16x16x32_bf16 v[108:111], a[196:199], v[96:99], v[40:43]\n"
"v_mfma_f32_16x16x32_bf16 v[124:127], a[196:199], v[128:131], v[44:47]\n"
"v_mfma_f32_16x16x32_bf16 v[140:143], a[196:199], v[160:163], v[48:51]\n"
"v_mfma_f32_16x16x32_bf16 v[156:159], a[196:199], v[192:195], v[52:55]\n"
"v_exp_f32 v100, v100\n"
"v_mfma_f32_16x16x32_bf16 v[172:175], a[196:199], v[224:227], v[56:59]\n"
"s_waitcnt lgkmcnt(2)\n"
"v_exp_f32 v101, v101\n"
"v_mfma_f32_16x16x32_bf16 v[108:111], a[200:203], v[104:107], v[108:111]\n"
"v_exp_f32 v102, v102\n"
"v_mfma_f32_16x16x32_bf16 v[124:127], a[200:203], v[136:139], v[124:127]\n"
"v_exp_f32 v103, v103\n"
"v_mfma_f32_16x16x32_bf16 v[140:143], a[200:203], v[168:171], v[140:143]\n"
"ds_read_b64_tr_b16 a[222:223], v38 offset:20480\n"
"v_cvt_pk_bf16_f32 v180, v100, v101\n"
"v_cvt_pk_bf16_f32 v181, v102, v103\n"
"v_mfma_f32_16x16x32_bf16 v[156:159], a[200:203], v[200:203], v[156:159]\n"
"ds_read_b64_tr_b16 a[220:221], v38 offset:16384\n"
"v_exp_f32 v116, v116\n"
"v_mfma_f32_16x16x32_bf16 v[172:175], a[200:203], v[232:235], v[172:175]\n"
"s_waitcnt lgkmcnt(3)\n"
"v_exp_f32 v117, v117\n"
"v_mfma_f32_16x16x32_bf16 v[108:111], a[180:183], v[112:115], v[108:111]\n"
"v_exp_f32 v118, v118\n"
"v_mfma_f32_16x16x32_bf16 v[124:127], a[180:183], v[144:147], v[124:127]\n"
"v_exp_f32 v119, v119\n"
"v_mfma_f32_16x16x32_bf16 v[140:143], a[180:183], v[176:179], v[140:143]\n"
"ds_read_b64_tr_b16 a[224:225], v39 offset:16384\n"
"v_cvt_pk_bf16_f32 v188, v116, v117\n"
"v_cvt_pk_bf16_f32 v189, v118, v119\n"
"v_mfma_f32_16x16x32_bf16 v[156:159], a[180:183], v[208:211], v[156:159]\n"
"ds_read_b64_tr_b16 a[226:227], v39 offset:20480\n"
"v_exp_f32 v132, v132\n"
"v_mfma_f32_16x16x32_bf16 v[172:175], a[180:183], v[240:243], v[172:175]\n"
"s_waitcnt lgkmcnt(4)\n"
"v_exp_f32 v133, v133\n"
"v_mfma_f32_16x16x32_bf16 v[108:111], a[184:187], v[120:123], v[108:111]\n"
"v_exp_f32 v134, v134\n"
"v_mfma_f32_16x16x32_bf16 v[124:127], a[184:187], v[152:155], v[124:127]\n"
"ds_read_b64_tr_b16 a[204:205], v38 offset:16896\n"
"v_exp_f32 v135, v135\n"
"v_mfma_f32_16x16x32_bf16 v[140:143], a[184:187], v[184:187], v[140:143]\n"
"ds_read_b64_tr_b16 a[206:207], v38 offset:20992\n"
"v_cvt_pk_bf16_f32 v196, v132, v133\n"
"v_cvt_pk_bf16_f32 v197, v134, v135\n"
"v_mfma_f32_16x16x32_bf16 v[156:159], a[184:187], v[216:219], v[156:159]\n"
"v_exp_f32 v148, v148\n"
"v_mfma_f32_16x16x32_bf16 v[172:175], a[184:187], v[248:251], v[172:175]\n"
"v_exp_f32 v149, v149\n"
"v_mfma_f32_16x16x32_bf16 a[160:163], v[60:63], v[212:215], a[160:163]\n"
"ds_read_b64_tr_b16 a[208:209], v39 offset:16896\n"
"v_exp_f32 v150, v150\n"
"v_mfma_f32_16x16x32_bf16 a[164:167], v[60:63], v[220:223], a[164:167]\n"
"ds_read_b64_tr_b16 a[210:211], v39 offset:20992\n"
"v_exp_f32 v151, v151\n"
"v_mfma_f32_16x16x32_bf16 a[168:171], v[60:63], v[228:231], a[168:171]\n"
"v_cvt_pk_bf16_f32 v204, v148, v149\n"
"v_cvt_pk_bf16_f32 v205, v150, v151\n"
"v_mfma_f32_16x16x32_bf16 a[172:175], v[60:63], v[236:239], a[172:175]\n"
"s_waitcnt lgkmcnt(6)\n"
"v_exp_f32 v164, v164\n"
"v_mfma_f32_16x16x32_bf16 a[0:3], a[220:223], v[212:215], a[0:3]\n"
"ds_read_b64_tr_b16 a[212:213], v38 offset:17408\n"
"v_exp_f32 v165, v165\n"
"v_mfma_f32_16x16x32_bf16 a[32:35], a[220:223], v[220:223], a[32:35]\n"
"ds_read_b64_tr_b16 a[214:215], v38 offset:21504\n"
"v_exp_f32 v166, v166\n"
"v_mfma_f32_16x16x32_bf16 a[64:67], a[220:223], v[228:231], a[64:67]\n"
"v_exp_f32 v167, v167\n"
"v_mfma_f32_16x16x32_bf16 a[96:99], a[220:223], v[236:239], a[96:99]\n"
"s_waitcnt lgkmcnt(6)\n"
"v_cvt_pk_bf16_f32 v244, v164, v165\n"
"v_cvt_pk_bf16_f32 v245, v166, v167\n"
"v_mfma_f32_16x16x32_bf16 a[4:7], a[224:227], v[212:215], a[4:7]\n"
"ds_read_b64_tr_b16 a[216:217], v39 offset:17408\n"
"v_exp_f32 v108, v108\n"
"v_mfma_f32_16x16x32_bf16 a[36:39], a[224:227], v[220:223], a[36:39]\n"
"ds_read_b64_tr_b16 a[218:219], v39 offset:21504\n"
"v_exp_f32 v109, v109\n"
"v_mfma_f32_16x16x32_bf16 a[68:71], a[224:227], v[228:231], a[68:71]\n"
"v_exp_f32 v110, v110\n"
"v_mfma_f32_16x16x32_bf16 a[100:103], a[224:227], v[236:239], a[100:103]\n"
"s_waitcnt lgkmcnt(6)\n"
"v_exp_f32 v111, v111\n"
"v_mfma_f32_16x16x32_bf16 a[8:11], a[204:207], v[212:215], a[8:11]\n"
"ds_read_b64_tr_b16 a[220:221], v38 offset:17920\n"
"v_cvt_pk_bf16_f32 v182, v108, v109\n"
"v_cvt_pk_bf16_f32 v183, v110, v111\n"
"v_mfma_f32_16x16x32_bf16 a[40:43], a[204:207], v[220:223], a[40:43]\n"
"ds_read_b64_tr_b16 a[222:223], v38 offset:22016\n"
"v_exp_f32 v124, v124\n"
"v_mfma_f32_16x16x32_bf16 a[72:75], a[204:207], v[228:231], a[72:75]\n"
"v_exp_f32 v125, v125\n"
"v_mfma_f32_16x16x32_bf16 a[104:107], a[204:207], v[236:239], a[104:107]\n"
"s_waitcnt lgkmcnt(6)\n"
"v_exp_f32 v126, v126\n"
"v_mfma_f32_16x16x32_bf16 a[12:15], a[208:211], v[212:215], a[12:15]\n"
"ds_read_b64_tr_b16 a[224:225], v39 offset:17920\n"
"v_exp_f32 v127, v127\n"
"v_mfma_f32_16x16x32_bf16 a[44:47], a[208:211], v[220:223], a[44:47]\n"
"ds_read_b64_tr_b16 a[226:227], v39 offset:22016\n"
"v_cvt_pk_bf16_f32 v190, v124, v125\n"
"v_cvt_pk_bf16_f32 v191, v126, v127\n"
"v_mfma_f32_16x16x32_bf16 a[76:79], a[208:211], v[228:231], a[76:79]\n"
"v_exp_f32 v140, v140\n"
"v_mfma_f32_16x16x32_bf16 a[108:111], a[208:211], v[236:239], a[108:111]\n"
"s_waitcnt lgkmcnt(6)\n"
"v_exp_f32 v141, v141\n"
"v_mfma_f32_16x16x32_bf16 a[16:19], a[212:215], v[212:215], a[16:19]\n"
"ds_read_b128 a[188:191], v32 offset:32768\n"
"v_exp_f32 v142, v142\n"
"v_mfma_f32_16x16x32_bf16 a[48:51], a[212:215], v[220:223], a[48:51]\n"
"v_exp_f32 v143, v143\n"
"v_mfma_f32_16x16x32_bf16 a[80:83], a[212:215], v[228:231], a[80:83]\n"
"v_cvt_pk_bf16_f32 v198, v140, v141\n"
"v_cvt_pk_bf16_f32 v199, v142, v143\n"
"v_mfma_f32_16x16x32_bf16 a[112:115], a[212:215], v[236:239], a[112:115]\n"
"s_waitcnt lgkmcnt(5)\n"
"v_exp_f32 v156, v156\n"
"v_mfma_f32_16x16x32_bf16 a[20:23], a[216:219], v[212:215], a[20:23]\n"
"ds_read_b128 a[192:195], v33 offset:32768\n"
"v_exp_f32 v157, v157\n"
"v_mfma_f32_16x16x32_bf16 a[52:55], a[216:219], v[220:223], a[52:55]\n"
"v_exp_f32 v158, v158\n"
"v_mfma_f32_16x16x32_bf16 a[84:87], a[216:219], v[228:231], a[84:87]\n"
"v_exp_f32 v159, v159\n"
"v_mfma_f32_16x16x32_bf16 a[116:119], a[216:219], v[236:239], a[116:119]\n"
"s_waitcnt lgkmcnt(4)\n"
"v_cvt_pk_bf16_f32 v206, v156, v157\n"
"v_cvt_pk_bf16_f32 v207, v158, v159\n"
"v_mfma_f32_16x16x32_bf16 a[24:27], a[220:223], v[212:215], a[24:27]\n"
"ds_read_b128 a[196:199], v34 offset:32768\n"
"v_exp_f32 v172, v172\n"
"v_mfma_f32_16x16x32_bf16 a[56:59], a[220:223], v[220:223], a[56:59]\n"
"v_exp_f32 v173, v173\n"
"v_mfma_f32_16x16x32_bf16 a[88:91], a[220:223], v[228:231], a[88:91]\n"
"v_exp_f32 v174, v174\n"
"v_mfma_f32_16x16x32_bf16 a[120:123], a[220:223], v[236:239], a[120:123]\n"
"s_waitcnt lgkmcnt(3)\n"
"v_exp_f32 v175, v175\n"
"v_mfma_f32_16x16x32_bf16 a[28:31], a[224:227], v[212:215], a[28:31]\n"
"ds_read_b128 a[200:203], v35 offset:32768\n"
"v_cvt_pk_bf16_f32 v246, v172, v173\n"
"v_cvt_pk_bf16_f32 v247, v174, v175\n"
"v_mfma_f32_16x16x32_bf16 a[60:63], a[224:227], v[220:223], a[60:63]\n"
"buffer_load_dword a228, v26, s[92:95], s76 offen\n"
"v_mfma_f32_16x16x32_bf16 a[92:95], a[224:227], v[228:231], a[92:95]\n"
"s_add_u32 s76, s76, 4096\n"
"v_mfma_f32_16x16x32_bf16 a[124:127], a[224:227], v[236:239], a[124:127]\n"
"s_waitcnt lgkmcnt(3)\n"
"v_mfma_f32_16x16x32_bf16 v[100:103], a[188:191], v[96:99], v[40:43]\n"
"ds_read_b128 a[180:183], v32 offset:36864\n"
"v_mfma_f32_16x16x32_bf16 v[116:119], a[188:191], v[128:131], v[44:47]\n"
"s_add_u32 m0, s61, 16384\n"
"s_mov_b32 s65, 147456\n"
"v_mfma_f32_16x16x32_bf16 v[132:135], a[188:191], v[160:163], v[48:51]\n"
"buffer_load_dwordx4 v24, s[48:51], s65 offen nt lds\n"
"v_mfma_f32_16x16x32_bf16 v[148:151], a[188:191], v[192:195], v[52:55]\n"
"s_waitcnt lgkmcnt(3)\n"
"v_mfma_f32_16x16x32_bf16 v[100:103], a[192:195], v[104:107], v[100:103]\n"
"ds_read_b128 a[184:187], v33 offset:36864\n"
"v_mfma_f32_16x16x32_bf16 v[116:119], a[192:195], v[136:139], v[116:119]\n"
"v_mfma_f32_16x16x32_bf16 v[132:135], a[192:195], v[168:171], v[132:135]\n"
"buffer_load_dwordx4 v25, s[48:51], s65 offen offset:1024 nt lds\n"
"v_mfma_f32_16x16x32_bf16 v[148:151], a[192:195], v[200:203], v[148:151]\n"
"s_waitcnt lgkmcnt(3)\n"
"v_mfma_f32_16x16x32_bf16 v[100:103], a[196:199], v[112:115], v[100:103]\n"
"ds_read_b128 a[188:191], v34 offset:36864\n"
"v_mfma_f32_16x16x32_bf16 v[116:119], a[196:199], v[144:147], v[116:119]\n"
"v_mfma_f32_16x16x32_bf16 v[132:135], a[196:199], v[176:179], v[132:135]\n"
"buffer_load_dwordx4 v93, s[48:51], s65 offen offset:2048 nt lds\n"
"v_mfma_f32_16x16x32_bf16 v[148:151], a[196:199], v[208:211], v[148:151]\n"
"s_waitcnt lgkmcnt(3)\n"
"v_mfma_f32_16x16x32_bf16 v[100:103], a[200:203], v[120:123], v[100:103]\n"
"ds_read_b128 a[192:195], v35 offset:36864\n"
"v_mfma_f32_16x16x32_bf16 v[116:119], a[200:203], v[152:155], v[116:119]\n"
"v_mfma_f32_16x16x32_bf16 v[132:135], a[200:203], v[184:187], v[132:135]\n"
"buffer_load_dwordx4 v94, s[48:51], s65 offen offset:3072 nt lds\n"
"v_mfma_f32_16x16x32_bf16 v[148:151], a[200:203], v[216:219], v[148:151]\n"
"s_waitcnt lgkmcnt(3)\n"
"v_mfma_f32_16x16x32_bf16 v[108:111], a[180:183], v[96:99], v[40:43]\n"
"v_mfma_f32_16x16x32_bf16 v[124:127], a[180:183], v[128:131], v[44:47]\n"
"ds_read_b64_tr_b16 a[204:205], v36 offset:32768\n"
"v_mfma_f32_16x16x32_bf16 v[140:143], a[180:183], v[160:163], v[48:51]\n"
"ds_read_b64_tr_b16 a[206:207], v36 offset:36864\n"
"v_mfma_f32_16x16x32_bf16 v[156:159], a[180:183], v[192:195], v[52:55]\n"
"s_waitcnt lgkmcnt(4)\n"
"v_mfma_f32_16x16x32_bf16 v[108:111], a[184:187], v[104:107], v[108:111]\n"
"v_exp_f32 v100, v100\n"
"v_mfma_f32_16x16x32_bf16 v[124:127], a[184:187], v[136:139], v[124:127]\n"
"ds_read_b64_tr_b16 a[210:211], v37 offset:36864\n"
"v_exp_f32 v101, v101\n"
"v_mfma_f32_16x16x32_bf16 v[140:143], a[184:187], v[168:171], v[140:143]\n"
"ds_read_b64_tr_b16 a[208:209], v37 offset:32768\n"
"v_exp_f32 v102, v102\n"
"v_mfma_f32_16x16x32_bf16 v[156:159], a[184:187], v[200:203], v[156:159]\n"
"s_waitcnt lgkmcnt(5)\n"
"v_exp_f32 v103, v103\n"
"v_mfma_f32_16x16x32_bf16 v[108:111], a[188:191], v[112:115], v[108:111]\n"
"v_cvt_pk_bf16_f32 v212, v100, v101\n"
"v_cvt_pk_bf16_f32 v213, v102, v103\n"
"v_mfma_f32_16x16x32_bf16 v[124:127], a[188:191], v[144:147], v[124:127]\n"
"v_exp_f32 v116, v116\n"
"v_mfma_f32_16x16x32_bf16 v[140:143], a[188:191], v[176:179], v[140:143]\n"
"ds_read_b64_tr_b16 a[212:213], v36 offset:33280\n"
"v_exp_f32 v117, v117\n"
"v_mfma_f32_16x16x32_bf16 v[156:159], a[188:191], v[208:211], v[156:159]\n"
"s_waitcnt lgkmcnt(5)\n"
"v_exp_f32 v118, v118\n"
"v_mfma_f32_16x16x32_bf16 v[108:111], a[192:195], v[120:123], v[108:111]\n"
"ds_read_b64_tr_b16 a[214:215], v36 offset:37376\n"
"v_exp_f32 v119, v119\n"
"v_mfma_f32_16x16x32_bf16 v[124:127], a[192:195], v[152:155], v[124:127]\n"
"v_cvt_pk_bf16_f32 v220, v116, v117\n"
"v_cvt_pk_bf16_f32 v221, v118, v119\n"
"v_mfma_f32_16x16x32_bf16 v[140:143], a[192:195], v[184:187], v[140:143]\n"
"v_exp_f32 v132, v132\n"
"v_mfma_f32_16x16x32_bf16 v[156:159], a[192:195], v[216:219], v[156:159]\n"
"s_waitcnt vmcnt(13)\n"
"s_barrier\n"
"v_exp_f32 v133, v133\n"
"v_mfma_f32_16x16x32_bf16 a[160:163], v[60:63], v[180:183], a[160:163]\n"
"ds_read_b64_tr_b16 a[216:217], v37 offset:33280\n"
"v_exp_f32 v134, v134\n"
"v_mfma_f32_16x16x32_bf16 a[164:167], v[60:63], v[188:191], a[164:167]\n"
"ds_read_b64_tr_b16 a[218:219], v37 offset:37376\n"
"v_exp_f32 v135, v135\n"
"v_mfma_f32_16x16x32_bf16 a[168:171], v[60:63], v[196:199], a[168:171]\n"
"v_cvt_pk_bf16_f32 v228, v132, v133\n"
"v_cvt_pk_bf16_f32 v229, v134, v135\n"
"v_mfma_f32_16x16x32_bf16 a[172:175], v[60:63], v[204:207], a[172:175]\n"
"v_exp_f32 v148, v148\n"
"v_mfma_f32_16x16x32_bf16 a[176:179], v[60:63], v[244:247], a[176:179]\n"
"s_waitcnt lgkmcnt(6)\n"
"v_exp_f32 v149, v149\n"
"v_mfma_f32_16x16x32_bf16 a[0:3], a[204:207], v[180:183], a[0:3]\n"
"ds_read_b64_tr_b16 a[220:221], v36 offset:33792\n"
"v_exp_f32 v150, v150\n"
"v_mfma_f32_16x16x32_bf16 a[32:35], a[204:207], v[188:191], a[32:35]\n"
"ds_read_b64_tr_b16 a[222:223], v36 offset:37888\n"
"v_exp_f32 v151, v151\n"
"v_mfma_f32_16x16x32_bf16 a[64:67], a[204:207], v[196:199], a[64:67]\n"
"v_cvt_pk_bf16_f32 v236, v148, v149\n"
"v_cvt_pk_bf16_f32 v237, v150, v151\n"
"v_mfma_f32_16x16x32_bf16 a[96:99], a[204:207], v[204:207], a[96:99]\n"
"v_exp_f32 v108, v108\n"
"v_mfma_f32_16x16x32_bf16 a[128:131], a[204:207], v[244:247], a[128:131]\n"
"s_waitcnt lgkmcnt(6)\n"
"v_exp_f32 v109, v109\n"
"v_mfma_f32_16x16x32_bf16 a[4:7], a[208:211], v[180:183], a[4:7]\n"
"ds_read_b64_tr_b16 a[224:225], v37 offset:33792\n"
"v_exp_f32 v110, v110\n"
"v_mfma_f32_16x16x32_bf16 a[36:39], a[208:211], v[188:191], a[36:39]\n"
"ds_read_b64_tr_b16 a[226:227], v37 offset:37888\n"
"v_exp_f32 v111, v111\n"
"v_mfma_f32_16x16x32_bf16 a[68:71], a[208:211], v[196:199], a[68:71]\n"
"v_cvt_pk_bf16_f32 v214, v108, v109\n"
"v_cvt_pk_bf16_f32 v215, v110, v111\n"
"v_mfma_f32_16x16x32_bf16 a[100:103], a[208:211], v[204:207], a[100:103]\n"
"v_exp_f32 v124, v124\n"
"v_mfma_f32_16x16x32_bf16 a[132:135], a[208:211], v[244:247], a[132:135]\n"
"s_waitcnt lgkmcnt(6)\n"
"v_exp_f32 v125, v125\n"
"v_mfma_f32_16x16x32_bf16 a[8:11], a[212:215], v[180:183], a[8:11]\n"
"ds_read_b64_tr_b16 a[204:205], v36 offset:34304\n"
"v_exp_f32 v126, v126\n"
"v_mfma_f32_16x16x32_bf16 a[40:43], a[212:215], v[188:191], a[40:43]\n"
"ds_read_b64_tr_b16 a[206:207], v36 offset:38400\n"
"v_exp_f32 v127, v127\n"
"v_mfma_f32_16x16x32_bf16 a[72:75], a[212:215], v[196:199], a[72:75]\n"
"v_cvt_pk_bf16_f32 v222, v124, v125\n"
"v_cvt_pk_bf16_f32 v223, v126, v127\n"
"v_mfma_f32_16x16x32_bf16 a[104:107], a[212:215], v[204:207], a[104:107]\n"
"v_exp_f32 v140, v140\n"
"v_mfma_f32_16x16x32_bf16 a[136:139], a[212:215], v[244:247], a[136:139]\n"
"s_waitcnt lgkmcnt(6)\n"
"v_exp_f32 v141, v141\n"
"v_mfma_f32_16x16x32_bf16 a[12:15], a[216:219], v[180:183], a[12:15]\n"
"ds_read_b64_tr_b16 a[208:209], v37 offset:34304\n"
"v_exp_f32 v142, v142\n"
"v_mfma_f32_16x16x32_bf16 a[44:47], a[216:219], v[188:191], a[44:47]\n"
"ds_read_b64_tr_b16 a[210:211], v37 offset:38400\n"
"v_exp_f32 v143, v143\n"
"v_mfma_f32_16x16x32_bf16 a[76:79], a[216:219], v[196:199], a[76:79]\n"
"v_cvt_pk_bf16_f32 v230, v140, v141\n"
"v_cvt_pk_bf16_f32 v231, v142, v143\n"
"v_mfma_f32_16x16x32_bf16 a[108:111], a[216:219], v[204:207], a[108:111]\n"
"v_exp_f32 v156, v156\n"
"v_mfma_f32_16x16x32_bf16 a[140:143], a[216:219], v[244:247], a[140:143]\n"
"s_waitcnt lgkmcnt(6)\n"
"v_exp_f32 v157, v157\n"
"v_mfma_f32_16x16x32_bf16 a[16:19], a[220:223], v[180:183], a[16:19]\n"
"ds_read_b128 a[196:199], v28 offset:49152\n"
"v_exp_f32 v158, v158\n"
"v_mfma_f32_16x16x32_bf16 a[48:51], a[220:223], v[188:191], a[48:51]\n"
"v_exp_f32 v159, v159\n"
"v_mfma_f32_16x16x32_bf16 a[80:83], a[220:223], v[196:199], a[80:83]\n"
"v_cvt_pk_bf16_f32 v238, v156, v157\n"
"v_cvt_pk_bf16_f32 v239, v158, v159\n"
"v_mfma_f32_16x16x32_bf16 a[112:115], a[220:223], v[204:207], a[112:115]\n"
"v_mfma_f32_16x16x32_bf16 a[144:147], a[220:223], v[244:247], a[144:147]\n"
"s_waitcnt lgkmcnt(5)\n"
"v_mfma_f32_16x16x32_bf16 a[20:23], a[224:227], v[180:183], a[20:23]\n"
"ds_read_b128 a[200:203], v29 offset:49152\n"
"v_mfma_f32_16x16x32_bf16 a[52:55], a[224:227], v[188:191], a[52:55]\n"
"v_mfma_f32_16x16x32_bf16 a[84:87], a[224:227], v[196:199], a[84:87]\n"
"v_mfma_f32_16x16x32_bf16 a[116:119], a[224:227], v[204:207], a[116:119]\n"
"v_mfma_f32_16x16x32_bf16 a[148:151], a[224:227], v[244:247], a[148:151]\n"
"s_waitcnt lgkmcnt(4)\n"
"v_mfma_f32_16x16x32_bf16 a[24:27], a[204:207], v[180:183], a[24:27]\n"
"ds_read_b128 a[180:183], v30 offset:49152\n"
"v_mfma_f32_16x16x32_bf16 a[56:59], a[204:207], v[188:191], a[56:59]\n"
"v_mfma_f32_16x16x32_bf16 a[88:91], a[204:207], v[196:199], a[88:91]\n"
"v_mfma_f32_16x16x32_bf16 a[120:123], a[204:207], v[204:207], a[120:123]\n"
"v_mfma_f32_16x16x32_bf16 a[152:155], a[204:207], v[244:247], a[152:155]\n"
"s_waitcnt lgkmcnt(3)\n"
"v_mfma_f32_16x16x32_bf16 a[28:31], a[208:211], v[180:183], a[28:31]\n"
"ds_read_b128 a[184:187], v31 offset:49152\n"
"v_mfma_f32_16x16x32_bf16 a[60:63], a[208:211], v[188:191], a[60:63]\n"
"v_mfma_f32_16x16x32_bf16 a[92:95], a[208:211], v[196:199], a[92:95]\n"
"v_mfma_f32_16x16x32_bf16 a[124:127], a[208:211], v[204:207], a[124:127]\n"
"v_mfma_f32_16x16x32_bf16 a[156:159], a[208:211], v[244:247], a[156:159]\n"
"s_waitcnt lgkmcnt(3)\n"
"v_mfma_f32_16x16x32_bf16 v[100:103], a[196:199], v[96:99], v[40:43]\n"
"ds_read_b128 a[188:191], v28 offset:53248\n"
"v_mfma_f32_16x16x32_bf16 v[116:119], a[196:199], v[128:131], v[44:47]\n"
"s_add_u32 m0, s61, 81920\n"
"s_mov_b32 s66, 147456\n"
"v_mfma_f32_16x16x32_bf16 v[132:135], a[196:199], v[160:163], v[48:51]\n"
"buffer_load_dwordx4 v95, s[52:55], s66 offen nt lds\n"
"v_mfma_f32_16x16x32_bf16 v[148:151], a[196:199], v[192:195], v[52:55]\n"
"v_mfma_f32_16x16x32_bf16 v[164:167], a[196:199], v[224:227], v[56:59]\n"
"s_waitcnt lgkmcnt(3)\n"
"v_mfma_f32_16x16x32_bf16 v[100:103], a[200:203], v[104:107], v[100:103]\n"
"ds_read_b128 a[192:195], v29 offset:53248\n"
"v_mfma_f32_16x16x32_bf16 v[116:119], a[200:203], v[136:139], v[116:119]\n"
"buffer_load_dwordx4 v27, s[52:55], s66 offen offset:1024 nt lds\n"
"v_mfma_f32_16x16x32_bf16 v[132:135], a[200:203], v[168:171], v[132:135]\n"
"v_mfma_f32_16x16x32_bf16 v[148:151], a[200:203], v[200:203], v[148:151]\n"
"v_mfma_f32_16x16x32_bf16 v[164:167], a[200:203], v[232:235], v[164:167]\n"
"s_waitcnt lgkmcnt(3)\n"
"v_mfma_f32_16x16x32_bf16 v[100:103], a[180:183], v[112:115], v[100:103]\n"
"ds_read_b128 a[196:199], v30 offset:53248\n"
"v_mfma_f32_16x16x32_bf16 v[116:119], a[180:183], v[144:147], v[116:119]\n"
"buffer_load_dwordx4 v95, s[52:55], s66 offen offset:2048 nt lds\n"
"v_mfma_f32_16x16x32_bf16 v[132:135], a[180:183], v[176:179], v[132:135]\n"
"v_mfma_f32_16x16x32_bf16 v[148:151], a[180:183], v[208:211], v[148:151]\n"
"v_mfma_f32_16x16x32_bf16 v[164:167], a[180:183], v[240:243], v[164:167]\n"
"s_waitcnt lgkmcnt(3)\n"
"v_mfma_f32_16x16x32_bf16 v[100:103], a[184:187], v[120:123], v[100:103]\n"
"ds_read_b128 a[200:203], v31 offset:53248\n"
"v_mfma_f32_16x16x32_bf16 v[116:119], a[184:187], v[152:155], v[116:119]\n"
"buffer_load_dwordx4 v27, s[52:55], s66 offen offset:3072 nt lds\n"
"v_mfma_f32_16x16x32_bf16 v[132:135], a[184:187], v[184:187], v[132:135]\n"
"v_mfma_f32_16x16x32_bf16 v[148:151], a[184:187], v[216:219], v[148:151]\n"
"v_mfma_f32_16x16x32_bf16 v[164:167], a[184:187], v[248:251], v[164:167]\n"
"s_waitcnt lgkmcnt(3)\n"
"v_mfma_f32_16x16x32_bf16 v[108:111], a[188:191], v[96:99], v[40:43]\n"
"v_mfma_f32_16x16x32_bf16 v[124:127], a[188:191], v[128:131], v[44:47]\n"
"v_mfma_f32_16x16x32_bf16 v[140:143], a[188:191], v[160:163], v[48:51]\n"
"v_mfma_f32_16x16x32_bf16 v[156:159], a[188:191], v[192:195], v[52:55]\n"
"v_exp_f32 v100, v100\n"
"v_mfma_f32_16x16x32_bf16 v[172:175], a[188:191], v[224:227], v[56:59]\n"
"s_waitcnt lgkmcnt(2)\n"
"v_exp_f32 v101, v101\n"
"v_mfma_f32_16x16x32_bf16 v[108:111], a[192:195], v[104:107], v[108:111]\n"
"v_exp_f32 v102, v102\n"
"v_mfma_f32_16x16x32_bf16 v[124:127], a[192:195], v[136:139], v[124:127]\n"
"v_exp_f32 v103, v103\n"
"v_mfma_f32_16x16x32_bf16 v[140:143], a[192:195], v[168:171], v[140:143]\n"
"ds_read_b64_tr_b16 a[214:215], v38 offset:36864\n"
"v_cvt_pk_bf16_f32 v180, v100, v101\n"
"v_cvt_pk_bf16_f32 v181, v102, v103\n"
"v_mfma_f32_16x16x32_bf16 v[156:159], a[192:195], v[200:203], v[156:159]\n"
"ds_read_b64_tr_b16 a[212:213], v38 offset:32768\n"
"v_exp_f32 v116, v116\n"
"v_mfma_f32_16x16x32_bf16 v[172:175], a[192:195], v[232:235], v[172:175]\n"
"s_waitcnt lgkmcnt(3)\n"
"v_exp_f32 v117, v117\n"
"v_mfma_f32_16x16x32_bf16 v[108:111], a[196:199], v[112:115], v[108:111]\n"
"v_exp_f32 v118, v118\n"
"v_mfma_f32_16x16x32_bf16 v[124:127], a[196:199], v[144:147], v[124:127]\n"
"v_exp_f32 v119, v119\n"
"v_mfma_f32_16x16x32_bf16 v[140:143], a[196:199], v[176:179], v[140:143]\n"
"ds_read_b64_tr_b16 a[216:217], v39 offset:32768\n"
"v_cvt_pk_bf16_f32 v188, v116, v117\n"
"v_cvt_pk_bf16_f32 v189, v118, v119\n"
"v_mfma_f32_16x16x32_bf16 v[156:159], a[196:199], v[208:211], v[156:159]\n"
"ds_read_b64_tr_b16 a[218:219], v39 offset:36864\n"
"v_exp_f32 v132, v132\n"
"v_mfma_f32_16x16x32_bf16 v[172:175], a[196:199], v[240:243], v[172:175]\n"
"s_waitcnt lgkmcnt(4)\n"
"v_exp_f32 v133, v133\n"
"v_mfma_f32_16x16x32_bf16 v[108:111], a[200:203], v[120:123], v[108:111]\n"
"v_exp_f32 v134, v134\n"
"v_mfma_f32_16x16x32_bf16 v[124:127], a[200:203], v[152:155], v[124:127]\n"
"ds_read_b64_tr_b16 a[220:221], v38 offset:33280\n"
"v_exp_f32 v135, v135\n"
"v_mfma_f32_16x16x32_bf16 v[140:143], a[200:203], v[184:187], v[140:143]\n"
"ds_read_b64_tr_b16 a[222:223], v38 offset:37376\n"
"v_cvt_pk_bf16_f32 v196, v132, v133\n"
"v_cvt_pk_bf16_f32 v197, v134, v135\n"
"v_mfma_f32_16x16x32_bf16 v[156:159], a[200:203], v[216:219], v[156:159]\n"
"v_exp_f32 v148, v148\n"
"v_mfma_f32_16x16x32_bf16 v[172:175], a[200:203], v[248:251], v[172:175]\n"
"v_exp_f32 v149, v149\n"
"v_mfma_f32_16x16x32_bf16 a[160:163], v[60:63], v[212:215], a[160:163]\n"
"ds_read_b64_tr_b16 a[224:225], v39 offset:33280\n"
"v_exp_f32 v150, v150\n"
"v_mfma_f32_16x16x32_bf16 a[164:167], v[60:63], v[220:223], a[164:167]\n"
"ds_read_b64_tr_b16 a[226:227], v39 offset:37376\n"
"v_exp_f32 v151, v151\n"
"v_mfma_f32_16x16x32_bf16 a[168:171], v[60:63], v[228:231], a[168:171]\n"
"v_cvt_pk_bf16_f32 v204, v148, v149\n"
"v_cvt_pk_bf16_f32 v205, v150, v151\n"
"v_mfma_f32_16x16x32_bf16 a[172:175], v[60:63], v[236:239], a[172:175]\n"
"s_waitcnt lgkmcnt(6)\n"
"v_exp_f32 v164, v164\n"
"v_mfma_f32_16x16x32_bf16 a[0:3], a[212:215], v[212:215], a[0:3]\n"
"ds_read_b64_tr_b16 a[204:205], v38 offset:33792\n"
"v_exp_f32 v165, v165\n"
"v_mfma_f32_16x16x32_bf16 a[32:35], a[212:215], v[220:223], a[32:35]\n"
"ds_read_b64_tr_b16 a[206:207], v38 offset:37888\n"
"v_exp_f32 v166, v166\n"
"v_mfma_f32_16x16x32_bf16 a[64:67], a[212:215], v[228:231], a[64:67]\n"
"v_exp_f32 v167, v167\n"
"v_mfma_f32_16x16x32_bf16 a[96:99], a[212:215], v[236:239], a[96:99]\n"
"s_waitcnt lgkmcnt(6)\n"
"v_cvt_pk_bf16_f32 v244, v164, v165\n"
"v_cvt_pk_bf16_f32 v245, v166, v167\n"
"v_mfma_f32_16x16x32_bf16 a[4:7], a[216:219], v[212:215], a[4:7]\n"
"ds_read_b64_tr_b16 a[208:209], v39 offset:33792\n"
"v_exp_f32 v108, v108\n"
"v_mfma_f32_16x16x32_bf16 a[36:39], a[216:219], v[220:223], a[36:39]\n"
"ds_read_b64_tr_b16 a[210:211], v39 offset:37888\n"
"v_exp_f32 v109, v109\n"
"v_mfma_f32_16x16x32_bf16 a[68:71], a[216:219], v[228:231], a[68:71]\n"
"v_exp_f32 v110, v110\n"
"v_mfma_f32_16x16x32_bf16 a[100:103], a[216:219], v[236:239], a[100:103]\n"
"s_waitcnt lgkmcnt(6)\n"
"v_exp_f32 v111, v111\n"
"v_mfma_f32_16x16x32_bf16 a[8:11], a[220:223], v[212:215], a[8:11]\n"
"ds_read_b64_tr_b16 a[212:213], v38 offset:34304\n"
"v_cvt_pk_bf16_f32 v182, v108, v109\n"
"v_cvt_pk_bf16_f32 v183, v110, v111\n"
"v_mfma_f32_16x16x32_bf16 a[40:43], a[220:223], v[220:223], a[40:43]\n"
"ds_read_b64_tr_b16 a[214:215], v38 offset:38400\n"
"v_exp_f32 v124, v124\n"
"v_mfma_f32_16x16x32_bf16 a[72:75], a[220:223], v[228:231], a[72:75]\n"
"v_exp_f32 v125, v125\n"
"v_mfma_f32_16x16x32_bf16 a[104:107], a[220:223], v[236:239], a[104:107]\n"
"s_waitcnt lgkmcnt(6)\n"
"v_exp_f32 v126, v126\n"
"v_mfma_f32_16x16x32_bf16 a[12:15], a[224:227], v[212:215], a[12:15]\n"
"ds_read_b64_tr_b16 a[216:217], v39 offset:34304\n"
"v_exp_f32 v127, v127\n"
"v_mfma_f32_16x16x32_bf16 a[44:47], a[224:227], v[220:223], a[44:47]\n"
"ds_read_b64_tr_b16 a[218:219], v39 offset:38400\n"
"v_cvt_pk_bf16_f32 v190, v124, v125\n"
"v_cvt_pk_bf16_f32 v191, v126, v127\n"
"v_mfma_f32_16x16x32_bf16 a[76:79], a[224:227], v[228:231], a[76:79]\n"
"v_exp_f32 v140, v140\n"
"v_mfma_f32_16x16x32_bf16 a[108:111], a[224:227], v[236:239], a[108:111]\n"
"s_waitcnt lgkmcnt(6)\n"
"v_exp_f32 v141, v141\n"
"v_mfma_f32_16x16x32_bf16 a[16:19], a[204:207], v[212:215], a[16:19]\n"
"ds_read_b128 a[180:183], v32 offset:49152\n"
"v_exp_f32 v142, v142\n"
"v_mfma_f32_16x16x32_bf16 a[48:51], a[204:207], v[220:223], a[48:51]\n"
"v_exp_f32 v143, v143\n"
"v_mfma_f32_16x16x32_bf16 a[80:83], a[204:207], v[228:231], a[80:83]\n"
"v_cvt_pk_bf16_f32 v198, v140, v141\n"
"v_cvt_pk_bf16_f32 v199, v142, v143\n"
"v_mfma_f32_16x16x32_bf16 a[112:115], a[204:207], v[236:239], a[112:115]\n"
"s_waitcnt lgkmcnt(5)\n"
"v_exp_f32 v156, v156\n"
"v_mfma_f32_16x16x32_bf16 a[20:23], a[208:211], v[212:215], a[20:23]\n"
"ds_read_b128 a[184:187], v33 offset:49152\n"
"v_exp_f32 v157, v157\n"
"v_mfma_f32_16x16x32_bf16 a[52:55], a[208:211], v[220:223], a[52:55]\n"
"v_exp_f32 v158, v158\n"
"v_mfma_f32_16x16x32_bf16 a[84:87], a[208:211], v[228:231], a[84:87]\n"
"v_exp_f32 v159, v159\n"
"v_mfma_f32_16x16x32_bf16 a[116:119], a[208:211], v[236:239], a[116:119]\n"
"s_waitcnt lgkmcnt(4)\n"
"v_cvt_pk_bf16_f32 v206, v156, v157\n"
"v_cvt_pk_bf16_f32 v207, v158, v159\n"
"v_mfma_f32_16x16x32_bf16 a[24:27], a[212:215], v[212:215], a[24:27]\n"
"ds_read_b128 a[188:191], v34 offset:49152\n"
"v_exp_f32 v172, v172\n"
"v_mfma_f32_16x16x32_bf16 a[56:59], a[212:215], v[220:223], a[56:59]\n"
"v_exp_f32 v173, v173\n"
"v_mfma_f32_16x16x32_bf16 a[88:91], a[212:215], v[228:231], a[88:91]\n"
"v_exp_f32 v174, v174\n"
"v_mfma_f32_16x16x32_bf16 a[120:123], a[212:215], v[236:239], a[120:123]\n"
"s_waitcnt lgkmcnt(3)\n"
"v_exp_f32 v175, v175\n"
"v_mfma_f32_16x16x32_bf16 a[28:31], a[216:219], v[212:215], a[28:31]\n"
"ds_read_b128 a[192:195], v35 offset:49152\n"
"v_cvt_pk_bf16_f32 v246, v172, v173\n"
"v_cvt_pk_bf16_f32 v247, v174, v175\n"
"v_mfma_f32_16x16x32_bf16 a[60:63], a[216:219], v[220:223], a[60:63]\n"
"buffer_load_dword a229, v26, s[92:95], s76 offen\n"
"v_mfma_f32_16x16x32_bf16 a[92:95], a[216:219], v[228:231], a[92:95]\n"
"s_add_u32 s76, s76, 4096\n"
"v_mfma_f32_16x16x32_bf16 a[124:127], a[216:219], v[236:239], a[124:127]\n"
"s_waitcnt lgkmcnt(3)\n"
"v_mfma_f32_16x16x32_bf16 v[100:103], a[180:183], v[96:99], v[40:43]\n"
"ds_read_b128 a[196:199], v32 offset:53248\n"
"v_mfma_f32_16x16x32_bf16 v[116:119], a[180:183], v[128:131], v[44:47]\n"
"s_add_u32 m0, s61, 32768\n"
"s_mov_b32 s65, 163840\n"
"v_mfma_f32_16x16x32_bf16 v[132:135], a[180:183], v[160:163], v[48:51]\n"
"buffer_load_dwordx4 v24, s[48:51], s65 offen nt lds\n"
"v_mfma_f32_16x16x32_bf16 v[148:151], a[180:183], v[192:195], v[52:55]\n"
"s_waitcnt lgkmcnt(3)\n"
"v_mfma_f32_16x16x32_bf16 v[100:103], a[184:187], v[104:107], v[100:103]\n"
"ds_read_b128 a[200:203], v33 offset:53248\n"
"v_mfma_f32_16x16x32_bf16 v[116:119], a[184:187], v[136:139], v[116:119]\n"
"v_mfma_f32_16x16x32_bf16 v[132:135], a[184:187], v[168:171], v[132:135]\n"
"buffer_load_dwordx4 v25, s[48:51], s65 offen offset:1024 nt lds\n"
"v_mfma_f32_16x16x32_bf16 v[148:151], a[184:187], v[200:203], v[148:151]\n"
"s_waitcnt lgkmcnt(3)\n"
"v_mfma_f32_16x16x32_bf16 v[100:103], a[188:191], v[112:115], v[100:103]\n"
"ds_read_b128 a[180:183], v34 offset:53248\n"
"v_mfma_f32_16x16x32_bf16 v[116:119], a[188:191], v[144:147], v[116:119]\n"
"v_mfma_f32_16x16x32_bf16 v[132:135], a[188:191], v[176:179], v[132:135]\n"
"buffer_load_dwordx4 v93, s[48:51], s65 offen offset:2048 nt lds\n"
"v_mfma_f32_16x16x32_bf16 v[148:151], a[188:191], v[208:211], v[148:151]\n"
"s_waitcnt lgkmcnt(3)\n"
"v_mfma_f32_16x16x32_bf16 v[100:103], a[192:195], v[120:123], v[100:103]\n"
"ds_read_b128 a[184:187], v35 offset:53248\n"
"v_mfma_f32_16x16x32_bf16 v[116:119], a[192:195], v[152:155], v[116:119]\n"
"v_mfma_f32_16x16x32_bf16 v[132:135], a[192:195], v[184:187], v[132:135]\n"
"buffer_load_dwordx4 v94, s[48:51], s65 offen offset:3072 nt lds\n"
"v_mfma_f32_16x16x32_bf16 v[148:151], a[192:195], v[216:219], v[148:151]\n"
"s_waitcnt lgkmcnt(3)\n"
"v_mfma_f32_16x16x32_bf16 v[108:111], a[196:199], v[96:99], v[40:43]\n"
"v_mfma_f32_16x16x32_bf16 v[124:127], a[196:199], v[128:131], v[44:47]\n"
"ds_read_b64_tr_b16 a[220:221], v36 offset:49152\n"
"v_mfma_f32_16x16x32_bf16 v[140:143], a[196:199], v[160:163], v[48:51]\n"
"ds_read_b64_tr_b16 a[222:223], v36 offset:53248\n"
"v_mfma_f32_16x16x32_bf16 v[156:159], a[196:199], v[192:195], v[52:55]\n"
"s_waitcnt lgkmcnt(4)\n"
"v_mfma_f32_16x16x32_bf16 v[108:111], a[200:203], v[104:107], v[108:111]\n"
"v_exp_f32 v100, v100\n"
"v_mfma_f32_16x16x32_bf16 v[124:127], a[200:203], v[136:139], v[124:127]\n"
"ds_read_b64_tr_b16 a[226:227], v37 offset:53248\n"
"v_exp_f32 v101, v101\n"
"v_mfma_f32_16x16x32_bf16 v[140:143], a[200:203], v[168:171], v[140:143]\n"
"ds_read_b64_tr_b16 a[224:225], v37 offset:49152\n"
"v_exp_f32 v102, v102\n"
"v_mfma_f32_16x16x32_bf16 v[156:159], a[200:203], v[200:203], v[156:159]\n"
"s_waitcnt lgkmcnt(5)\n"
"v_exp_f32 v103, v103\n"
"v_mfma_f32_16x16x32_bf16 v[108:111], a[180:183], v[112:115], v[108:111]\n"
"v_cvt_pk_bf16_f32 v212, v100, v101\n"
"v_cvt_pk_bf16_f32 v213, v102, v103\n"
"v_mfma_f32_16x16x32_bf16 v[124:127], a[180:183], v[144:147], v[124:127]\n"
"v_exp_f32 v116, v116\n"
"v_mfma_f32_16x16x32_bf16 v[140:143], a[180:183], v[176:179], v[140:143]\n"
"ds_read_b64_tr_b16 a[204:205], v36 offset:49664\n"
"v_exp_f32 v117, v117\n"
"v_mfma_f32_16x16x32_bf16 v[156:159], a[180:183], v[208:211], v[156:159]\n"
"s_waitcnt lgkmcnt(5)\n"
"v_exp_f32 v118, v118\n"
"v_mfma_f32_16x16x32_bf16 v[108:111], a[184:187], v[120:123], v[108:111]\n"
"ds_read_b64_tr_b16 a[206:207], v36 offset:53760\n"
"v_exp_f32 v119, v119\n"
"v_mfma_f32_16x16x32_bf16 v[124:127], a[184:187], v[152:155], v[124:127]\n"
"v_cvt_pk_bf16_f32 v220, v116, v117\n"
"v_cvt_pk_bf16_f32 v221, v118, v119\n"
"v_mfma_f32_16x16x32_bf16 v[140:143], a[184:187], v[184:187], v[140:143]\n"
"v_exp_f32 v132, v132\n"
"v_mfma_f32_16x16x32_bf16 v[156:159], a[184:187], v[216:219], v[156:159]\n"
"s_waitcnt vmcnt(14)\n"
"s_barrier\n"
"v_exp_f32 v133, v133\n"
"v_mfma_f32_16x16x32_bf16 a[160:163], v[60:63], v[180:183], a[160:163]\n"
"ds_read_b64_tr_b16 a[208:209], v37 offset:49664\n"
"v_exp_f32 v134, v134\n"
"v_mfma_f32_16x16x32_bf16 a[164:167], v[60:63], v[188:191], a[164:167]\n"
"ds_read_b64_tr_b16 a[210:211], v37 offset:53760\n"
"v_exp_f32 v135, v135\n"
"v_mfma_f32_16x16x32_bf16 a[168:171], v[60:63], v[196:199], a[168:171]\n"
"v_cvt_pk_bf16_f32 v228, v132, v133\n"
"v_cvt_pk_bf16_f32 v229, v134, v135\n"
"v_mfma_f32_16x16x32_bf16 a[172:175], v[60:63], v[204:207], a[172:175]\n"
"v_exp_f32 v148, v148\n"
"v_mfma_f32_16x16x32_bf16 a[176:179], v[60:63], v[244:247], a[176:179]\n"
"s_waitcnt lgkmcnt(6)\n"
"v_exp_f32 v149, v149\n"
"v_mfma_f32_16x16x32_bf16 a[0:3], a[220:223], v[180:183], a[0:3]\n"
"ds_read_b64_tr_b16 a[212:213], v36 offset:50176\n"
"v_exp_f32 v150, v150\n"
"v_mfma_f32_16x16x32_bf16 a[32:35], a[220:223], v[188:191], a[32:35]\n"
"ds_read_b64_tr_b16 a[214:215], v36 offset:54272\n"
"v_exp_f32 v151, v151\n"
"v_mfma_f32_16x16x32_bf16 a[64:67], a[220:223], v[196:199], a[64:67]\n"
"v_cvt_pk_bf16_f32 v236, v148, v149\n"
"v_cvt_pk_bf16_f32 v237, v150, v151\n"
"v_mfma_f32_16x16x32_bf16 a[96:99], a[220:223], v[204:207], a[96:99]\n"
"v_exp_f32 v108, v108\n"
"v_mfma_f32_16x16x32_bf16 a[128:131], a[220:223], v[244:247], a[128:131]\n"
"s_waitcnt lgkmcnt(6)\n"
"v_exp_f32 v109, v109\n"
"v_mfma_f32_16x16x32_bf16 a[4:7], a[224:227], v[180:183], a[4:7]\n"
"ds_read_b64_tr_b16 a[216:217], v37 offset:50176\n"
"v_exp_f32 v110, v110\n"
"v_mfma_f32_16x16x32_bf16 a[36:39], a[224:227], v[188:191], a[36:39]\n"
"ds_read_b64_tr_b16 a[218:219], v37 offset:54272\n"
"v_exp_f32 v111, v111\n"
"v_mfma_f32_16x16x32_bf16 a[68:71], a[224:227], v[196:199], a[68:71]\n"
"v_cvt_pk_bf16_f32 v214, v108, v109\n"
"v_cvt_pk_bf16_f32 v215, v110, v111\n"
"v_mfma_f32_16x16x32_bf16 a[100:103], a[224:227], v[204:207], a[100:103]\n"
"v_exp_f32 v124, v124\n"
"v_mfma_f32_16x16x32_bf16 a[132:135], a[224:227], v[244:247], a[132:135]\n"
"s_waitcnt lgkmcnt(6)\n"
"v_exp_f32 v125, v125\n"
"v_mfma_f32_16x16x32_bf16 a[8:11], a[204:207], v[180:183], a[8:11]\n"
"ds_read_b64_tr_b16 a[220:221], v36 offset:50688\n"
"v_exp_f32 v126, v126\n"
"v_mfma_f32_16x16x32_bf16 a[40:43], a[204:207], v[188:191], a[40:43]\n"
"ds_read_b64_tr_b16 a[222:223], v36 offset:54784\n"
"v_exp_f32 v127, v127\n"
"v_mfma_f32_16x16x32_bf16 a[72:75], a[204:207], v[196:199], a[72:75]\n"
"v_cvt_pk_bf16_f32 v222, v124, v125\n"
"v_cvt_pk_bf16_f32 v223, v126, v127\n"
"v_mfma_f32_16x16x32_bf16 a[104:107], a[204:207], v[204:207], a[104:107]\n"
"v_exp_f32 v140, v140\n"
"v_mfma_f32_16x16x32_bf16 a[136:139], a[204:207], v[244:247], a[136:139]\n"
"s_waitcnt lgkmcnt(6)\n"
"v_exp_f32 v141, v141\n"
"v_mfma_f32_16x16x32_bf16 a[12:15], a[208:211], v[180:183], a[12:15]\n"
"ds_read_b64_tr_b16 a[224:225], v37 offset:50688\n"
"v_exp_f32 v142, v142\n"
"v_mfma_f32_16x16x32_bf16 a[44:47], a[208:211], v[188:191], a[44:47]\n"
"ds_read_b64_tr_b16 a[226:227], v37 offset:54784\n"
"v_exp_f32 v143, v143\n"
"v_mfma_f32_16x16x32_bf16 a[76:79], a[208:211], v[196:199], a[76:79]\n"
"v_cvt_pk_bf16_f32 v230, v140, v141\n"
"v_cvt_pk_bf16_f32 v231, v142, v143\n"
"v_mfma_f32_16x16x32_bf16 a[108:111], a[208:211], v[204:207], a[108:111]\n"
"v_exp_f32 v156, v156\n"
"v_mfma_f32_16x16x32_bf16 a[140:143], a[208:211], v[244:247], a[140:143]\n"
"s_waitcnt lgkmcnt(6)\n"
"v_exp_f32 v157, v157\n"
"v_mfma_f32_16x16x32_bf16 a[16:19], a[212:215], v[180:183], a[16:19]\n"
"ds_read_b128 a[188:191], v28 offset:0\n"
"v_exp_f32 v158, v158\n"
"v_mfma_f32_16x16x32_bf16 a[48:51], a[212:215], v[188:191], a[48:51]\n"
"v_exp_f32 v159, v159\n"
"v_mfma_f32_16x16x32_bf16 a[80:83], a[212:215], v[196:199], a[80:83]\n"
"v_cvt_pk_bf16_f32 v238, v156, v157\n"
"v_cvt_pk_bf16_f32 v239, v158, v159\n"
"v_mfma_f32_16x16x32_bf16 a[112:115], a[212:215], v[204:207], a[112:115]\n"
"v_mfma_f32_16x16x32_bf16 a[144:147], a[212:215], v[244:247], a[144:147]\n"
"s_waitcnt lgkmcnt(5)\n"
"v_mfma_f32_16x16x32_bf16 a[20:23], a[216:219], v[180:183], a[20:23]\n"
"ds_read_b128 a[192:195], v29 offset:0\n"
"v_mfma_f32_16x16x32_bf16 a[52:55], a[216:219], v[188:191], a[52:55]\n"
"v_mfma_f32_16x16x32_bf16 a[84:87], a[216:219], v[196:199], a[84:87]\n"
"v_mfma_f32_16x16x32_bf16 a[116:119], a[216:219], v[204:207], a[116:119]\n"
"v_mfma_f32_16x16x32_bf16 a[148:151], a[216:219], v[244:247], a[148:151]\n"
"s_waitcnt lgkmcnt(4)\n"
"v_mfma_f32_16x16x32_bf16 a[24:27], a[220:223], v[180:183], a[24:27]\n"
"ds_read_b128 a[196:199], v30 offset:0\n"
"v_mfma_f32_16x16x32_bf16 a[56:59], a[220:223], v[188:191], a[56:59]\n"
"v_mfma_f32_16x16x32_bf16 a[88:91], a[220:223], v[196:199], a[88:91]\n"
"v_mfma_f32_16x16x32_bf16 a[120:123], a[220:223], v[204:207], a[120:123]\n"
"v_mfma_f32_16x16x32_bf16 a[152:155], a[220:223], v[244:247], a[152:155]\n"
"s_waitcnt lgkmcnt(3)\n"
"v_mfma_f32_16x16x32_bf16 a[28:31], a[224:227], v[180:183], a[28:31]\n"
"ds_read_b128 a[200:203], v31 offset:0\n"
"v_mfma_f32_16x16x32_bf16 a[60:63], a[224:227], v[188:191], a[60:63]\n"
"v_mfma_f32_16x16x32_bf16 a[92:95], a[224:227], v[196:199], a[92:95]\n"
"v_mfma_f32_16x16x32_bf16 a[124:127], a[224:227], v[204:207], a[124:127]\n"
"v_mfma_f32_16x16x32_bf16 a[156:159], a[224:227], v[244:247], a[156:159]\n"
"s_waitcnt lgkmcnt(3)\n"
"v_mfma_f32_16x16x32_bf16 v[100:103], a[188:191], v[96:99], v[40:43]\n"
"ds_read_b128 a[180:183], v28 offset:4096\n"
"v_mfma_f32_16x16x32_bf16 v[116:119], a[188:191], v[128:131], v[44:47]\n"
"s_add_u32 m0, s61, 98304\n"
"s_mov_b32 s66, 163840\n"
"v_mfma_f32_16x16x32_bf16 v[132:135], a[188:191], v[160:163], v[48:51]\n"
"buffer_load_dwordx4 v95, s[52:55], s66 offen nt lds\n"
"v_mfma_f32_16x16x32_bf16 v[148:151], a[188:191], v[192:195], v[52:55]\n"
"v_mfma_f32_16x16x32_bf16 v[164:167], a[188:191], v[224:227], v[56:59]\n"
"s_waitcnt lgkmcnt(3)\n"
"v_mfma_f32_16x16x32_bf16 v[100:103], a[192:195], v[104:107], v[100:103]\n"
"ds_read_b128 a[184:187], v29 offset:4096\n"
"v_mfma_f32_16x16x32_bf16 v[116:119], a[192:195], v[136:139], v[116:119]\n"
"buffer_load_dwordx4 v27, s[52:55], s66 offen offset:1024 nt lds\n"
"v_mfma_f32_16x16x32_bf16 v[132:135], a[192:195], v[168:171], v[132:135]\n"
"v_mfma_f32_16x16x32_bf16 v[148:151], a[192:195], v[200:203], v[148:151]\n"
"v_mfma_f32_16x16x32_bf16 v[164:167], a[192:195], v[232:235], v[164:167]\n"
"s_waitcnt lgkmcnt(3)\n"
"v_mfma_f32_16x16x32_bf16 v[100:103], a[196:199], v[112:115], v[100:103]\n"
"ds_read_b128 a[188:191], v30 offset:4096\n"
"v_mfma_f32_16x16x32_bf16 v[116:119], a[196:199], v[144:147], v[116:119]\n"
"buffer_load_dwordx4 v95, s[52:55], s66 offen offset:2048 nt lds\n"
"v_mfma_f32_16x16x32_bf16 v[132:135], a[196:199], v[176:179], v[132:135]\n"
"v_mfma_f32_16x16x32_bf16 v[148:151], a[196:199], v[208:211], v[148:151]\n"
"v_mfma_f32_16x16x32_bf16 v[164:167], a[196:199], v[240:243], v[164:167]\n"
"s_waitcnt lgkmcnt(3)\n"
"v_mfma_f32_16x16x32_bf16 v[100:103], a[200:203], v[120:123], v[100:103]\n"
"ds_read_b128 a[192:195], v31 offset:4096\n"
"v_mfma_f32_16x16x32_bf16 v[116:119], a[200:203], v[152:155], v[116:119]\n"
"buffer_load_dwordx4 v27, s[52:55], s66 offen offset:3072 nt lds\n"
"v_mfma_f32_16x16x32_bf16 v[132:135], a[200:203], v[184:187], v[132:135]\n"
"v_mfma_f32_16x16x32_bf16 v[148:151], a[200:203], v[216:219], v[148:151]\n"
"v_mfma_f32_16x16x32_bf16 v[164:167], a[200:203], v[248:251], v[164:167]\n"
"s_waitcnt lgkmcnt(3)\n"
"v_mfma_f32_16x16x32_bf16 v[108:111], a[180:183], v[96:99], v[40:43]\n"
"v_mfma_f32_16x16x32_bf16 v[124:127], a[180:183], v[128:131], v[44:47]\n"
"v_mfma_f32_16x16x32_bf16 v[140:143], a[180:183], v[160:163], v[48:51]\n"
"v_mfma_f32_16x16x32_bf16 v[156:159], a[180:183], v[192:195], v[52:55]\n"
"v_exp_f32 v100, v100\n"
"v_mfma_f32_16x16x32_bf16 v[172:175], a[180:183], v[224:227], v[56:59]\n"
"s_waitcnt lgkmcnt(2)\n"
"v_exp_f32 v101, v101\n"
"v_mfma_f32_16x16x32_bf16 v[108:111], a[184:187], v[104:107], v[108:111]\n"
"v_exp_f32 v102, v102\n"
"v_mfma_f32_16x16x32_bf16 v[124:127], a[184:187], v[136:139], v[124:127]\n"
"v_exp_f32 v103, v103\n"
"v_mfma_f32_16x16x32_bf16 v[140:143], a[184:187], v[168:171], v[140:143]\n"
"ds_read_b64_tr_b16 a[206:207], v38 offset:53248\n"
"v_cvt_pk_bf16_f32 v180, v100, v101\n"
"v_cvt_pk_bf16_f32 v181, v102, v103\n"
"v_mfma_f32_16x16x32_bf16 v[156:159], a[184:187], v[200:203], v[156:159]\n"
"ds_read_b64_tr_b16 a[204:205], v38 offset:49152\n"
"v_exp_f32 v116, v116\n"
"v_mfma_f32_16x16x32_bf16 v[172:175], a[184:187], v[232:235], v[172:175]\n"
"s_waitcnt lgkmcnt(3)\n"
"v_exp_f32 v117, v117\n"
"v_mfma_f32_16x16x32_bf16 v[108:111], a[188:191], v[112:115], v[108:111]\n"
"v_exp_f32 v118, v118\n"
"v_mfma_f32_16x16x32_bf16 v[124:127], a[188:191], v[144:147], v[124:127]\n"
"v_exp_f32 v119, v119\n"
"v_mfma_f32_16x16x32_bf16 v[140:143], a[188:191], v[176:179], v[140:143]\n"
"ds_read_b64_tr_b16 a[208:209], v39 offset:49152\n"
"v_cvt_pk_bf16_f32 v188, v116, v117\n"
"v_cvt_pk_bf16_f32 v189, v118, v119\n"
"v_mfma_f32_16x16x32_bf16 v[156:159], a[188:191], v[208:211], v[156:159]\n"
"ds_read_b64_tr_b16 a[210:211], v39 offset:53248\n"
"v_exp_f32 v132, v132\n"
"v_mfma_f32_16x16x32_bf16 v[172:175], a[188:191], v[240:243], v[172:175]\n"
"s_waitcnt lgkmcnt(4)\n"
"v_exp_f32 v133, v133\n"
"v_mfma_f32_16x16x32_bf16 v[108:111], a[192:195], v[120:123], v[108:111]\n"
"v_exp_f32 v134, v134\n"
"v_mfma_f32_16x16x32_bf16 v[124:127], a[192:195], v[152:155], v[124:127]\n"
"ds_read_b64_tr_b16 a[212:213], v38 offset:49664\n"
"v_exp_f32 v135, v135\n"
"v_mfma_f32_16x16x32_bf16 v[140:143], a[192:195], v[184:187], v[140:143]\n"
"ds_read_b64_tr_b16 a[214:215], v38 offset:53760\n"
"v_cvt_pk_bf16_f32 v196, v132, v133\n"
"v_cvt_pk_bf16_f32 v197, v134, v135\n"
"v_mfma_f32_16x16x32_bf16 v[156:159], a[192:195], v[216:219], v[156:159]\n"
"v_exp_f32 v148, v148\n"
"v_mfma_f32_16x16x32_bf16 v[172:175], a[192:195], v[248:251], v[172:175]\n"
"v_exp_f32 v149, v149\n"
"v_mfma_f32_16x16x32_bf16 a[160:163], v[60:63], v[212:215], a[160:163]\n"
"ds_read_b64_tr_b16 a[216:217], v39 offset:49664\n"
"v_exp_f32 v150, v150\n"
"v_mfma_f32_16x16x32_bf16 a[164:167], v[60:63], v[220:223], a[164:167]\n"
"ds_read_b64_tr_b16 a[218:219], v39 offset:53760\n"
"v_exp_f32 v151, v151\n"
"v_mfma_f32_16x16x32_bf16 a[168:171], v[60:63], v[228:231], a[168:171]\n"
"v_cvt_pk_bf16_f32 v204, v148, v149\n"
"v_cvt_pk_bf16_f32 v205, v150, v151\n"
"v_mfma_f32_16x16x32_bf16 a[172:175], v[60:63], v[236:239], a[172:175]\n"
"s_waitcnt lgkmcnt(6)\n"
"v_exp_f32 v164, v164\n"
"v_mfma_f32_16x16x32_bf16 a[0:3], a[204:207], v[212:215], a[0:3]\n"
"ds_read_b64_tr_b16 a[220:221], v38 offset:50176\n"
"v_exp_f32 v165, v165\n"
"v_mfma_f32_16x16x32_bf16 a[32:35], a[204:207], v[220:223], a[32:35]\n"
"ds_read_b64_tr_b16 a[222:223], v38 offset:54272\n"
"v_exp_f32 v166, v166\n"
"v_mfma_f32_16x16x32_bf16 a[64:67], a[204:207], v[228:231], a[64:67]\n"
"v_exp_f32 v167, v167\n"
"v_mfma_f32_16x16x32_bf16 a[96:99], a[204:207], v[236:239], a[96:99]\n"
"s_waitcnt lgkmcnt(6)\n"
"v_cvt_pk_bf16_f32 v244, v164, v165\n"
"v_cvt_pk_bf16_f32 v245, v166, v167\n"
"v_mfma_f32_16x16x32_bf16 a[4:7], a[208:211], v[212:215], a[4:7]\n"
"ds_read_b64_tr_b16 a[224:225], v39 offset:50176\n"
"v_exp_f32 v108, v108\n"
"v_mfma_f32_16x16x32_bf16 a[36:39], a[208:211], v[220:223], a[36:39]\n"
"ds_read_b64_tr_b16 a[226:227], v39 offset:54272\n"
"v_exp_f32 v109, v109\n"
"v_mfma_f32_16x16x32_bf16 a[68:71], a[208:211], v[228:231], a[68:71]\n"
"v_exp_f32 v110, v110\n"
"v_mfma_f32_16x16x32_bf16 a[100:103], a[208:211], v[236:239], a[100:103]\n"
"s_waitcnt lgkmcnt(6)\n"
"v_exp_f32 v111, v111\n"
"v_mfma_f32_16x16x32_bf16 a[8:11], a[212:215], v[212:215], a[8:11]\n"
"ds_read_b64_tr_b16 a[204:205], v38 offset:50688\n"
"v_cvt_pk_bf16_f32 v182, v108, v109\n"
"v_cvt_pk_bf16_f32 v183, v110, v111\n"
"v_mfma_f32_16x16x32_bf16 a[40:43], a[212:215], v[220:223], a[40:43]\n"
"ds_read_b64_tr_b16 a[206:207], v38 offset:54784\n"
"v_exp_f32 v124, v124\n"
"v_mfma_f32_16x16x32_bf16 a[72:75], a[212:215], v[228:231], a[72:75]\n"
"v_exp_f32 v125, v125\n"
"v_mfma_f32_16x16x32_bf16 a[104:107], a[212:215], v[236:239], a[104:107]\n"
"s_waitcnt lgkmcnt(6)\n"
"v_exp_f32 v126, v126\n"
"v_mfma_f32_16x16x32_bf16 a[12:15], a[216:219], v[212:215], a[12:15]\n"
"ds_read_b64_tr_b16 a[208:209], v39 offset:50688\n"
"v_exp_f32 v127, v127\n"
"v_mfma_f32_16x16x32_bf16 a[44:47], a[216:219], v[220:223], a[44:47]\n"
"ds_read_b64_tr_b16 a[210:211], v39 offset:54784\n"
"v_cvt_pk_bf16_f32 v190, v124, v125\n"
"v_cvt_pk_bf16_f32 v191, v126, v127\n"
"v_mfma_f32_16x16x32_bf16 a[76:79], a[216:219], v[228:231], a[76:79]\n"
"v_exp_f32 v140, v140\n"
"v_mfma_f32_16x16x32_bf16 a[108:111], a[216:219], v[236:239], a[108:111]\n"
"s_waitcnt lgkmcnt(6)\n"
"v_exp_f32 v141, v141\n"
"v_mfma_f32_16x16x32_bf16 a[16:19], a[220:223], v[212:215], a[16:19]\n"
"ds_read_b128 a[196:199], v32 offset:0\n"
"v_exp_f32 v142, v142\n"
"v_mfma_f32_16x16x32_bf16 a[48:51], a[220:223], v[220:223], a[48:51]\n"
"v_exp_f32 v143, v143\n"
"v_mfma_f32_16x16x32_bf16 a[80:83], a[220:223], v[228:231], a[80:83]\n"
"v_cvt_pk_bf16_f32 v198, v140, v141\n"
"v_cvt_pk_bf16_f32 v199, v142, v143\n"
"v_mfma_f32_16x16x32_bf16 a[112:115], a[220:223], v[236:239], a[112:115]\n"
"s_waitcnt lgkmcnt(5)\n"
"v_exp_f32 v156, v156\n"
"v_mfma_f32_16x16x32_bf16 a[20:23], a[224:227], v[212:215], a[20:23]\n"
"ds_read_b128 a[200:203], v33 offset:0\n"
"v_exp_f32 v157, v157\n"
"v_mfma_f32_16x16x32_bf16 a[52:55], a[224:227], v[220:223], a[52:55]\n"
"v_exp_f32 v158, v158\n"
"v_mfma_f32_16x16x32_bf16 a[84:87], a[224:227], v[228:231], a[84:87]\n"
"v_exp_f32 v159, v159\n"
"v_mfma_f32_16x16x32_bf16 a[116:119], a[224:227], v[236:239], a[116:119]\n"
"s_waitcnt lgkmcnt(4)\n"
"v_cvt_pk_bf16_f32 v206, v156, v157\n"
"v_cvt_pk_bf16_f32 v207, v158, v159\n"
"v_mfma_f32_16x16x32_bf16 a[24:27], a[204:207], v[212:215], a[24:27]\n"
"ds_read_b128 a[180:183], v34 offset:0\n"
"v_exp_f32 v172, v172\n"
"v_mfma_f32_16x16x32_bf16 a[56:59], a[204:207], v[220:223], a[56:59]\n"
"v_exp_f32 v173, v173\n"
"v_mfma_f32_16x16x32_bf16 a[88:91], a[204:207], v[228:231], a[88:91]\n"
"v_exp_f32 v174, v174\n"
"v_mfma_f32_16x16x32_bf16 a[120:123], a[204:207], v[236:239], a[120:123]\n"
"s_waitcnt lgkmcnt(3)\n"
"v_exp_f32 v175, v175\n"
"v_mfma_f32_16x16x32_bf16 a[28:31], a[208:211], v[212:215], a[28:31]\n"
"ds_read_b128 a[184:187], v35 offset:0\n"
"v_cvt_pk_bf16_f32 v246, v172, v173\n"
"v_cvt_pk_bf16_f32 v247, v174, v175\n"
"v_mfma_f32_16x16x32_bf16 a[60:63], a[208:211], v[220:223], a[60:63]\n"
"buffer_load_dword a230, v26, s[92:95], s76 offen\n"
"v_mfma_f32_16x16x32_bf16 a[92:95], a[208:211], v[228:231], a[92:95]\n"
"s_add_u32 s76, s76, 4096\n"
"v_mfma_f32_16x16x32_bf16 a[124:127], a[208:211], v[236:239], a[124:127]\n"
"s_waitcnt lgkmcnt(3)\n"
"v_mfma_f32_16x16x32_bf16 v[100:103], a[196:199], v[96:99], v[40:43]\n"
"ds_read_b128 a[188:191], v32 offset:4096\n"
"v_mfma_f32_16x16x32_bf16 v[116:119], a[196:199], v[128:131], v[44:47]\n"
"s_add_u32 m0, s61, 49152\n"
"s_mov_b32 s65, 180224\n"
"v_mfma_f32_16x16x32_bf16 v[132:135], a[196:199], v[160:163], v[48:51]\n"
"buffer_load_dwordx4 v24, s[48:51], s65 offen nt lds\n"
"v_mfma_f32_16x16x32_bf16 v[148:151], a[196:199], v[192:195], v[52:55]\n"
"s_waitcnt lgkmcnt(3)\n"
"v_mfma_f32_16x16x32_bf16 v[100:103], a[200:203], v[104:107], v[100:103]\n"
"ds_read_b128 a[192:195], v33 offset:4096\n"
"v_mfma_f32_16x16x32_bf16 v[116:119], a[200:203], v[136:139], v[116:119]\n"
"v_mfma_f32_16x16x32_bf16 v[132:135], a[200:203], v[168:171], v[132:135]\n"
"buffer_load_dwordx4 v25, s[48:51], s65 offen offset:1024 nt lds\n"
"v_mfma_f32_16x16x32_bf16 v[148:151], a[200:203], v[200:203], v[148:151]\n"
"s_waitcnt lgkmcnt(3)\n"
"v_mfma_f32_16x16x32_bf16 v[100:103], a[180:183], v[112:115], v[100:103]\n"
"ds_read_b128 a[196:199], v34 offset:4096\n"
"v_mfma_f32_16x16x32_bf16 v[116:119], a[180:183], v[144:147], v[116:119]\n"
"v_mfma_f32_16x16x32_bf16 v[132:135], a[180:183], v[176:179], v[132:135]\n"
"buffer_load_dwordx4 v93, s[48:51], s65 offen offset:2048 nt lds\n"
"v_mfma_f32_16x16x32_bf16 v[148:151], a[180:183], v[208:211], v[148:151]\n"
"s_waitcnt lgkmcnt(3)\n"
"v_mfma_f32_16x16x32_bf16 v[100:103], a[184:187], v[120:123], v[100:103]\n"
"ds_read_b128 a[200:203], v35 offset:4096\n"
"v_mfma_f32_16x16x32_bf16 v[116:119], a[184:187], v[152:155], v[116:119]\n"
"v_mfma_f32_16x16x32_bf16 v[132:135], a[184:187], v[184:187], v[132:135]\n"
"buffer_load_dwordx4 v94, s[48:51], s65 offen offset:3072 nt lds\n"
"v_mfma_f32_16x16x32_bf16 v[148:151], a[184:187], v[216:219], v[148:151]\n"
"s_waitcnt lgkmcnt(3)\n"
"v_mfma_f32_16x16x32_bf16 v[108:111], a[188:191], v[96:99], v[40:43]\n"
"v_mfma_f32_16x16x32_bf16 v[124:127], a[188:191], v[128:131], v[44:47]\n"
"ds_read_b64_tr_b16 a[212:213], v36 offset:0\n"
"v_mfma_f32_16x16x32_bf16 v[140:143], a[188:191], v[160:163], v[48:51]\n"
"ds_read_b64_tr_b16 a[214:215], v36 offset:4096\n"
"v_mfma_f32_16x16x32_bf16 v[156:159], a[188:191], v[192:195], v[52:55]\n"
"s_waitcnt lgkmcnt(4)\n"
"v_mfma_f32_16x16x32_bf16 v[108:111], a[192:195], v[104:107], v[108:111]\n"
"v_exp_f32 v100, v100\n"
"v_mfma_f32_16x16x32_bf16 v[124:127], a[192:195], v[136:139], v[124:127]\n"
"ds_read_b64_tr_b16 a[218:219], v37 offset:4096\n"
"v_exp_f32 v101, v101\n"
"v_mfma_f32_16x16x32_bf16 v[140:143], a[192:195], v[168:171], v[140:143]\n"
"ds_read_b64_tr_b16 a[216:217], v37 offset:0\n"
"v_exp_f32 v102, v102\n"
"v_mfma_f32_16x16x32_bf16 v[156:159], a[192:195], v[200:203], v[156:159]\n"
"s_waitcnt lgkmcnt(5)\n"
"v_exp_f32 v103, v103\n"
"v_mfma_f32_16x16x32_bf16 v[108:111], a[196:199], v[112:115], v[108:111]\n"
"v_cvt_pk_bf16_f32 v212, v100, v101\n"
"v_cvt_pk_bf16_f32 v213, v102, v103\n"
"v_mfma_f32_16x16x32_bf16 v[124:127], a[196:199], v[144:147], v[124:127]\n"
"v_exp_f32 v116, v116\n"
"v_mfma_f32_16x16x32_bf16 v[140:143], a[196:199], v[176:179], v[140:143]\n"
"ds_read_b64_tr_b16 a[220:221], v36 offset:512\n"
"v_exp_f32 v117, v117\n"
"v_mfma_f32_16x16x32_bf16 v[156:159], a[196:199], v[208:211], v[156:159]\n"
"s_waitcnt lgkmcnt(5)\n"
"v_exp_f32 v118, v118\n"
"v_mfma_f32_16x16x32_bf16 v[108:111], a[200:203], v[120:123], v[108:111]\n"
"ds_read_b64_tr_b16 a[222:223], v36 offset:4608\n"
"v_exp_f32 v119, v119\n"
"v_mfma_f32_16x16x32_bf16 v[124:127], a[200:203], v[152:155], v[124:127]\n"
"v_cvt_pk_bf16_f32 v220, v116, v117\n"
"v_cvt_pk_bf16_f32 v221, v118, v119\n"
"v_mfma_f32_16x16x32_bf16 v[140:143], a[200:203], v[184:187], v[140:143]\n"
"v_exp_f32 v132, v132\n"
"v_mfma_f32_16x16x32_bf16 v[156:159], a[200:203], v[216:219], v[156:159]\n"
"s_waitcnt vmcnt(14)\n"
"s_barrier\n"
"v_exp_f32 v133, v133\n"
"v_mfma_f32_16x16x32_bf16 a[160:163], v[60:63], v[180:183], a[160:163]\n"
"ds_read_b64_tr_b16 a[224:225], v37 offset:512\n"
"v_exp_f32 v134, v134\n"
"v_mfma_f32_16x16x32_bf16 a[164:167], v[60:63], v[188:191], a[164:167]\n"
"ds_read_b64_tr_b16 a[226:227], v37 offset:4608\n"
"v_exp_f32 v135, v135\n"
"v_mfma_f32_16x16x32_bf16 a[168:171], v[60:63], v[196:199], a[168:171]\n"
"v_cvt_pk_bf16_f32 v228, v132, v133\n"
"v_cvt_pk_bf16_f32 v229, v134, v135\n"
"v_mfma_f32_16x16x32_bf16 a[172:175], v[60:63], v[204:207], a[172:175]\n"
"v_exp_f32 v148, v148\n"
"v_mfma_f32_16x16x32_bf16 a[176:179], v[60:63], v[244:247], a[176:179]\n"
"s_waitcnt lgkmcnt(6)\n"
"v_exp_f32 v149, v149\n"
"v_mfma_f32_16x16x32_bf16 a[0:3], a[212:215], v[180:183], a[0:3]\n"
"ds_read_b64_tr_b16 a[204:205], v36 offset:1024\n"
"v_exp_f32 v150, v150\n"
"v_mfma_f32_16x16x32_bf16 a[32:35], a[212:215], v[188:191], a[32:35]\n"
"ds_read_b64_tr_b16 a[206:207], v36 offset:5120\n"
"v_exp_f32 v151, v151\n"
"v_mfma_f32_16x16x32_bf16 a[64:67], a[212:215], v[196:199], a[64:67]\n"
"v_cvt_pk_bf16_f32 v236, v148, v149\n"
"v_cvt_pk_bf16_f32 v237, v150, v151\n"
"v_mfma_f32_16x16x32_bf16 a[96:99], a[212:215], v[204:207], a[96:99]\n"
"v_exp_f32 v108, v108\n"
"v_mfma_f32_16x16x32_bf16 a[128:131], a[212:215], v[244:247], a[128:131]\n"
"s_waitcnt lgkmcnt(6)\n"
"v_exp_f32 v109, v109\n"
"v_mfma_f32_16x16x32_bf16 a[4:7], a[216:219], v[180:183], a[4:7]\n"
"ds_read_b64_tr_b16 a[208:209], v37 offset:1024\n"
"v_exp_f32 v110, v110\n"
"v_mfma_f32_16x16x32_bf16 a[36:39], a[216:219], v[188:191], a[36:39]\n"
"ds_read_b64_tr_b16 a[210:211], v37 offset:5120\n"
"v_exp_f32 v111, v111\n"
"v_mfma_f32_16x16x32_bf16 a[68:71], a[216:219], v[196:199], a[68:71]\n"
"v_cvt_pk_bf16_f32 v214, v108, v109\n"
"v_cvt_pk_bf16_f32 v215, v110, v111\n"
"v_mfma_f32_16x16x32_bf16 a[100:103], a[216:219], v[204:207], a[100:103]\n"
"v_exp_f32 v124, v124\n"
"v_mfma_f32_16x16x32_bf16 a[132:135], a[216:219], v[244:247], a[132:135]\n"
"s_waitcnt lgkmcnt(6)\n"
"v_exp_f32 v125, v125\n"
"v_mfma_f32_16x16x32_bf16 a[8:11], a[220:223], v[180:183], a[8:11]\n"
"ds_read_b64_tr_b16 a[212:213], v36 offset:1536\n"
"v_exp_f32 v126, v126\n"
"v_mfma_f32_16x16x32_bf16 a[40:43], a[220:223], v[188:191], a[40:43]\n"
"ds_read_b64_tr_b16 a[214:215], v36 offset:5632\n"
"v_exp_f32 v127, v127\n"
"v_mfma_f32_16x16x32_bf16 a[72:75], a[220:223], v[196:199], a[72:75]\n"
"v_cvt_pk_bf16_f32 v222, v124, v125\n"
"v_cvt_pk_bf16_f32 v223, v126, v127\n"
"v_mfma_f32_16x16x32_bf16 a[104:107], a[220:223], v[204:207], a[104:107]\n"
"v_exp_f32 v140, v140\n"
"v_mfma_f32_16x16x32_bf16 a[136:139], a[220:223], v[244:247], a[136:139]\n"
"s_waitcnt lgkmcnt(6)\n"
"v_exp_f32 v141, v141\n"
"v_mfma_f32_16x16x32_bf16 a[12:15], a[224:227], v[180:183], a[12:15]\n"
"ds_read_b64_tr_b16 a[216:217], v37 offset:1536\n"
"v_exp_f32 v142, v142\n"
"v_mfma_f32_16x16x32_bf16 a[44:47], a[224:227], v[188:191], a[44:47]\n"
"ds_read_b64_tr_b16 a[218:219], v37 offset:5632\n"
"v_exp_f32 v143, v143\n"
"v_mfma_f32_16x16x32_bf16 a[76:79], a[224:227], v[196:199], a[76:79]\n"
"v_cvt_pk_bf16_f32 v230, v140, v141\n"
"v_cvt_pk_bf16_f32 v231, v142, v143\n"
"v_mfma_f32_16x16x32_bf16 a[108:111], a[224:227], v[204:207], a[108:111]\n"
"v_exp_f32 v156, v156\n"
"v_mfma_f32_16x16x32_bf16 a[140:143], a[224:227], v[244:247], a[140:143]\n"
"s_waitcnt lgkmcnt(6)\n"
"v_exp_f32 v157, v157\n"
"v_mfma_f32_16x16x32_bf16 a[16:19], a[204:207], v[180:183], a[16:19]\n"
"ds_read_b128 a[180:183], v28 offset:16384\n"
"v_exp_f32 v158, v158\n"
"v_mfma_f32_16x16x32_bf16 a[48:51], a[204:207], v[188:191], a[48:51]\n"
"v_exp_f32 v159, v159\n"
"v_mfma_f32_16x16x32_bf16 a[80:83], a[204:207], v[196:199], a[80:83]\n"
"v_cvt_pk_bf16_f32 v238, v156, v157\n"
"v_cvt_pk_bf16_f32 v239, v158, v159\n"
"v_mfma_f32_16x16x32_bf16 a[112:115], a[204:207], v[204:207], a[112:115]\n"
"v_mfma_f32_16x16x32_bf16 a[144:147], a[204:207], v[244:247], a[144:147]\n"
"s_waitcnt lgkmcnt(5)\n"
"v_mfma_f32_16x16x32_bf16 a[20:23], a[208:211], v[180:183], a[20:23]\n"
"ds_read_b128 a[184:187], v29 offset:16384\n"
"v_mfma_f32_16x16x32_bf16 a[52:55], a[208:211], v[188:191], a[52:55]\n"
"v_mfma_f32_16x16x32_bf16 a[84:87], a[208:211], v[196:199], a[84:87]\n"
"v_mfma_f32_16x16x32_bf16 a[116:119], a[208:211], v[204:207], a[116:119]\n"
"v_mfma_f32_16x16x32_bf16 a[148:151], a[208:211], v[244:247], a[148:151]\n"
"s_waitcnt lgkmcnt(4)\n"
"v_mfma_f32_16x16x32_bf16 a[24:27], a[212:215], v[180:183], a[24:27]\n"
"ds_read_b128 a[188:191], v30 offset:16384\n"
"v_mfma_f32_16x16x32_bf16 a[56:59], a[212:215], v[188:191], a[56:59]\n"
"v_mfma_f32_16x16x32_bf16 a[88:91], a[212:215], v[196:199], a[88:91]\n"
"v_mfma_f32_16x16x32_bf16 a[120:123], a[212:215], v[204:207], a[120:123]\n"
"v_mfma_f32_16x16x32_bf16 a[152:155], a[212:215], v[244:247], a[152:155]\n"
"s_waitcnt lgkmcnt(3)\n"
"v_mfma_f32_16x16x32_bf16 a[28:31], a[216:219], v[180:183], a[28:31]\n"
"ds_read_b128 a[192:195], v31 offset:16384\n"
"v_mfma_f32_16x16x32_bf16 a[60:63], a[216:219], v[188:191], a[60:63]\n"
"v_mfma_f32_16x16x32_bf16 a[92:95], a[216:219], v[196:199], a[92:95]\n"
"v_mfma_f32_16x16x32_bf16 a[124:127], a[216:219], v[204:207], a[124:127]\n"
"v_mfma_f32_16x16x32_bf16 a[156:159], a[216:219], v[244:247], a[156:159]\n"
"s_waitcnt lgkmcnt(3)\n"
"v_mfma_f32_16x16x32_bf16 v[100:103], a[180:183], v[96:99], v[40:43]\n"
"ds_read_b128 a[196:199], v28 offset:20480\n"
"v_mfma_f32_16x16x32_bf16 v[116:119], a[180:183], v[128:131], v[44:47]\n"
"s_add_u32 m0, s61, 114688\n"
"s_mov_b32 s66, 180224\n"
"v_mfma_f32_16x16x32_bf16 v[132:135], a[180:183], v[160:163], v[48:51]\n"
"buffer_load_dwordx4 v95, s[52:55], s66 offen nt lds\n"
"v_mfma_f32_16x16x32_bf16 v[148:151], a[180:183], v[192:195], v[52:55]\n"
"v_mfma_f32_16x16x32_bf16 v[164:167], a[180:183], v[224:227], v[56:59]\n"
"s_waitcnt lgkmcnt(3)\n"
"v_mfma_f32_16x16x32_bf16 v[100:103], a[184:187], v[104:107], v[100:103]\n"
"ds_read_b128 a[200:203], v29 offset:20480\n"
"v_mfma_f32_16x16x32_bf16 v[116:119], a[184:187], v[136:139], v[116:119]\n"
"buffer_load_dwordx4 v27, s[52:55], s66 offen offset:1024 nt lds\n"
"v_mfma_f32_16x16x32_bf16 v[132:135], a[184:187], v[168:171], v[132:135]\n"
"v_mfma_f32_16x16x32_bf16 v[148:151], a[184:187], v[200:203], v[148:151]\n"
"v_mfma_f32_16x16x32_bf16 v[164:167], a[184:187], v[232:235], v[164:167]\n"
"s_waitcnt lgkmcnt(3)\n"
"v_mfma_f32_16x16x32_bf16 v[100:103], a[188:191], v[112:115], v[100:103]\n"
"ds_read_b128 a[180:183], v30 offset:20480\n"
"v_mfma_f32_16x16x32_bf16 v[116:119], a[188:191], v[144:147], v[116:119]\n"
"buffer_load_dwordx4 v95, s[52:55], s66 offen offset:2048 nt lds\n"
"v_mfma_f32_16x16x32_bf16 v[132:135], a[188:191], v[176:179], v[132:135]\n"
"v_mfma_f32_16x16x32_bf16 v[148:151], a[188:191], v[208:211], v[148:151]\n"
"v_mfma_f32_16x16x32_bf16 v[164:167], a[188:191], v[240:243], v[164:167]\n"
"s_waitcnt lgkmcnt(3)\n"
"v_mfma_f32_16x16x32_bf16 v[100:103], a[192:195], v[120:123], v[100:103]\n"
"ds_read_b128 a[184:187], v31 offset:20480\n"
"v_mfma_f32_16x16x32_bf16 v[116:119], a[192:195], v[152:155], v[116:119]\n"
"buffer_load_dwordx4 v27, s[52:55], s66 offen offset:3072 nt lds\n"
"v_mfma_f32_16x16x32_bf16 v[132:135], a[192:195], v[184:187], v[132:135]\n"
"v_mfma_f32_16x16x32_bf16 v[148:151], a[192:195], v[216:219], v[148:151]\n"
"v_mfma_f32_16x16x32_bf16 v[164:167], a[192:195], v[248:251], v[164:167]\n"
"s_waitcnt lgkmcnt(3)\n"
"v_mfma_f32_16x16x32_bf16 v[108:111], a[196:199], v[96:99], v[40:43]\n"
"v_mfma_f32_16x16x32_bf16 v[124:127], a[196:199], v[128:131], v[44:47]\n"
"v_mfma_f32_16x16x32_bf16 v[140:143], a[196:199], v[160:163], v[48:51]\n"
"v_mfma_f32_16x16x32_bf16 v[156:159], a[196:199], v[192:195], v[52:55]\n"
"v_exp_f32 v100, v100\n"
"v_mfma_f32_16x16x32_bf16 v[172:175], a[196:199], v[224:227], v[56:59]\n"
"s_waitcnt lgkmcnt(2)\n"
"v_exp_f32 v101, v101\n"
"v_mfma_f32_16x16x32_bf16 v[108:111], a[200:203], v[104:107], v[108:111]\n"
"v_exp_f32 v102, v102\n"
"v_mfma_f32_16x16x32_bf16 v[124:127], a[200:203], v[136:139], v[124:127]\n"
"v_exp_f32 v103, v103\n"
"v_mfma_f32_16x16x32_bf16 v[140:143], a[200:203], v[168:171], v[140:143]\n"
"ds_read_b64_tr_b16 a[222:223], v38 offset:4096\n"
"v_cvt_pk_bf16_f32 v180, v100, v101\n"
"v_cvt_pk_bf16_f32 v181, v102, v103\n"
"v_mfma_f32_16x16x32_bf16 v[156:159], a[200:203], v[200:203], v[156:159]\n"
"ds_read_b64_tr_b16 a[220:221], v38 offset:0\n"
"v_exp_f32 v116, v116\n"
"v_mfma_f32_16x16x32_bf16 v[172:175], a[200:203], v[232:235], v[172:175]\n"
"s_waitcnt lgkmcnt(3)\n"
"v_exp_f32 v117, v117\n"
"v_mfma_f32_16x16x32_bf16 v[108:111], a[180:183], v[112:115], v[108:111]\n"
"v_exp_f32 v118, v118\n"
"v_mfma_f32_16x16x32_bf16 v[124:127], a[180:183], v[144:147], v[124:127]\n"
"v_exp_f32 v119, v119\n"
"v_mfma_f32_16x16x32_bf16 v[140:143], a[180:183], v[176:179], v[140:143]\n"
"ds_read_b64_tr_b16 a[224:225], v39 offset:0\n"
"v_cvt_pk_bf16_f32 v188, v116, v117\n"
"v_cvt_pk_bf16_f32 v189, v118, v119\n"
"v_mfma_f32_16x16x32_bf16 v[156:159], a[180:183], v[208:211], v[156:159]\n"
"ds_read_b64_tr_b16 a[226:227], v39 offset:4096\n"
"v_exp_f32 v132, v132\n"
"v_mfma_f32_16x16x32_bf16 v[172:175], a[180:183], v[240:243], v[172:175]\n"
"s_waitcnt lgkmcnt(4)\n"
"v_exp_f32 v133, v133\n"
"v_mfma_f32_16x16x32_bf16 v[108:111], a[184:187], v[120:123], v[108:111]\n"
"v_exp_f32 v134, v134\n"
"v_mfma_f32_16x16x32_bf16 v[124:127], a[184:187], v[152:155], v[124:127]\n"
"ds_read_b64_tr_b16 a[204:205], v38 offset:512\n"
"v_exp_f32 v135, v135\n"
"v_mfma_f32_16x16x32_bf16 v[140:143], a[184:187], v[184:187], v[140:143]\n"
"ds_read_b64_tr_b16 a[206:207], v38 offset:4608\n"
"v_cvt_pk_bf16_f32 v196, v132, v133\n"
"v_cvt_pk_bf16_f32 v197, v134, v135\n"
"v_mfma_f32_16x16x32_bf16 v[156:159], a[184:187], v[216:219], v[156:159]\n"
"v_exp_f32 v148, v148\n"
"v_mfma_f32_16x16x32_bf16 v[172:175], a[184:187], v[248:251], v[172:175]\n"
"v_exp_f32 v149, v149\n"
"v_mfma_f32_16x16x32_bf16 a[160:163], v[60:63], v[212:215], a[160:163]\n"
"ds_read_b64_tr_b16 a[208:209], v39 offset:512\n"
"v_exp_f32 v150, v150\n"
"v_mfma_f32_16x16x32_bf16 a[164:167], v[60:63], v[220:223], a[164:167]\n"
"ds_read_b64_tr_b16 a[210:211], v39 offset:4608\n"
"v_exp_f32 v151, v151\n"
"v_mfma_f32_16x16x32_bf16 a[168:171], v[60:63], v[228:231], a[168:171]\n"
"v_cvt_pk_bf16_f32 v204, v148, v149\n"
"v_cvt_pk_bf16_f32 v205, v150, v151\n"
"v_mfma_f32_16x16x32_bf16 a[172:175], v[60:63], v[236:239], a[172:175]\n"
"s_waitcnt lgkmcnt(6)\n"
"v_exp_f32 v164, v164\n"
"v_mfma_f32_16x16x32_bf16 a[0:3], a[220:223], v[212:215], a[0:3]\n"
"ds_read_b64_tr_b16 a[212:213], v38 offset:1024\n"
"v_exp_f32 v165, v165\n"
"v_mfma_f32_16x16x32_bf16 a[32:35], a[220:223], v[220:223], a[32:35]\n"
"ds_read_b64_tr_b16 a[214:215], v38 offset:5120\n"
"v_exp_f32 v166, v166\n"
"v_mfma_f32_16x16x32_bf16 a[64:67], a[220:223], v[228:231], a[64:67]\n"
"v_exp_f32 v167, v167\n"
"v_mfma_f32_16x16x32_bf16 a[96:99], a[220:223], v[236:239], a[96:99]\n"
"s_waitcnt lgkmcnt(6)\n"
"v_cvt_pk_bf16_f32 v244, v164, v165\n"
"v_cvt_pk_bf16_f32 v245, v166, v167\n"
"v_mfma_f32_16x16x32_bf16 a[4:7], a[224:227], v[212:215], a[4:7]\n"
"ds_read_b64_tr_b16 a[216:217], v39 offset:1024\n"
"v_exp_f32 v108, v108\n"
"v_mfma_f32_16x16x32_bf16 a[36:39], a[224:227], v[220:223], a[36:39]\n"
"ds_read_b64_tr_b16 a[218:219], v39 offset:5120\n"
"v_exp_f32 v109, v109\n"
"v_mfma_f32_16x16x32_bf16 a[68:71], a[224:227], v[228:231], a[68:71]\n"
"v_exp_f32 v110, v110\n"
"v_mfma_f32_16x16x32_bf16 a[100:103], a[224:227], v[236:239], a[100:103]\n"
"s_waitcnt lgkmcnt(6)\n"
"v_exp_f32 v111, v111\n"
"v_mfma_f32_16x16x32_bf16 a[8:11], a[204:207], v[212:215], a[8:11]\n"
"ds_read_b64_tr_b16 a[220:221], v38 offset:1536\n"
"v_cvt_pk_bf16_f32 v182, v108, v109\n"
"v_cvt_pk_bf16_f32 v183, v110, v111\n"
"v_mfma_f32_16x16x32_bf16 a[40:43], a[204:207], v[220:223], a[40:43]\n"
"ds_read_b64_tr_b16 a[222:223], v38 offset:5632\n"
"v_exp_f32 v124, v124\n"
"v_mfma_f32_16x16x32_bf16 a[72:75], a[204:207], v[228:231], a[72:75]\n"
"v_exp_f32 v125, v125\n"
"v_mfma_f32_16x16x32_bf16 a[104:107], a[204:207], v[236:239], a[104:107]\n"
"s_waitcnt lgkmcnt(6)\n"
"v_exp_f32 v126, v126\n"
"v_mfma_f32_16x16x32_bf16 a[12:15], a[208:211], v[212:215], a[12:15]\n"
"ds_read_b64_tr_b16 a[224:225], v39 offset:1536\n"
"v_exp_f32 v127, v127\n"
"v_mfma_f32_16x16x32_bf16 a[44:47], a[208:211], v[220:223], a[44:47]\n"
"ds_read_b64_tr_b16 a[226:227], v39 offset:5632\n"
"v_cvt_pk_bf16_f32 v190, v124, v125\n"
"v_cvt_pk_bf16_f32 v191, v126, v127\n"
"v_mfma_f32_16x16x32_bf16 a[76:79], a[208:211], v[228:231], a[76:79]\n"
"v_exp_f32 v140, v140\n"
"v_mfma_f32_16x16x32_bf16 a[108:111], a[208:211], v[236:239], a[108:111]\n"
"s_waitcnt lgkmcnt(6)\n"
"v_exp_f32 v141, v141\n"
"v_mfma_f32_16x16x32_bf16 a[16:19], a[212:215], v[212:215], a[16:19]\n"
"ds_read_b128 a[188:191], v32 offset:16384\n"
"v_exp_f32 v142, v142\n"
"v_mfma_f32_16x16x32_bf16 a[48:51], a[212:215], v[220:223], a[48:51]\n"
"v_exp_f32 v143, v143\n"
"v_mfma_f32_16x16x32_bf16 a[80:83], a[212:215], v[228:231], a[80:83]\n"
"v_cvt_pk_bf16_f32 v198, v140, v141\n"
"v_cvt_pk_bf16_f32 v199, v142, v143\n"
"v_mfma_f32_16x16x32_bf16 a[112:115], a[212:215], v[236:239], a[112:115]\n"
"s_waitcnt lgkmcnt(5)\n"
"v_exp_f32 v156, v156\n"
"v_mfma_f32_16x16x32_bf16 a[20:23], a[216:219], v[212:215], a[20:23]\n"
"ds_read_b128 a[192:195], v33 offset:16384\n"
"v_exp_f32 v157, v157\n"
"v_mfma_f32_16x16x32_bf16 a[52:55], a[216:219], v[220:223], a[52:55]\n"
"v_exp_f32 v158, v158\n"
"v_mfma_f32_16x16x32_bf16 a[84:87], a[216:219], v[228:231], a[84:87]\n"
"v_exp_f32 v159, v159\n"
"v_mfma_f32_16x16x32_bf16 a[116:119], a[216:219], v[236:239], a[116:119]\n"
"s_waitcnt lgkmcnt(4)\n"
"v_cvt_pk_bf16_f32 v206, v156, v157\n"
"v_cvt_pk_bf16_f32 v207, v158, v159\n"
"v_mfma_f32_16x16x32_bf16 a[24:27], a[220:223], v[212:215], a[24:27]\n"
"ds_read_b128 a[196:199], v34 offset:16384\n"
"v_exp_f32 v172, v172\n"
"v_mfma_f32_16x16x32_bf16 a[56:59], a[220:223], v[220:223], a[56:59]\n"
"v_exp_f32 v173, v173\n"
"v_mfma_f32_16x16x32_bf16 a[88:91], a[220:223], v[228:231], a[88:91]\n"
"v_exp_f32 v174, v174\n"
"v_mfma_f32_16x16x32_bf16 a[120:123], a[220:223], v[236:239], a[120:123]\n"
"s_waitcnt lgkmcnt(3)\n"
"v_exp_f32 v175, v175\n"
"v_mfma_f32_16x16x32_bf16 a[28:31], a[224:227], v[212:215], a[28:31]\n"
"ds_read_b128 a[200:203], v35 offset:16384\n"
"v_cvt_pk_bf16_f32 v246, v172, v173\n"
"v_cvt_pk_bf16_f32 v247, v174, v175\n"
"v_mfma_f32_16x16x32_bf16 a[60:63], a[224:227], v[220:223], a[60:63]\n"
"v_mfma_f32_16x16x32_bf16 a[92:95], a[224:227], v[228:231], a[92:95]\n"
"v_mfma_f32_16x16x32_bf16 a[124:127], a[224:227], v[236:239], a[124:127]\n"
"s_waitcnt lgkmcnt(3)\n"
"v_mfma_f32_16x16x32_bf16 v[100:103], a[188:191], v[96:99], v[40:43]\n"
"ds_read_b128 a[180:183], v32 offset:20480\n"
"v_mfma_f32_16x16x32_bf16 v[116:119], a[188:191], v[128:131], v[44:47]\n"
"s_add_u32 m0, s61, 0\n"
"s_mov_b32 s65, 196608\n"
"v_mfma_f32_16x16x32_bf16 v[132:135], a[188:191], v[160:163], v[48:51]\n"
"buffer_load_dwordx4 v24, s[48:51], s65 offen nt lds\n"
"v_mfma_f32_16x16x32_bf16 v[148:151], a[188:191], v[192:195], v[52:55]\n"
"s_waitcnt lgkmcnt(3)\n"
"v_mfma_f32_16x16x32_bf16 v[100:103], a[192:195], v[104:107], v[100:103]\n"
"ds_read_b128 a[184:187], v33 offset:20480\n"
"v_mfma_f32_16x16x32_bf16 v[116:119], a[192:195], v[136:139], v[116:119]\n"
"v_mfma_f32_16x16x32_bf16 v[132:135], a[192:195], v[168:171], v[132:135]\n"
"buffer_load_dwordx4 v25, s[48:51], s65 offen offset:1024 nt lds\n"
"v_mfma_f32_16x16x32_bf16 v[148:151], a[192:195], v[200:203], v[148:151]\n"
"s_waitcnt lgkmcnt(3)\n"
"v_mfma_f32_16x16x32_bf16 v[100:103], a[196:199], v[112:115], v[100:103]\n"
"ds_read_b128 a[188:191], v34 offset:20480\n"
"v_mfma_f32_16x16x32_bf16 v[116:119], a[196:199], v[144:147], v[116:119]\n"
"v_mfma_f32_16x16x32_bf16 v[132:135], a[196:199], v[176:179], v[132:135]\n"
"buffer_load_dwordx4 v93, s[48:51], s65 offen offset:2048 nt lds\n"
"v_mfma_f32_16x16x32_bf16 v[148:151], a[196:199], v[208:211], v[148:151]\n"
"s_waitcnt lgkmcnt(3)\n"
"v_mfma_f32_16x16x32_bf16 v[100:103], a[200:203], v[120:123], v[100:103]\n"
"ds_read_b128 a[192:195], v35 offset:20480\n"
"v_mfma_f32_16x16x32_bf16 v[116:119], a[200:203], v[152:155], v[116:119]\n"
"v_mfma_f32_16x16x32_bf16 v[132:135], a[200:203], v[184:187], v[132:135]\n"
"buffer_load_dwordx4 v94, s[48:51], s65 offen offset:3072 nt lds\n"
"v_mfma_f32_16x16x32_bf16 v[148:151], a[200:203], v[216:219], v[148:151]\n"
"s_waitcnt lgkmcnt(3)\n"
"v_mfma_f32_16x16x32_bf16 v[108:111], a[180:183], v[96:99], v[40:43]\n"
"v_mfma_f32_16x16x32_bf16 v[124:127], a[180:183], v[128:131], v[44:47]\n"
"ds_read_b64_tr_b16 a[204:205], v36 offset:16384\n"
"v_mfma_f32_16x16x32_bf16 v[140:143], a[180:183], v[160:163], v[48:51]\n"
"ds_read_b64_tr_b16 a[206:207], v36 offset:20480\n"
"v_mfma_f32_16x16x32_bf16 v[156:159], a[180:183], v[192:195], v[52:55]\n"
"s_waitcnt lgkmcnt(4)\n"
"v_mfma_f32_16x16x32_bf16 v[108:111], a[184:187], v[104:107], v[108:111]\n"
"v_exp_f32 v100, v100\n"
"v_mfma_f32_16x16x32_bf16 v[124:127], a[184:187], v[136:139], v[124:127]\n"
"ds_read_b64_tr_b16 a[210:211], v37 offset:20480\n"
"v_exp_f32 v101, v101\n"
"v_mfma_f32_16x16x32_bf16 v[140:143], a[184:187], v[168:171], v[140:143]\n"
"ds_read_b64_tr_b16 a[208:209], v37 offset:16384\n"
"v_exp_f32 v102, v102\n"
"v_mfma_f32_16x16x32_bf16 v[156:159], a[184:187], v[200:203], v[156:159]\n"
"s_waitcnt lgkmcnt(5)\n"
"v_exp_f32 v103, v103\n"
"v_mfma_f32_16x16x32_bf16 v[108:111], a[188:191], v[112:115], v[108:111]\n"
"v_cvt_pk_bf16_f32 v212, v100, v101\n"
"v_cvt_pk_bf16_f32 v213, v102, v103\n"
"v_mfma_f32_16x16x32_bf16 v[124:127], a[188:191], v[144:147], v[124:127]\n"
"v_exp_f32 v116, v116\n"
"v_mfma_f32_16x16x32_bf16 v[140:143], a[188:191], v[176:179], v[140:143]\n"
"ds_read_b64_tr_b16 a[212:213], v36 offset:16896\n"
"v_exp_f32 v117, v117\n"
"v_mfma_f32_16x16x32_bf16 v[156:159], a[188:191], v[208:211], v[156:159]\n"
"s_waitcnt lgkmcnt(5)\n"
"v_exp_f32 v118, v118\n"
"v_mfma_f32_16x16x32_bf16 v[108:111], a[192:195], v[120:123], v[108:111]\n"
"ds_read_b64_tr_b16 a[214:215], v36 offset:20992\n"
"v_exp_f32 v119, v119\n"
"v_mfma_f32_16x16x32_bf16 v[124:127], a[192:195], v[152:155], v[124:127]\n"
"v_cvt_pk_bf16_f32 v220, v116, v117\n"
"v_cvt_pk_bf16_f32 v221, v118, v119\n"
"v_mfma_f32_16x16x32_bf16 v[140:143], a[192:195], v[184:187], v[140:143]\n"
"v_exp_f32 v132, v132\n"
"v_mfma_f32_16x16x32_bf16 v[156:159], a[192:195], v[216:219], v[156:159]\n"
"s_waitcnt vmcnt(13)\n"
"s_barrier\n"
"v_exp_f32 v133, v133\n"
"v_mfma_f32_16x16x32_bf16 a[160:163], v[60:63], v[180:183], a[160:163]\n"
"ds_read_b64_tr_b16 a[216:217], v37 offset:16896\n"
"v_exp_f32 v134, v134\n"
"v_mfma_f32_16x16x32_bf16 a[164:167], v[60:63], v[188:191], a[164:167]\n"
"ds_read_b64_tr_b16 a[218:219], v37 offset:20992\n"
"v_exp_f32 v135, v135\n"
"v_mfma_f32_16x16x32_bf16 a[168:171], v[60:63], v[196:199], a[168:171]\n"
"v_cvt_pk_bf16_f32 v228, v132, v133\n"
"v_cvt_pk_bf16_f32 v229, v134, v135\n"
"v_mfma_f32_16x16x32_bf16 a[172:175], v[60:63], v[204:207], a[172:175]\n"
"v_exp_f32 v148, v148\n"
"v_mfma_f32_16x16x32_bf16 a[176:179], v[60:63], v[244:247], a[176:179]\n"
"s_waitcnt lgkmcnt(6)\n"
"v_exp_f32 v149, v149\n"
"v_mfma_f32_16x16x32_bf16 a[0:3], a[204:207], v[180:183], a[0:3]\n"
"ds_read_b64_tr_b16 a[220:221], v36 offset:17408\n"
"v_exp_f32 v150, v150\n"
"v_mfma_f32_16x16x32_bf16 a[32:35], a[204:207], v[188:191], a[32:35]\n"
"ds_read_b64_tr_b16 a[222:223], v36 offset:21504\n"
"v_exp_f32 v151, v151\n"
"v_mfma_f32_16x16x32_bf16 a[64:67], a[204:207], v[196:199], a[64:67]\n"
"v_cvt_pk_bf16_f32 v236, v148, v149\n"
"v_cvt_pk_bf16_f32 v237, v150, v151\n"
"v_mfma_f32_16x16x32_bf16 a[96:99], a[204:207], v[204:207], a[96:99]\n"
"v_exp_f32 v108, v108\n"
"v_mfma_f32_16x16x32_bf16 a[128:131], a[204:207], v[244:247], a[128:131]\n"
"s_waitcnt lgkmcnt(6)\n"
"v_exp_f32 v109, v109\n"
"v_mfma_f32_16x16x32_bf16 a[4:7], a[208:211], v[180:183], a[4:7]\n"
"ds_read_b64_tr_b16 a[224:225], v37 offset:17408\n"
"v_exp_f32 v110, v110\n"
"v_mfma_f32_16x16x32_bf16 a[36:39], a[208:211], v[188:191], a[36:39]\n"
"ds_read_b64_tr_b16 a[226:227], v37 offset:21504\n"
"v_exp_f32 v111, v111\n"
"v_mfma_f32_16x16x32_bf16 a[68:71], a[208:211], v[196:199], a[68:71]\n"
"v_cvt_pk_bf16_f32 v214, v108, v109\n"
"v_cvt_pk_bf16_f32 v215, v110, v111\n"
"v_mfma_f32_16x16x32_bf16 a[100:103], a[208:211], v[204:207], a[100:103]\n"
"v_exp_f32 v124, v124\n"
"v_mfma_f32_16x16x32_bf16 a[132:135], a[208:211], v[244:247], a[132:135]\n"
"s_waitcnt lgkmcnt(6)\n"
"v_exp_f32 v125, v125\n"
"v_mfma_f32_16x16x32_bf16 a[8:11], a[212:215], v[180:183], a[8:11]\n"
"ds_read_b64_tr_b16 a[204:205], v36 offset:17920\n"
"v_exp_f32 v126, v126\n"
"v_mfma_f32_16x16x32_bf16 a[40:43], a[212:215], v[188:191], a[40:43]\n"
"ds_read_b64_tr_b16 a[206:207], v36 offset:22016\n"
"v_exp_f32 v127, v127\n"
"v_mfma_f32_16x16x32_bf16 a[72:75], a[212:215], v[196:199], a[72:75]\n"
"v_cvt_pk_bf16_f32 v222, v124, v125\n"
"v_cvt_pk_bf16_f32 v223, v126, v127\n"
"v_mfma_f32_16x16x32_bf16 a[104:107], a[212:215], v[204:207], a[104:107]\n"
"v_exp_f32 v140, v140\n"
"v_mfma_f32_16x16x32_bf16 a[136:139], a[212:215], v[244:247], a[136:139]\n"
"s_waitcnt lgkmcnt(6)\n"
"v_exp_f32 v141, v141\n"
"v_mfma_f32_16x16x32_bf16 a[12:15], a[216:219], v[180:183], a[12:15]\n"
"ds_read_b64_tr_b16 a[208:209], v37 offset:17920\n"
"v_exp_f32 v142, v142\n"
"v_mfma_f32_16x16x32_bf16 a[44:47], a[216:219], v[188:191], a[44:47]\n"
"ds_read_b64_tr_b16 a[210:211], v37 offset:22016\n"
"v_exp_f32 v143, v143\n"
"v_mfma_f32_16x16x32_bf16 a[76:79], a[216:219], v[196:199], a[76:79]\n"
"v_cvt_pk_bf16_f32 v230, v140, v141\n"
"v_cvt_pk_bf16_f32 v231, v142, v143\n"
"v_mfma_f32_16x16x32_bf16 a[108:111], a[216:219], v[204:207], a[108:111]\n"
"v_exp_f32 v156, v156\n"
"v_mfma_f32_16x16x32_bf16 a[140:143], a[216:219], v[244:247], a[140:143]\n"
"s_waitcnt lgkmcnt(6)\n"
"v_exp_f32 v157, v157\n"
"v_mfma_f32_16x16x32_bf16 a[16:19], a[220:223], v[180:183], a[16:19]\n"
"ds_read_b128 a[196:199], v28 offset:32768\n"
"v_exp_f32 v158, v158\n"
"v_mfma_f32_16x16x32_bf16 a[48:51], a[220:223], v[188:191], a[48:51]\n"
"v_exp_f32 v159, v159\n"
"v_mfma_f32_16x16x32_bf16 a[80:83], a[220:223], v[196:199], a[80:83]\n"
"v_cvt_pk_bf16_f32 v238, v156, v157\n"
"v_cvt_pk_bf16_f32 v239, v158, v159\n"
"v_mfma_f32_16x16x32_bf16 a[112:115], a[220:223], v[204:207], a[112:115]\n"
"v_mfma_f32_16x16x32_bf16 a[144:147], a[220:223], v[244:247], a[144:147]\n"
"s_waitcnt lgkmcnt(5)\n"
"v_mfma_f32_16x16x32_bf16 a[20:23], a[224:227], v[180:183], a[20:23]\n"
"ds_read_b128 a[200:203], v29 offset:32768\n"
"v_mfma_f32_16x16x32_bf16 a[52:55], a[224:227], v[188:191], a[52:55]\n"
"v_mfma_f32_16x16x32_bf16 a[84:87], a[224:227], v[196:199], a[84:87]\n"
"v_mfma_f32_16x16x32_bf16 a[116:119], a[224:227], v[204:207], a[116:119]\n"
"v_mfma_f32_16x16x32_bf16 a[148:151], a[224:227], v[244:247], a[148:151]\n"
"s_waitcnt lgkmcnt(4)\n"
"v_mfma_f32_16x16x32_bf16 a[24:27], a[204:207], v[180:183], a[24:27]\n"
"ds_read_b128 a[180:183], v30 offset:32768\n"
"v_mfma_f32_16x16x32_bf16 a[56:59], a[204:207], v[188:191], a[56:59]\n"
"v_mfma_f32_16x16x32_bf16 a[88:91], a[204:207], v[196:199], a[88:91]\n"
"v_mfma_f32_16x16x32_bf16 a[120:123], a[204:207], v[204:207], a[120:123]\n"
"v_mfma_f32_16x16x32_bf16 a[152:155], a[204:207], v[244:247], a[152:155]\n"
"s_waitcnt lgkmcnt(3)\n"
"v_mfma_f32_16x16x32_bf16 a[28:31], a[208:211], v[180:183], a[28:31]\n"
"ds_read_b128 a[184:187], v31 offset:32768\n"
"v_mfma_f32_16x16x32_bf16 a[60:63], a[208:211], v[188:191], a[60:63]\n"
"v_mfma_f32_16x16x32_bf16 a[92:95], a[208:211], v[196:199], a[92:95]\n"
"v_mfma_f32_16x16x32_bf16 a[124:127], a[208:211], v[204:207], a[124:127]\n"
"v_mfma_f32_16x16x32_bf16 a[156:159], a[208:211], v[244:247], a[156:159]\n"
"s_waitcnt lgkmcnt(3)\n"
"v_mfma_f32_16x16x32_bf16 v[100:103], a[196:199], v[96:99], v[40:43]\n"
"ds_read_b128 a[188:191], v28 offset:36864\n"
"v_mfma_f32_16x16x32_bf16 v[116:119], a[196:199], v[128:131], v[44:47]\n"
"s_add_u32 m0, s61, 65536\n"
"s_mov_b32 s66, 196608\n"
"v_mfma_f32_16x16x32_bf16 v[132:135], a[196:199], v[160:163], v[48:51]\n"
"buffer_load_dwordx4 v95, s[52:55], s66 offen nt lds\n"
"v_mfma_f32_16x16x32_bf16 v[148:151], a[196:199], v[192:195], v[52:55]\n"
"v_mfma_f32_16x16x32_bf16 v[164:167], a[196:199], v[224:227], v[56:59]\n"
"s_waitcnt lgkmcnt(3)\n"
"v_mfma_f32_16x16x32_bf16 v[100:103], a[200:203], v[104:107], v[100:103]\n"
"ds_read_b128 a[192:195], v29 offset:36864\n"
"v_mfma_f32_16x16x32_bf16 v[116:119], a[200:203], v[136:139], v[116:119]\n"
"buffer_load_dwordx4 v27, s[52:55], s66 offen offset:1024 nt lds\n"
"v_mfma_f32_16x16x32_bf16 v[132:135], a[200:203], v[168:171], v[132:135]\n"
"v_mfma_f32_16x16x32_bf16 v[148:151], a[200:203], v[200:203], v[148:151]\n"
"v_mfma_f32_16x16x32_bf16 v[164:167], a[200:203], v[232:235], v[164:167]\n"
"s_waitcnt lgkmcnt(3)\n"
"v_mfma_f32_16x16x32_bf16 v[100:103], a[180:183], v[112:115], v[100:103]\n"
"ds_read_b128 a[196:199], v30 offset:36864\n"
"v_mfma_f32_16x16x32_bf16 v[116:119], a[180:183], v[144:147], v[116:119]\n"
"buffer_load_dwordx4 v95, s[52:55], s66 offen offset:2048 nt lds\n"
"v_mfma_f32_16x16x32_bf16 v[132:135], a[180:183], v[176:179], v[132:135]\n"
"v_mfma_f32_16x16x32_bf16 v[148:151], a[180:183], v[208:211], v[148:151]\n"
"v_mfma_f32_16x16x32_bf16 v[164:167], a[180:183], v[240:243], v[164:167]\n"
"s_waitcnt lgkmcnt(3)\n"
"v_mfma_f32_16x16x32_bf16 v[100:103], a[184:187], v[120:123], v[100:103]\n"
"ds_read_b128 a[200:203], v31 offset:36864\n"
"v_mfma_f32_16x16x32_bf16 v[116:119], a[184:187], v[152:155], v[116:119]\n"
"buffer_load_dwordx4 v27, s[52:55], s66 offen offset:3072 nt lds\n"
"v_mfma_f32_16x16x32_bf16 v[132:135], a[184:187], v[184:187], v[132:135]\n"
"v_mfma_f32_16x16x32_bf16 v[148:151], a[184:187], v[216:219], v[148:151]\n"
"v_mfma_f32_16x16x32_bf16 v[164:167], a[184:187], v[248:251], v[164:167]\n"
"s_waitcnt lgkmcnt(3)\n"
"v_mfma_f32_16x16x32_bf16 v[108:111], a[188:191], v[96:99], v[40:43]\n"
"v_mfma_f32_16x16x32_bf16 v[124:127], a[188:191], v[128:131], v[44:47]\n"
"v_mfma_f32_16x16x32_bf16 v[140:143], a[188:191], v[160:163], v[48:51]\n"
"v_mfma_f32_16x16x32_bf16 v[156:159], a[188:191], v[192:195], v[52:55]\n"
"v_exp_f32 v100, v100\n"
"v_mfma_f32_16x16x32_bf16 v[172:175], a[188:191], v[224:227], v[56:59]\n"
"s_waitcnt lgkmcnt(2)\n"
"v_exp_f32 v101, v101\n"
"v_mfma_f32_16x16x32_bf16 v[108:111], a[192:195], v[104:107], v[108:111]\n"
"v_exp_f32 v102, v102\n"
"v_mfma_f32_16x16x32_bf16 v[124:127], a[192:195], v[136:139], v[124:127]\n"
"v_exp_f32 v103, v103\n"
"v_mfma_f32_16x16x32_bf16 v[140:143], a[192:195], v[168:171], v[140:143]\n"
"ds_read_b64_tr_b16 a[214:215], v38 offset:20480\n"
"v_cvt_pk_bf16_f32 v180, v100, v101\n"
"v_cvt_pk_bf16_f32 v181, v102, v103\n"
"v_mfma_f32_16x16x32_bf16 v[156:159], a[192:195], v[200:203], v[156:159]\n"
"ds_read_b64_tr_b16 a[212:213], v38 offset:16384\n"
"v_exp_f32 v116, v116\n"
"v_mfma_f32_16x16x32_bf16 v[172:175], a[192:195], v[232:235], v[172:175]\n"
"s_waitcnt lgkmcnt(3)\n"
"v_exp_f32 v117, v117\n"
"v_mfma_f32_16x16x32_bf16 v[108:111], a[196:199], v[112:115], v[108:111]\n"
"v_exp_f32 v118, v118\n"
"v_mfma_f32_16x16x32_bf16 v[124:127], a[196:199], v[144:147], v[124:127]\n"
"v_exp_f32 v119, v119\n"
"v_mfma_f32_16x16x32_bf16 v[140:143], a[196:199], v[176:179], v[140:143]\n"
"ds_read_b64_tr_b16 a[216:217], v39 offset:16384\n"
"v_cvt_pk_bf16_f32 v188, v116, v117\n"
"v_cvt_pk_bf16_f32 v189, v118, v119\n"
"v_mfma_f32_16x16x32_bf16 v[156:159], a[196:199], v[208:211], v[156:159]\n"
"ds_read_b64_tr_b16 a[218:219], v39 offset:20480\n"
"v_exp_f32 v132, v132\n"
"v_mfma_f32_16x16x32_bf16 v[172:175], a[196:199], v[240:243], v[172:175]\n"
"s_waitcnt lgkmcnt(4)\n"
"v_exp_f32 v133, v133\n"
"v_mfma_f32_16x16x32_bf16 v[108:111], a[200:203], v[120:123], v[108:111]\n"
"v_exp_f32 v134, v134\n"
"v_mfma_f32_16x16x32_bf16 v[124:127], a[200:203], v[152:155], v[124:127]\n"
"ds_read_b64_tr_b16 a[220:221], v38 offset:16896\n"
"v_exp_f32 v135, v135\n"
"v_mfma_f32_16x16x32_bf16 v[140:143], a[200:203], v[184:187], v[140:143]\n"
"ds_read_b64_tr_b16 a[222:223], v38 offset:20992\n"
"v_cvt_pk_bf16_f32 v196, v132, v133\n"
"v_cvt_pk_bf16_f32 v197, v134, v135\n"
"v_mfma_f32_16x16x32_bf16 v[156:159], a[200:203], v[216:219], v[156:159]\n"
"v_exp_f32 v148, v148\n"
"v_mfma_f32_16x16x32_bf16 v[172:175], a[200:203], v[248:251], v[172:175]\n"
"v_exp_f32 v149, v149\n"
"v_mfma_f32_16x16x32_bf16 a[160:163], v[60:63], v[212:215], a[160:163]\n"
"ds_read_b64_tr_b16 a[224:225], v39 offset:16896\n"
"v_exp_f32 v150, v150\n"
"v_mfma_f32_16x16x32_bf16 a[164:167], v[60:63], v[220:223], a[164:167]\n"
"ds_read_b64_tr_b16 a[226:227], v39 offset:20992\n"
"v_exp_f32 v151, v151\n"
"v_mfma_f32_16x16x32_bf16 a[168:171], v[60:63], v[228:231], a[168:171]\n"
"v_cvt_pk_bf16_f32 v204, v148, v149\n"
"v_cvt_pk_bf16_f32 v205, v150, v151\n"
"v_mfma_f32_16x16x32_bf16 a[172:175], v[60:63], v[236:239], a[172:175]\n"
"s_waitcnt lgkmcnt(6)\n"
"v_exp_f32 v164, v164\n"
"v_mfma_f32_16x16x32_bf16 a[0:3], a[212:215], v[212:215], a[0:3]\n"
"ds_read_b64_tr_b16 a[204:205], v38 offset:17408\n"
"v_exp_f32 v165, v165\n"
"v_mfma_f32_16x16x32_bf16 a[32:35], a[212:215], v[220:223], a[32:35]\n"
"ds_read_b64_tr_b16 a[206:207], v38 offset:21504\n"
"v_exp_f32 v166, v166\n"
"v_mfma_f32_16x16x32_bf16 a[64:67], a[212:215], v[228:231], a[64:67]\n"
"v_exp_f32 v167, v167\n"
"v_mfma_f32_16x16x32_bf16 a[96:99], a[212:215], v[236:239], a[96:99]\n"
"s_waitcnt lgkmcnt(6)\n"
"v_cvt_pk_bf16_f32 v244, v164, v165\n"
"v_cvt_pk_bf16_f32 v245, v166, v167\n"
"v_mfma_f32_16x16x32_bf16 a[4:7], a[216:219], v[212:215], a[4:7]\n"
"ds_read_b64_tr_b16 a[208:209], v39 offset:17408\n"
"v_exp_f32 v108, v108\n"
"v_mfma_f32_16x16x32_bf16 a[36:39], a[216:219], v[220:223], a[36:39]\n"
"ds_read_b64_tr_b16 a[210:211], v39 offset:21504\n"
"v_exp_f32 v109, v109\n"
"v_mfma_f32_16x16x32_bf16 a[68:71], a[216:219], v[228:231], a[68:71]\n"
"v_exp_f32 v110, v110\n"
"v_mfma_f32_16x16x32_bf16 a[100:103], a[216:219], v[236:239], a[100:103]\n"
"s_waitcnt lgkmcnt(6)\n"
"v_exp_f32 v111, v111\n"
"v_mfma_f32_16x16x32_bf16 a[8:11], a[220:223], v[212:215], a[8:11]\n"
"ds_read_b64_tr_b16 a[212:213], v38 offset:17920\n"
"v_cvt_pk_bf16_f32 v182, v108, v109\n"
"v_cvt_pk_bf16_f32 v183, v110, v111\n"
"v_mfma_f32_16x16x32_bf16 a[40:43], a[220:223], v[220:223], a[40:43]\n"
"ds_read_b64_tr_b16 a[214:215], v38 offset:22016\n"
"v_exp_f32 v124, v124\n"
"v_mfma_f32_16x16x32_bf16 a[72:75], a[220:223], v[228:231], a[72:75]\n"
"v_exp_f32 v125, v125\n"
"v_mfma_f32_16x16x32_bf16 a[104:107], a[220:223], v[236:239], a[104:107]\n"
"s_waitcnt lgkmcnt(6)\n"
"v_exp_f32 v126, v126\n"
"v_mfma_f32_16x16x32_bf16 a[12:15], a[224:227], v[212:215], a[12:15]\n"
"ds_read_b64_tr_b16 a[216:217], v39 offset:17920\n"
"v_exp_f32 v127, v127\n"
"v_mfma_f32_16x16x32_bf16 a[44:47], a[224:227], v[220:223], a[44:47]\n"
"ds_read_b64_tr_b16 a[218:219], v39 offset:22016\n"
"v_cvt_pk_bf16_f32 v190, v124, v125\n"
"v_cvt_pk_bf16_f32 v191, v126, v127\n"
"v_mfma_f32_16x16x32_bf16 a[76:79], a[224:227], v[228:231], a[76:79]\n"
"v_exp_f32 v140, v140\n"
"v_mfma_f32_16x16x32_bf16 a[108:111], a[224:227], v[236:239], a[108:111]\n"
"s_waitcnt lgkmcnt(6)\n"
"v_exp_f32 v141, v141\n"
"v_mfma_f32_16x16x32_bf16 a[16:19], a[204:207], v[212:215], a[16:19]\n"
"ds_read_b128 a[180:183], v32 offset:32768\n"
"v_exp_f32 v142, v142\n"
"v_mfma_f32_16x16x32_bf16 a[48:51], a[204:207], v[220:223], a[48:51]\n"
"v_exp_f32 v143, v143\n"
"v_mfma_f32_16x16x32_bf16 a[80:83], a[204:207], v[228:231], a[80:83]\n"
"v_cvt_pk_bf16_f32 v198, v140, v141\n"
"v_cvt_pk_bf16_f32 v199, v142, v143\n"
"v_mfma_f32_16x16x32_bf16 a[112:115], a[204:207], v[236:239], a[112:115]\n"
"s_waitcnt lgkmcnt(5)\n"
"v_exp_f32 v156, v156\n"
"v_mfma_f32_16x16x32_bf16 a[20:23], a[208:211], v[212:215], a[20:23]\n"
"ds_read_b128 a[184:187], v33 offset:32768\n"
"v_exp_f32 v157, v157\n"
"v_mfma_f32_16x16x32_bf16 a[52:55], a[208:211], v[220:223], a[52:55]\n"
"v_exp_f32 v158, v158\n"
"v_mfma_f32_16x16x32_bf16 a[84:87], a[208:211], v[228:231], a[84:87]\n"
"v_exp_f32 v159, v159\n"
"v_mfma_f32_16x16x32_bf16 a[116:119], a[208:211], v[236:239], a[116:119]\n"
"s_waitcnt lgkmcnt(4)\n"
"v_cvt_pk_bf16_f32 v206, v156, v157\n"
"v_cvt_pk_bf16_f32 v207, v158, v159\n"
"v_mfma_f32_16x16x32_bf16 a[24:27], a[212:215], v[212:215], a[24:27]\n"
"ds_read_b128 a[188:191], v34 offset:32768\n"
"v_exp_f32 v172, v172\n"
"v_mfma_f32_16x16x32_bf16 a[56:59], a[212:215], v[220:223], a[56:59]\n"
"v_exp_f32 v173, v173\n"
"v_mfma_f32_16x16x32_bf16 a[88:91], a[212:215], v[228:231], a[88:91]\n"
"v_exp_f32 v174, v174\n"
"v_mfma_f32_16x16x32_bf16 a[120:123], a[212:215], v[236:239], a[120:123]\n"
"s_waitcnt lgkmcnt(3)\n"
"v_exp_f32 v175, v175\n"
"v_mfma_f32_16x16x32_bf16 a[28:31], a[216:219], v[212:215], a[28:31]\n"
"ds_read_b128 a[192:195], v35 offset:32768\n"
"v_cvt_pk_bf16_f32 v246, v172, v173\n"
"v_cvt_pk_bf16_f32 v247, v174, v175\n"
"v_mfma_f32_16x16x32_bf16 a[60:63], a[216:219], v[220:223], a[60:63]\n"
"v_mfma_f32_16x16x32_bf16 a[92:95], a[216:219], v[228:231], a[92:95]\n"
"v_mfma_f32_16x16x32_bf16 a[124:127], a[216:219], v[236:239], a[124:127]\n"
"s_waitcnt lgkmcnt(3)\n"
"v_mfma_f32_16x16x32_bf16 v[100:103], a[180:183], v[96:99], v[40:43]\n"
"ds_read_b128 a[196:199], v32 offset:36864\n"
"v_mfma_f32_16x16x32_bf16 v[116:119], a[180:183], v[128:131], v[44:47]\n"
"s_add_u32 m0, s61, 16384\n"
"s_mov_b32 s65, 212992\n"
"v_mfma_f32_16x16x32_bf16 v[132:135], a[180:183], v[160:163], v[48:51]\n"
"buffer_load_dwordx4 v24, s[48:51], s65 offen nt lds\n"
"v_mfma_f32_16x16x32_bf16 v[148:151], a[180:183], v[192:195], v[52:55]\n"
"s_waitcnt lgkmcnt(3)\n"
"v_mfma_f32_16x16x32_bf16 v[100:103], a[184:187], v[104:107], v[100:103]\n"
"ds_read_b128 a[200:203], v33 offset:36864\n"
"v_mfma_f32_16x16x32_bf16 v[116:119], a[184:187], v[136:139], v[116:119]\n"
"v_mfma_f32_16x16x32_bf16 v[132:135], a[184:187], v[168:171], v[132:135]\n"
"buffer_load_dwordx4 v25, s[48:51], s65 offen offset:1024 nt lds\n"
"v_mfma_f32_16x16x32_bf16 v[148:151], a[184:187], v[200:203], v[148:151]\n"
"s_waitcnt lgkmcnt(3)\n"
"v_mfma_f32_16x16x32_bf16 v[100:103], a[188:191], v[112:115], v[100:103]\n"
"ds_read_b128 a[180:183], v34 offset:36864\n"
"v_mfma_f32_16x16x32_bf16 v[116:119], a[188:191], v[144:147], v[116:119]\n"
"v_mfma_f32_16x16x32_bf16 v[132:135], a[188:191], v[176:179], v[132:135]\n"
"buffer_load_dwordx4 v93, s[48:51], s65 offen offset:2048 nt lds\n"
"v_mfma_f32_16x16x32_bf16 v[148:151], a[188:191], v[208:211], v[148:151]\n"
"s_waitcnt lgkmcnt(3)\n"
"v_mfma_f32_16x16x32_bf16 v[100:103], a[192:195], v[120:123], v[100:103]\n"
"ds_read_b128 a[184:187], v35 offset:36864\n"
"v_mfma_f32_16x16x32_bf16 v[116:119], a[192:195], v[152:155], v[116:119]\n"
"v_mfma_f32_16x16x32_bf16 v[132:135], a[192:195], v[184:187], v[132:135]\n"
"buffer_load_dwordx4 v94, s[48:51], s65 offen offset:3072 nt lds\n"
"v_mfma_f32_16x16x32_bf16 v[148:151], a[192:195], v[216:219], v[148:151]\n"
"s_waitcnt lgkmcnt(3)\n"
"v_mfma_f32_16x16x32_bf16 v[108:111], a[196:199], v[96:99], v[40:43]\n"
"v_mfma_f32_16x16x32_bf16 v[124:127], a[196:199], v[128:131], v[44:47]\n"
"ds_read_b64_tr_b16 a[220:221], v36 offset:32768\n"
"v_mfma_f32_16x16x32_bf16 v[140:143], a[196:199], v[160:163], v[48:51]\n"
"ds_read_b64_tr_b16 a[222:223], v36 offset:36864\n"
"v_mfma_f32_16x16x32_bf16 v[156:159], a[196:199], v[192:195], v[52:55]\n"
"s_waitcnt lgkmcnt(4)\n"
"v_mfma_f32_16x16x32_bf16 v[108:111], a[200:203], v[104:107], v[108:111]\n"
"v_exp_f32 v100, v100\n"
"v_mfma_f32_16x16x32_bf16 v[124:127], a[200:203], v[136:139], v[124:127]\n"
"ds_read_b64_tr_b16 a[226:227], v37 offset:36864\n"
"v_exp_f32 v101, v101\n"
"v_mfma_f32_16x16x32_bf16 v[140:143], a[200:203], v[168:171], v[140:143]\n"
"ds_read_b64_tr_b16 a[224:225], v37 offset:32768\n"
"v_exp_f32 v102, v102\n"
"v_mfma_f32_16x16x32_bf16 v[156:159], a[200:203], v[200:203], v[156:159]\n"
"s_waitcnt lgkmcnt(5)\n"
"v_exp_f32 v103, v103\n"
"v_mfma_f32_16x16x32_bf16 v[108:111], a[180:183], v[112:115], v[108:111]\n"
"v_cvt_pk_bf16_f32 v212, v100, v101\n"
"v_cvt_pk_bf16_f32 v213, v102, v103\n"
"v_mfma_f32_16x16x32_bf16 v[124:127], a[180:183], v[144:147], v[124:127]\n"
"v_exp_f32 v116, v116\n"
"v_mfma_f32_16x16x32_bf16 v[140:143], a[180:183], v[176:179], v[140:143]\n"
"ds_read_b64_tr_b16 a[204:205], v36 offset:33280\n"
"v_exp_f32 v117, v117\n"
"v_mfma_f32_16x16x32_bf16 v[156:159], a[180:183], v[208:211], v[156:159]\n"
"s_waitcnt lgkmcnt(5)\n"
"v_exp_f32 v118, v118\n"
"v_mfma_f32_16x16x32_bf16 v[108:111], a[184:187], v[120:123], v[108:111]\n"
"ds_read_b64_tr_b16 a[206:207], v36 offset:37376\n"
"v_exp_f32 v119, v119\n"
"v_mfma_f32_16x16x32_bf16 v[124:127], a[184:187], v[152:155], v[124:127]\n"
"v_cvt_pk_bf16_f32 v220, v116, v117\n"
"v_cvt_pk_bf16_f32 v221, v118, v119\n"
"v_mfma_f32_16x16x32_bf16 v[140:143], a[184:187], v[184:187], v[140:143]\n"
"v_exp_f32 v132, v132\n"
"v_mfma_f32_16x16x32_bf16 v[156:159], a[184:187], v[216:219], v[156:159]\n"
"s_waitcnt vmcnt(12)\n"
"s_barrier\n"
"v_exp_f32 v133, v133\n"
"v_mfma_f32_16x16x32_bf16 a[160:163], v[60:63], v[180:183], a[160:163]\n"
"ds_read_b64_tr_b16 a[208:209], v37 offset:33280\n"
"v_exp_f32 v134, v134\n"
"v_mfma_f32_16x16x32_bf16 a[164:167], v[60:63], v[188:191], a[164:167]\n"
"ds_read_b64_tr_b16 a[210:211], v37 offset:37376\n"
"v_exp_f32 v135, v135\n"
"v_mfma_f32_16x16x32_bf16 a[168:171], v[60:63], v[196:199], a[168:171]\n"
"v_cvt_pk_bf16_f32 v228, v132, v133\n"
"v_cvt_pk_bf16_f32 v229, v134, v135\n"
"v_mfma_f32_16x16x32_bf16 a[172:175], v[60:63], v[204:207], a[172:175]\n"
"v_exp_f32 v148, v148\n"
"v_mfma_f32_16x16x32_bf16 a[176:179], v[60:63], v[244:247], a[176:179]\n"
"s_waitcnt lgkmcnt(6)\n"
"v_exp_f32 v149, v149\n"
"v_mfma_f32_16x16x32_bf16 a[0:3], a[220:223], v[180:183], a[0:3]\n"
"ds_read_b64_tr_b16 a[212:213], v36 offset:33792\n"
"v_exp_f32 v150, v150\n"
"v_mfma_f32_16x16x32_bf16 a[32:35], a[220:223], v[188:191], a[32:35]\n"
"ds_read_b64_tr_b16 a[214:215], v36 offset:37888\n"
"v_exp_f32 v151, v151\n"
"v_mfma_f32_16x16x32_bf16 a[64:67], a[220:223], v[196:199], a[64:67]\n"
"v_cvt_pk_bf16_f32 v236, v148, v149\n"
"v_cvt_pk_bf16_f32 v237, v150, v151\n"
"v_mfma_f32_16x16x32_bf16 a[96:99], a[220:223], v[204:207], a[96:99]\n"
"v_exp_f32 v108, v108\n"
"v_mfma_f32_16x16x32_bf16 a[128:131], a[220:223], v[244:247], a[128:131]\n"
"s_waitcnt lgkmcnt(6)\n"
"v_exp_f32 v109, v109\n"
"v_mfma_f32_16x16x32_bf16 a[4:7], a[224:227], v[180:183], a[4:7]\n"
"ds_read_b64_tr_b16 a[216:217], v37 offset:33792\n"
"v_exp_f32 v110, v110\n"
"v_mfma_f32_16x16x32_bf16 a[36:39], a[224:227], v[188:191], a[36:39]\n"
"ds_read_b64_tr_b16 a[218:219], v37 offset:37888\n"
"v_exp_f32 v111, v111\n"
"v_mfma_f32_16x16x32_bf16 a[68:71], a[224:227], v[196:199], a[68:71]\n"
"v_cvt_pk_bf16_f32 v214, v108, v109\n"
"v_cvt_pk_bf16_f32 v215, v110, v111\n"
"v_mfma_f32_16x16x32_bf16 a[100:103], a[224:227], v[204:207], a[100:103]\n"
"v_exp_f32 v124, v124\n"
"v_mfma_f32_16x16x32_bf16 a[132:135], a[224:227], v[244:247], a[132:135]\n"
"s_waitcnt lgkmcnt(6)\n"
"v_exp_f32 v125, v125\n"
"v_mfma_f32_16x16x32_bf16 a[8:11], a[204:207], v[180:183], a[8:11]\n"
"ds_read_b64_tr_b16 a[220:221], v36 offset:34304\n"
"v_exp_f32 v126, v126\n"
"v_mfma_f32_16x16x32_bf16 a[40:43], a[204:207], v[188:191], a[40:43]\n"
"ds_read_b64_tr_b16 a[222:223], v36 offset:38400\n"
"v_exp_f32 v127, v127\n"
"v_mfma_f32_16x16x32_bf16 a[72:75], a[204:207], v[196:199], a[72:75]\n"
"v_cvt_pk_bf16_f32 v222, v124, v125\n"
"v_cvt_pk_bf16_f32 v223, v126, v127\n"
"v_mfma_f32_16x16x32_bf16 a[104:107], a[204:207], v[204:207], a[104:107]\n"
"v_exp_f32 v140, v140\n"
"v_mfma_f32_16x16x32_bf16 a[136:139], a[204:207], v[244:247], a[136:139]\n"
"s_waitcnt lgkmcnt(6)\n"
"v_exp_f32 v141, v141\n"
"v_mfma_f32_16x16x32_bf16 a[12:15], a[208:211], v[180:183], a[12:15]\n"
"ds_read_b64_tr_b16 a[224:225], v37 offset:34304\n"
"v_exp_f32 v142, v142\n"
"v_mfma_f32_16x16x32_bf16 a[44:47], a[208:211], v[188:191], a[44:47]\n"
"ds_read_b64_tr_b16 a[226:227], v37 offset:38400\n"
"v_exp_f32 v143, v143\n"
"v_mfma_f32_16x16x32_bf16 a[76:79], a[208:211], v[196:199], a[76:79]\n"
"v_cvt_pk_bf16_f32 v230, v140, v141\n"
"v_cvt_pk_bf16_f32 v231, v142, v143\n"
"v_mfma_f32_16x16x32_bf16 a[108:111], a[208:211], v[204:207], a[108:111]\n"
"v_exp_f32 v156, v156\n"
"v_mfma_f32_16x16x32_bf16 a[140:143], a[208:211], v[244:247], a[140:143]\n"
"s_waitcnt lgkmcnt(6)\n"
"v_exp_f32 v157, v157\n"
"v_mfma_f32_16x16x32_bf16 a[16:19], a[212:215], v[180:183], a[16:19]\n"
"ds_read_b128 a[188:191], v28 offset:49152\n"
"v_exp_f32 v158, v158\n"
"v_mfma_f32_16x16x32_bf16 a[48:51], a[212:215], v[188:191], a[48:51]\n"
"v_exp_f32 v159, v159\n"
"v_mfma_f32_16x16x32_bf16 a[80:83], a[212:215], v[196:199], a[80:83]\n"
"v_cvt_pk_bf16_f32 v238, v156, v157\n"
"v_cvt_pk_bf16_f32 v239, v158, v159\n"
"v_mfma_f32_16x16x32_bf16 a[112:115], a[212:215], v[204:207], a[112:115]\n"
"v_mfma_f32_16x16x32_bf16 a[144:147], a[212:215], v[244:247], a[144:147]\n"
"s_waitcnt lgkmcnt(5)\n"
"v_mfma_f32_16x16x32_bf16 a[20:23], a[216:219], v[180:183], a[20:23]\n"
"ds_read_b128 a[192:195], v29 offset:49152\n"
"v_mfma_f32_16x16x32_bf16 a[52:55], a[216:219], v[188:191], a[52:55]\n"
"v_mfma_f32_16x16x32_bf16 a[84:87], a[216:219], v[196:199], a[84:87]\n"
"v_mfma_f32_16x16x32_bf16 a[116:119], a[216:219], v[204:207], a[116:119]\n"
"v_mfma_f32_16x16x32_bf16 a[148:151], a[216:219], v[244:247], a[148:151]\n"
"s_waitcnt lgkmcnt(4)\n"
"v_mfma_f32_16x16x32_bf16 a[24:27], a[220:223], v[180:183], a[24:27]\n"
"ds_read_b128 a[196:199], v30 offset:49152\n"
"v_mfma_f32_16x16x32_bf16 a[56:59], a[220:223], v[188:191], a[56:59]\n"
"v_mfma_f32_16x16x32_bf16 a[88:91], a[220:223], v[196:199], a[88:91]\n"
"v_mfma_f32_16x16x32_bf16 a[120:123], a[220:223], v[204:207], a[120:123]\n"
"v_mfma_f32_16x16x32_bf16 a[152:155], a[220:223], v[244:247], a[152:155]\n"
"s_waitcnt lgkmcnt(3)\n"
"v_mfma_f32_16x16x32_bf16 a[28:31], a[224:227], v[180:183], a[28:31]\n"
"ds_read_b128 a[200:203], v31 offset:49152\n"
"v_mfma_f32_16x16x32_bf16 a[60:63], a[224:227], v[188:191], a[60:63]\n"
"v_mfma_f32_16x16x32_bf16 a[92:95], a[224:227], v[196:199], a[92:95]\n"
"v_mfma_f32_16x16x32_bf16 a[124:127], a[224:227], v[204:207], a[124:127]\n"
"v_mfma_f32_16x16x32_bf16 a[156:159], a[224:227], v[244:247], a[156:159]\n"
"s_waitcnt lgkmcnt(3)\n"
"v_mfma_f32_16x16x32_bf16 v[100:103], a[188:191], v[96:99], v[40:43]\n"
"ds_read_b128 a[180:183], v28 offset:53248\n"
"v_mfma_f32_16x16x32_bf16 v[116:119], a[188:191], v[128:131], v[44:47]\n"
"s_add_u32 m0, s61, 81920\n"
"s_mov_b32 s66, 212992\n"
"v_mfma_f32_16x16x32_bf16 v[132:135], a[188:191], v[160:163], v[48:51]\n"
"buffer_load_dwordx4 v95, s[52:55], s66 offen nt lds\n"
"v_mfma_f32_16x16x32_bf16 v[148:151], a[188:191], v[192:195], v[52:55]\n"
"v_mfma_f32_16x16x32_bf16 v[164:167], a[188:191], v[224:227], v[56:59]\n"
"s_waitcnt lgkmcnt(3)\n"
"v_mfma_f32_16x16x32_bf16 v[100:103], a[192:195], v[104:107], v[100:103]\n"
"ds_read_b128 a[184:187], v29 offset:53248\n"
"v_mfma_f32_16x16x32_bf16 v[116:119], a[192:195], v[136:139], v[116:119]\n"
"buffer_load_dwordx4 v27, s[52:55], s66 offen offset:1024 nt lds\n"
"v_mfma_f32_16x16x32_bf16 v[132:135], a[192:195], v[168:171], v[132:135]\n"
"v_mfma_f32_16x16x32_bf16 v[148:151], a[192:195], v[200:203], v[148:151]\n"
"v_mfma_f32_16x16x32_bf16 v[164:167], a[192:195], v[232:235], v[164:167]\n"
"s_waitcnt lgkmcnt(3)\n"
"v_mfma_f32_16x16x32_bf16 v[100:103], a[196:199], v[112:115], v[100:103]\n"
"ds_read_b128 a[188:191], v30 offset:53248\n"
"v_mfma_f32_16x16x32_bf16 v[116:119], a[196:199], v[144:147], v[116:119]\n"
"buffer_load_dwordx4 v95, s[52:55], s66 offen offset:2048 nt lds\n"
"v_mfma_f32_16x16x32_bf16 v[132:135], a[196:199], v[176:179], v[132:135]\n"
"v_mfma_f32_16x16x32_bf16 v[148:151], a[196:199], v[208:211], v[148:151]\n"
"v_mfma_f32_16x16x32_bf16 v[164:167], a[196:199], v[240:243], v[164:167]\n"
"s_waitcnt lgkmcnt(3)\n"
"v_mfma_f32_16x16x32_bf16 v[100:103], a[200:203], v[120:123], v[100:103]\n"
"ds_read_b128 a[192:195], v31 offset:53248\n"
"v_mfma_f32_16x16x32_bf16 v[116:119], a[200:203], v[152:155], v[116:119]\n"
"buffer_load_dwordx4 v27, s[52:55], s66 offen offset:3072 nt lds\n"
"v_mfma_f32_16x16x32_bf16 v[132:135], a[200:203], v[184:187], v[132:135]\n"
"v_mfma_f32_16x16x32_bf16 v[148:151], a[200:203], v[216:219], v[148:151]\n"
"v_mfma_f32_16x16x32_bf16 v[164:167], a[200:203], v[248:251], v[164:167]\n"
"s_waitcnt lgkmcnt(3)\n"
"v_mfma_f32_16x16x32_bf16 v[108:111], a[180:183], v[96:99], v[40:43]\n"
"v_mfma_f32_16x16x32_bf16 v[124:127], a[180:183], v[128:131], v[44:47]\n"
"v_mfma_f32_16x16x32_bf16 v[140:143], a[180:183], v[160:163], v[48:51]\n"
"v_mfma_f32_16x16x32_bf16 v[156:159], a[180:183], v[192:195], v[52:55]\n"
"v_exp_f32 v100, v100\n"
"v_mfma_f32_16x16x32_bf16 v[172:175], a[180:183], v[224:227], v[56:59]\n"
"s_waitcnt lgkmcnt(2)\n"
"v_exp_f32 v101, v101\n"
"v_mfma_f32_16x16x32_bf16 v[108:111], a[184:187], v[104:107], v[108:111]\n"
"v_exp_f32 v102, v102\n"
"v_mfma_f32_16x16x32_bf16 v[124:127], a[184:187], v[136:139], v[124:127]\n"
"v_exp_f32 v103, v103\n"
"v_mfma_f32_16x16x32_bf16 v[140:143], a[184:187], v[168:171], v[140:143]\n"
"ds_read_b64_tr_b16 a[206:207], v38 offset:36864\n"
"v_cvt_pk_bf16_f32 v180, v100, v101\n"
"v_cvt_pk_bf16_f32 v181, v102, v103\n"
"v_mfma_f32_16x16x32_bf16 v[156:159], a[184:187], v[200:203], v[156:159]\n"
"ds_read_b64_tr_b16 a[204:205], v38 offset:32768\n"
"v_exp_f32 v116, v116\n"
"v_mfma_f32_16x16x32_bf16 v[172:175], a[184:187], v[232:235], v[172:175]\n"
"s_waitcnt lgkmcnt(3)\n"
"v_exp_f32 v117, v117\n"
"v_mfma_f32_16x16x32_bf16 v[108:111], a[188:191], v[112:115], v[108:111]\n"
"v_exp_f32 v118, v118\n"
"v_mfma_f32_16x16x32_bf16 v[124:127], a[188:191], v[144:147], v[124:127]\n"
"v_exp_f32 v119, v119\n"
"v_mfma_f32_16x16x32_bf16 v[140:143], a[188:191], v[176:179], v[140:143]\n"
"ds_read_b64_tr_b16 a[208:209], v39 offset:32768\n"
"v_cvt_pk_bf16_f32 v188, v116, v117\n"
"v_cvt_pk_bf16_f32 v189, v118, v119\n"
"v_mfma_f32_16x16x32_bf16 v[156:159], a[188:191], v[208:211], v[156:159]\n"
"ds_read_b64_tr_b16 a[210:211], v39 offset:36864\n"
"v_exp_f32 v132, v132\n"
"v_mfma_f32_16x16x32_bf16 v[172:175], a[188:191], v[240:243], v[172:175]\n"
"s_waitcnt lgkmcnt(4)\n"
"v_exp_f32 v133, v133\n"
"v_mfma_f32_16x16x32_bf16 v[108:111], a[192:195], v[120:123], v[108:111]\n"
"v_exp_f32 v134, v134\n"
"v_mfma_f32_16x16x32_bf16 v[124:127], a[192:195], v[152:155], v[124:127]\n"
"ds_read_b64_tr_b16 a[212:213], v38 offset:33280\n"
"v_exp_f32 v135, v135\n"
"v_mfma_f32_16x16x32_bf16 v[140:143], a[192:195], v[184:187], v[140:143]\n"
"ds_read_b64_tr_b16 a[214:215], v38 offset:37376\n"
"v_cvt_pk_bf16_f32 v196, v132, v133\n"
"v_cvt_pk_bf16_f32 v197, v134, v135\n"
"v_mfma_f32_16x16x32_bf16 v[156:159], a[192:195], v[216:219], v[156:159]\n"
"v_exp_f32 v148, v148\n"
"v_mfma_f32_16x16x32_bf16 v[172:175], a[192:195], v[248:251], v[172:175]\n"
"v_exp_f32 v149, v149\n"
"v_mfma_f32_16x16x32_bf16 a[160:163], v[60:63], v[212:215], a[160:163]\n"
"ds_read_b64_tr_b16 a[216:217], v39 offset:33280\n"
"v_exp_f32 v150, v150\n"
"v_mfma_f32_16x16x32_bf16 a[164:167], v[60:63], v[220:223], a[164:167]\n"
"ds_read_b64_tr_b16 a[218:219], v39 offset:37376\n"
"v_exp_f32 v151, v151\n"
"v_mfma_f32_16x16x32_bf16 a[168:171], v[60:63], v[228:231], a[168:171]\n"
"v_cvt_pk_bf16_f32 v204, v148, v149\n"
"v_cvt_pk_bf16_f32 v205, v150, v151\n"
"v_mfma_f32_16x16x32_bf16 a[172:175], v[60:63], v[236:239], a[172:175]\n"
"s_waitcnt lgkmcnt(6)\n"
"v_exp_f32 v164, v164\n"
"v_mfma_f32_16x16x32_bf16 a[0:3], a[204:207], v[212:215], a[0:3]\n"
"ds_read_b64_tr_b16 a[220:221], v38 offset:33792\n"
"v_exp_f32 v165, v165\n"
"v_mfma_f32_16x16x32_bf16 a[32:35], a[204:207], v[220:223], a[32:35]\n"
"ds_read_b64_tr_b16 a[222:223], v38 offset:37888\n"
"v_exp_f32 v166, v166\n"
"v_mfma_f32_16x16x32_bf16 a[64:67], a[204:207], v[228:231], a[64:67]\n"
"v_exp_f32 v167, v167\n"
"v_mfma_f32_16x16x32_bf16 a[96:99], a[204:207], v[236:239], a[96:99]\n"
"s_waitcnt lgkmcnt(6)\n"
"v_cvt_pk_bf16_f32 v244, v164, v165\n"
"v_cvt_pk_bf16_f32 v245, v166, v167\n"
"v_mfma_f32_16x16x32_bf16 a[4:7], a[208:211], v[212:215], a[4:7]\n"
"ds_read_b64_tr_b16 a[224:225], v39 offset:33792\n"
"v_exp_f32 v108, v108\n"
"v_mfma_f32_16x16x32_bf16 a[36:39], a[208:211], v[220:223], a[36:39]\n"
"ds_read_b64_tr_b16 a[226:227], v39 offset:37888\n"
"v_exp_f32 v109, v109\n"
"v_mfma_f32_16x16x32_bf16 a[68:71], a[208:211], v[228:231], a[68:71]\n"
"v_exp_f32 v110, v110\n"
"v_mfma_f32_16x16x32_bf16 a[100:103], a[208:211], v[236:239], a[100:103]\n"
"s_waitcnt lgkmcnt(6)\n"
"v_exp_f32 v111, v111\n"
"v_mfma_f32_16x16x32_bf16 a[8:11], a[212:215], v[212:215], a[8:11]\n"
"ds_read_b64_tr_b16 a[204:205], v38 offset:34304\n"
"v_cvt_pk_bf16_f32 v182, v108, v109\n"
"v_cvt_pk_bf16_f32 v183, v110, v111\n"
"v_mfma_f32_16x16x32_bf16 a[40:43], a[212:215], v[220:223], a[40:43]\n"
"ds_read_b64_tr_b16 a[206:207], v38 offset:38400\n"
"v_exp_f32 v124, v124\n"
"v_mfma_f32_16x16x32_bf16 a[72:75], a[212:215], v[228:231], a[72:75]\n"
"v_exp_f32 v125, v125\n"
"v_mfma_f32_16x16x32_bf16 a[104:107], a[212:215], v[236:239], a[104:107]\n"
"s_waitcnt lgkmcnt(6)\n"
"v_exp_f32 v126, v126\n"
"v_mfma_f32_16x16x32_bf16 a[12:15], a[216:219], v[212:215], a[12:15]\n"
"ds_read_b64_tr_b16 a[208:209], v39 offset:34304\n"
"v_exp_f32 v127, v127\n"
"v_mfma_f32_16x16x32_bf16 a[44:47], a[216:219], v[220:223], a[44:47]\n"
"ds_read_b64_tr_b16 a[210:211], v39 offset:38400\n"
"v_cvt_pk_bf16_f32 v190, v124, v125\n"
"v_cvt_pk_bf16_f32 v191, v126, v127\n"
"v_mfma_f32_16x16x32_bf16 a[76:79], a[216:219], v[228:231], a[76:79]\n"
"v_exp_f32 v140, v140\n"
"v_mfma_f32_16x16x32_bf16 a[108:111], a[216:219], v[236:239], a[108:111]\n"
"s_waitcnt lgkmcnt(6)\n"
"v_exp_f32 v141, v141\n"
"v_mfma_f32_16x16x32_bf16 a[16:19], a[220:223], v[212:215], a[16:19]\n"
"ds_read_b128 a[196:199], v32 offset:49152\n"
"v_exp_f32 v142, v142\n"
"v_mfma_f32_16x16x32_bf16 a[48:51], a[220:223], v[220:223], a[48:51]\n"
"v_exp_f32 v143, v143\n"
"v_mfma_f32_16x16x32_bf16 a[80:83], a[220:223], v[228:231], a[80:83]\n"
"v_cvt_pk_bf16_f32 v198, v140, v141\n"
"v_cvt_pk_bf16_f32 v199, v142, v143\n"
"v_mfma_f32_16x16x32_bf16 a[112:115], a[220:223], v[236:239], a[112:115]\n"
"s_waitcnt lgkmcnt(5)\n"
"v_exp_f32 v156, v156\n"
"v_mfma_f32_16x16x32_bf16 a[20:23], a[224:227], v[212:215], a[20:23]\n"
"ds_read_b128 a[200:203], v33 offset:49152\n"
"v_exp_f32 v157, v157\n"
"v_mfma_f32_16x16x32_bf16 a[52:55], a[224:227], v[220:223], a[52:55]\n"
"v_exp_f32 v158, v158\n"
"v_mfma_f32_16x16x32_bf16 a[84:87], a[224:227], v[228:231], a[84:87]\n"
"v_exp_f32 v159, v159\n"
"v_mfma_f32_16x16x32_bf16 a[116:119], a[224:227], v[236:239], a[116:119]\n"
"s_waitcnt lgkmcnt(4)\n"
"v_cvt_pk_bf16_f32 v206, v156, v157\n"
"v_cvt_pk_bf16_f32 v207, v158, v159\n"
"v_mfma_f32_16x16x32_bf16 a[24:27], a[204:207], v[212:215], a[24:27]\n"
"ds_read_b128 a[180:183], v34 offset:49152\n"
"v_exp_f32 v172, v172\n"
"v_mfma_f32_16x16x32_bf16 a[56:59], a[204:207], v[220:223], a[56:59]\n"
"v_exp_f32 v173, v173\n"
"v_mfma_f32_16x16x32_bf16 a[88:91], a[204:207], v[228:231], a[88:91]\n"
"v_exp_f32 v174, v174\n"
"v_mfma_f32_16x16x32_bf16 a[120:123], a[204:207], v[236:239], a[120:123]\n"
"s_waitcnt lgkmcnt(3)\n"
"v_exp_f32 v175, v175\n"
"v_mfma_f32_16x16x32_bf16 a[28:31], a[208:211], v[212:215], a[28:31]\n"
"ds_read_b128 a[184:187], v35 offset:49152\n"
"v_cvt_pk_bf16_f32 v246, v172, v173\n"
"v_cvt_pk_bf16_f32 v247, v174, v175\n"
"v_mfma_f32_16x16x32_bf16 a[60:63], a[208:211], v[220:223], a[60:63]\n"
"v_mfma_f32_16x16x32_bf16 a[92:95], a[208:211], v[228:231], a[92:95]\n"
"v_mfma_f32_16x16x32_bf16 a[124:127], a[208:211], v[236:239], a[124:127]\n"
"s_waitcnt lgkmcnt(3)\n"
"v_mfma_f32_16x16x32_bf16 v[100:103], a[196:199], v[96:99], v[40:43]\n"
"ds_read_b128 a[188:191], v32 offset:53248\n"
"v_mfma_f32_16x16x32_bf16 v[116:119], a[196:199], v[128:131], v[44:47]\n"
"s_add_u32 m0, s61, 32768\n"
"s_mov_b32 s65, 229376\n"
"v_mfma_f32_16x16x32_bf16 v[132:135], a[196:199], v[160:163], v[48:51]\n"
"buffer_load_dwordx4 v24, s[48:51], s65 offen nt lds\n"
"v_mfma_f32_16x16x32_bf16 v[148:151], a[196:199], v[192:195], v[52:55]\n"
"s_waitcnt lgkmcnt(3)\n"
"v_mfma_f32_16x16x32_bf16 v[100:103], a[200:203], v[104:107], v[100:103]\n"
"ds_read_b128 a[192:195], v33 offset:53248\n"
"v_mfma_f32_16x16x32_bf16 v[116:119], a[200:203], v[136:139], v[116:119]\n"
"v_mfma_f32_16x16x32_bf16 v[132:135], a[200:203], v[168:171], v[132:135]\n"
"buffer_load_dwordx4 v25, s[48:51], s65 offen offset:1024 nt lds\n"
"v_mfma_f32_16x16x32_bf16 v[148:151], a[200:203], v[200:203], v[148:151]\n"
"s_waitcnt lgkmcnt(3)\n"
"v_mfma_f32_16x16x32_bf16 v[100:103], a[180:183], v[112:115], v[100:103]\n"
"ds_read_b128 a[196:199], v34 offset:53248\n"
"v_mfma_f32_16x16x32_bf16 v[116:119], a[180:183], v[144:147], v[116:119]\n"
"v_mfma_f32_16x16x32_bf16 v[132:135], a[180:183], v[176:179], v[132:135]\n"
"buffer_load_dwordx4 v93, s[48:51], s65 offen offset:2048 nt lds\n"
"v_mfma_f32_16x16x32_bf16 v[148:151], a[180:183], v[208:211], v[148:151]\n"
"s_waitcnt lgkmcnt(3)\n"
"v_mfma_f32_16x16x32_bf16 v[100:103], a[184:187], v[120:123], v[100:103]\n"
"ds_read_b128 a[200:203], v35 offset:53248\n"
"v_mfma_f32_16x16x32_bf16 v[116:119], a[184:187], v[152:155], v[116:119]\n"
"v_mfma_f32_16x16x32_bf16 v[132:135], a[184:187], v[184:187], v[132:135]\n"
"buffer_load_dwordx4 v94, s[48:51], s65 offen offset:3072 nt lds\n"
"v_mfma_f32_16x16x32_bf16 v[148:151], a[184:187], v[216:219], v[148:151]\n"
"s_waitcnt lgkmcnt(3)\n"
"v_mfma_f32_16x16x32_bf16 v[108:111], a[188:191], v[96:99], v[40:43]\n"
"v_mfma_f32_16x16x32_bf16 v[124:127], a[188:191], v[128:131], v[44:47]\n"
"ds_read_b64_tr_b16 a[212:213], v36 offset:49152\n"
"v_mfma_f32_16x16x32_bf16 v[140:143], a[188:191], v[160:163], v[48:51]\n"
"ds_read_b64_tr_b16 a[214:215], v36 offset:53248\n"
"v_mfma_f32_16x16x32_bf16 v[156:159], a[188:191], v[192:195], v[52:55]\n"
"s_waitcnt lgkmcnt(4)\n"
"v_mfma_f32_16x16x32_bf16 v[108:111], a[192:195], v[104:107], v[108:111]\n"
"v_exp_f32 v100, v100\n"
"v_mfma_f32_16x16x32_bf16 v[124:127], a[192:195], v[136:139], v[124:127]\n"
"ds_read_b64_tr_b16 a[218:219], v37 offset:53248\n"
"v_exp_f32 v101, v101\n"
"v_mfma_f32_16x16x32_bf16 v[140:143], a[192:195], v[168:171], v[140:143]\n"
"ds_read_b64_tr_b16 a[216:217], v37 offset:49152\n"
"v_exp_f32 v102, v102\n"
"v_mfma_f32_16x16x32_bf16 v[156:159], a[192:195], v[200:203], v[156:159]\n"
"s_waitcnt lgkmcnt(5)\n"
"v_exp_f32 v103, v103\n"
"v_mfma_f32_16x16x32_bf16 v[108:111], a[196:199], v[112:115], v[108:111]\n"
"v_cvt_pk_bf16_f32 v212, v100, v101\n"
"v_cvt_pk_bf16_f32 v213, v102, v103\n"
"v_mfma_f32_16x16x32_bf16 v[124:127], a[196:199], v[144:147], v[124:127]\n"
"v_exp_f32 v116, v116\n"
"v_mfma_f32_16x16x32_bf16 v[140:143], a[196:199], v[176:179], v[140:143]\n"
"ds_read_b64_tr_b16 a[220:221], v36 offset:49664\n"
"v_exp_f32 v117, v117\n"
"v_mfma_f32_16x16x32_bf16 v[156:159], a[196:199], v[208:211], v[156:159]\n"
"s_waitcnt lgkmcnt(5)\n"
"v_exp_f32 v118, v118\n"
"v_mfma_f32_16x16x32_bf16 v[108:111], a[200:203], v[120:123], v[108:111]\n"
"ds_read_b64_tr_b16 a[222:223], v36 offset:53760\n"
"v_exp_f32 v119, v119\n"
"v_mfma_f32_16x16x32_bf16 v[124:127], a[200:203], v[152:155], v[124:127]\n"
"v_cvt_pk_bf16_f32 v220, v116, v117\n"
"v_cvt_pk_bf16_f32 v221, v118, v119\n"
"v_mfma_f32_16x16x32_bf16 v[140:143], a[200:203], v[184:187], v[140:143]\n"
"v_exp_f32 v132, v132\n"
"v_mfma_f32_16x16x32_bf16 v[156:159], a[200:203], v[216:219], v[156:159]\n"
"s_waitcnt vmcnt(12)\n"
"s_barrier\n"
"v_exp_f32 v133, v133\n"
"v_mfma_f32_16x16x32_bf16 a[160:163], v[60:63], v[180:183], a[160:163]\n"
"ds_read_b64_tr_b16 a[224:225], v37 offset:49664\n"
"v_exp_f32 v134, v134\n"
"v_mfma_f32_16x16x32_bf16 a[164:167], v[60:63], v[188:191], a[164:167]\n"
"ds_read_b64_tr_b16 a[226:227], v37 offset:53760\n"
"v_exp_f32 v135, v135\n"
"v_mfma_f32_16x16x32_bf16 a[168:171], v[60:63], v[196:199], a[168:171]\n"
"v_cvt_pk_bf16_f32 v228, v132, v133\n"
"v_cvt_pk_bf16_f32 v229, v134, v135\n"
"v_mfma_f32_16x16x32_bf16 a[172:175], v[60:63], v[204:207], a[172:175]\n"
"v_exp_f32 v148, v148\n"
"v_mfma_f32_16x16x32_bf16 a[176:179], v[60:63], v[244:247], a[176:179]\n"
"s_waitcnt lgkmcnt(6)\n"
"v_exp_f32 v149, v149\n"
"v_mfma_f32_16x16x32_bf16 a[0:3], a[212:215], v[180:183], a[0:3]\n"
"ds_read_b64_tr_b16 a[204:205], v36 offset:50176\n"
"v_exp_f32 v150, v150\n"
"v_mfma_f32_16x16x32_bf16 a[32:35], a[212:215], v[188:191], a[32:35]\n"
"ds_read_b64_tr_b16 a[206:207], v36 offset:54272\n"
"v_exp_f32 v151, v151\n"
"v_mfma_f32_16x16x32_bf16 a[64:67], a[212:215], v[196:199], a[64:67]\n"
"v_cvt_pk_bf16_f32 v236, v148, v149\n"
"v_cvt_pk_bf16_f32 v237, v150, v151\n"
"v_mfma_f32_16x16x32_bf16 a[96:99], a[212:215], v[204:207], a[96:99]\n"
"v_exp_f32 v108, v108\n"
"v_mfma_f32_16x16x32_bf16 a[128:131], a[212:215], v[244:247], a[128:131]\n"
"s_waitcnt lgkmcnt(6)\n"
"v_exp_f32 v109, v109\n"
"v_mfma_f32_16x16x32_bf16 a[4:7], a[216:219], v[180:183], a[4:7]\n"
"ds_read_b64_tr_b16 a[208:209], v37 offset:50176\n"
"v_exp_f32 v110, v110\n"
"v_mfma_f32_16x16x32_bf16 a[36:39], a[216:219], v[188:191], a[36:39]\n"
"ds_read_b64_tr_b16 a[210:211], v37 offset:54272\n"
"v_exp_f32 v111, v111\n"
"v_mfma_f32_16x16x32_bf16 a[68:71], a[216:219], v[196:199], a[68:71]\n"
"v_cvt_pk_bf16_f32 v214, v108, v109\n"
"v_cvt_pk_bf16_f32 v215, v110, v111\n"
"v_mfma_f32_16x16x32_bf16 a[100:103], a[216:219], v[204:207], a[100:103]\n"
"v_exp_f32 v124, v124\n"
"v_mfma_f32_16x16x32_bf16 a[132:135], a[216:219], v[244:247], a[132:135]\n"
"s_waitcnt lgkmcnt(6)\n"
"v_exp_f32 v125, v125\n"
"v_mfma_f32_16x16x32_bf16 a[8:11], a[220:223], v[180:183], a[8:11]\n"
"ds_read_b64_tr_b16 a[212:213], v36 offset:50688\n"
"v_exp_f32 v126, v126\n"
"v_mfma_f32_16x16x32_bf16 a[40:43], a[220:223], v[188:191], a[40:43]\n"
"ds_read_b64_tr_b16 a[214:215], v36 offset:54784\n"
"v_exp_f32 v127, v127\n"
"v_mfma_f32_16x16x32_bf16 a[72:75], a[220:223], v[196:199], a[72:75]\n"
"v_cvt_pk_bf16_f32 v222, v124, v125\n"
"v_cvt_pk_bf16_f32 v223, v126, v127\n"
"v_mfma_f32_16x16x32_bf16 a[104:107], a[220:223], v[204:207], a[104:107]\n"
"v_exp_f32 v140, v140\n"
"v_mfma_f32_16x16x32_bf16 a[136:139], a[220:223], v[244:247], a[136:139]\n"
"s_waitcnt lgkmcnt(6)\n"
"v_exp_f32 v141, v141\n"
"v_mfma_f32_16x16x32_bf16 a[12:15], a[224:227], v[180:183], a[12:15]\n"
"ds_read_b64_tr_b16 a[216:217], v37 offset:50688\n"
"v_exp_f32 v142, v142\n"
"v_mfma_f32_16x16x32_bf16 a[44:47], a[224:227], v[188:191], a[44:47]\n"
"ds_read_b64_tr_b16 a[218:219], v37 offset:54784\n"
"v_exp_f32 v143, v143\n"
"v_mfma_f32_16x16x32_bf16 a[76:79], a[224:227], v[196:199], a[76:79]\n"
"v_cvt_pk_bf16_f32 v230, v140, v141\n"
"v_cvt_pk_bf16_f32 v231, v142, v143\n"
"v_mfma_f32_16x16x32_bf16 a[108:111], a[224:227], v[204:207], a[108:111]\n"
"v_exp_f32 v156, v156\n"
"v_mfma_f32_16x16x32_bf16 a[140:143], a[224:227], v[244:247], a[140:143]\n"
"s_waitcnt lgkmcnt(6)\n"
"v_exp_f32 v157, v157\n"
"v_mfma_f32_16x16x32_bf16 a[16:19], a[204:207], v[180:183], a[16:19]\n"
"ds_read_b128 a[180:183], v28 offset:0\n"
"v_exp_f32 v158, v158\n"
"v_mfma_f32_16x16x32_bf16 a[48:51], a[204:207], v[188:191], a[48:51]\n"
"v_exp_f32 v159, v159\n"
"v_mfma_f32_16x16x32_bf16 a[80:83], a[204:207], v[196:199], a[80:83]\n"
"v_cvt_pk_bf16_f32 v238, v156, v157\n"
"v_cvt_pk_bf16_f32 v239, v158, v159\n"
"v_mfma_f32_16x16x32_bf16 a[112:115], a[204:207], v[204:207], a[112:115]\n"
"v_mfma_f32_16x16x32_bf16 a[144:147], a[204:207], v[244:247], a[144:147]\n"
"s_waitcnt lgkmcnt(5)\n"
"v_mfma_f32_16x16x32_bf16 a[20:23], a[208:211], v[180:183], a[20:23]\n"
"ds_read_b128 a[184:187], v29 offset:0\n"
"v_mfma_f32_16x16x32_bf16 a[52:55], a[208:211], v[188:191], a[52:55]\n"
"v_mfma_f32_16x16x32_bf16 a[84:87], a[208:211], v[196:199], a[84:87]\n"
"v_mfma_f32_16x16x32_bf16 a[116:119], a[208:211], v[204:207], a[116:119]\n"
"v_mfma_f32_16x16x32_bf16 a[148:151], a[208:211], v[244:247], a[148:151]\n"
"s_waitcnt lgkmcnt(4)\n"
"v_mfma_f32_16x16x32_bf16 a[24:27], a[212:215], v[180:183], a[24:27]\n"
"ds_read_b128 a[188:191], v30 offset:0\n"
"v_mfma_f32_16x16x32_bf16 a[56:59], a[212:215], v[188:191], a[56:59]\n"
"v_mfma_f32_16x16x32_bf16 a[88:91], a[212:215], v[196:199], a[88:91]\n"
"v_mfma_f32_16x16x32_bf16 a[120:123], a[212:215], v[204:207], a[120:123]\n"
"v_mfma_f32_16x16x32_bf16 a[152:155], a[212:215], v[244:247], a[152:155]\n"
"s_waitcnt lgkmcnt(3)\n"
"v_mfma_f32_16x16x32_bf16 a[28:31], a[216:219], v[180:183], a[28:31]\n"
"ds_read_b128 a[192:195], v31 offset:0\n"
"v_mfma_f32_16x16x32_bf16 a[60:63], a[216:219], v[188:191], a[60:63]\n"
"v_mfma_f32_16x16x32_bf16 a[92:95], a[216:219], v[196:199], a[92:95]\n"
"v_mfma_f32_16x16x32_bf16 a[124:127], a[216:219], v[204:207], a[124:127]\n"
"v_mfma_f32_16x16x32_bf16 a[156:159], a[216:219], v[244:247], a[156:159]\n"
"s_waitcnt lgkmcnt(3)\n"
"v_mfma_f32_16x16x32_bf16 v[100:103], a[180:183], v[96:99], v[40:43]\n"
"ds_read_b128 a[196:199], v28 offset:4096\n"
"v_mfma_f32_16x16x32_bf16 v[116:119], a[180:183], v[128:131], v[44:47]\n"
"s_add_u32 m0, s61, 98304\n"
"s_mov_b32 s66, 229376\n"
"v_mfma_f32_16x16x32_bf16 v[132:135], a[180:183], v[160:163], v[48:51]\n"
"buffer_load_dwordx4 v95, s[52:55], s66 offen nt lds\n"
"v_mfma_f32_16x16x32_bf16 v[148:151], a[180:183], v[192:195], v[52:55]\n"
"v_mfma_f32_16x16x32_bf16 v[164:167], a[180:183], v[224:227], v[56:59]\n"
"s_waitcnt lgkmcnt(3)\n"
"v_mfma_f32_16x16x32_bf16 v[100:103], a[184:187], v[104:107], v[100:103]\n"
"ds_read_b128 a[200:203], v29 offset:4096\n"
"v_mfma_f32_16x16x32_bf16 v[116:119], a[184:187], v[136:139], v[116:119]\n"
"buffer_load_dwordx4 v27, s[52:55], s66 offen offset:1024 nt lds\n"
"v_mfma_f32_16x16x32_bf16 v[132:135], a[184:187], v[168:171], v[132:135]\n"
"v_mfma_f32_16x16x32_bf16 v[148:151], a[184:187], v[200:203], v[148:151]\n"
"v_mfma_f32_16x16x32_bf16 v[164:167], a[184:187], v[232:235], v[164:167]\n"
"s_waitcnt lgkmcnt(3)\n"
"v_mfma_f32_16x16x32_bf16 v[100:103], a[188:191], v[112:115], v[100:103]\n"
"ds_read_b128 a[180:183], v30 offset:4096\n"
"v_mfma_f32_16x16x32_bf16 v[116:119], a[188:191], v[144:147], v[116:119]\n"
"buffer_load_dwordx4 v95, s[52:55], s66 offen offset:2048 nt lds\n"
"v_mfma_f32_16x16x32_bf16 v[132:135], a[188:191], v[176:179], v[132:135]\n"
"v_mfma_f32_16x16x32_bf16 v[148:151], a[188:191], v[208:211], v[148:151]\n"
"v_mfma_f32_16x16x32_bf16 v[164:167], a[188:191], v[240:243], v[164:167]\n"
"s_waitcnt lgkmcnt(3)\n"
"v_mfma_f32_16x16x32_bf16 v[100:103], a[192:195], v[120:123], v[100:103]\n"
"ds_read_b128 a[184:187], v31 offset:4096\n"
"v_mfma_f32_16x16x32_bf16 v[116:119], a[192:195], v[152:155], v[116:119]\n"
"buffer_load_dwordx4 v27, s[52:55], s66 offen offset:3072 nt lds\n"
"v_mfma_f32_16x16x32_bf16 v[132:135], a[192:195], v[184:187], v[132:135]\n"
"v_mfma_f32_16x16x32_bf16 v[148:151], a[192:195], v[216:219], v[148:151]\n"
"v_mfma_f32_16x16x32_bf16 v[164:167], a[192:195], v[248:251], v[164:167]\n"
"s_waitcnt lgkmcnt(3)\n"
"v_mfma_f32_16x16x32_bf16 v[108:111], a[196:199], v[96:99], v[40:43]\n"
"v_mfma_f32_16x16x32_bf16 v[124:127], a[196:199], v[128:131], v[44:47]\n"
"v_mfma_f32_16x16x32_bf16 v[140:143], a[196:199], v[160:163], v[48:51]\n"
"v_mfma_f32_16x16x32_bf16 v[156:159], a[196:199], v[192:195], v[52:55]\n"
"v_exp_f32 v100, v100\n"
"v_mfma_f32_16x16x32_bf16 v[172:175], a[196:199], v[224:227], v[56:59]\n"
"s_waitcnt lgkmcnt(2)\n"
"v_exp_f32 v101, v101\n"
"v_mfma_f32_16x16x32_bf16 v[108:111], a[200:203], v[104:107], v[108:111]\n"
"v_exp_f32 v102, v102\n"
"v_mfma_f32_16x16x32_bf16 v[124:127], a[200:203], v[136:139], v[124:127]\n"
"v_exp_f32 v103, v103\n"
"v_mfma_f32_16x16x32_bf16 v[140:143], a[200:203], v[168:171], v[140:143]\n"
"ds_read_b64_tr_b16 a[222:223], v38 offset:53248\n"
"v_cvt_pk_bf16_f32 v180, v100, v101\n"
"v_cvt_pk_bf16_f32 v181, v102, v103\n"
"v_mfma_f32_16x16x32_bf16 v[156:159], a[200:203], v[200:203], v[156:159]\n"
"ds_read_b64_tr_b16 a[220:221], v38 offset:49152\n"
"v_exp_f32 v116, v116\n"
"v_mfma_f32_16x16x32_bf16 v[172:175], a[200:203], v[232:235], v[172:175]\n"
"s_waitcnt lgkmcnt(3)\n"
"v_exp_f32 v117, v117\n"
"v_mfma_f32_16x16x32_bf16 v[108:111], a[180:183], v[112:115], v[108:111]\n"
"v_exp_f32 v118, v118\n"
"v_mfma_f32_16x16x32_bf16 v[124:127], a[180:183], v[144:147], v[124:127]\n"
"v_exp_f32 v119, v119\n"
"v_mfma_f32_16x16x32_bf16 v[140:143], a[180:183], v[176:179], v[140:143]\n"
"ds_read_b64_tr_b16 a[224:225], v39 offset:49152\n"
"v_cvt_pk_bf16_f32 v188, v116, v117\n"
"v_cvt_pk_bf16_f32 v189, v118, v119\n"
"v_mfma_f32_16x16x32_bf16 v[156:159], a[180:183], v[208:211], v[156:159]\n"
"ds_read_b64_tr_b16 a[226:227], v39 offset:53248\n"
"v_exp_f32 v132, v132\n"
"v_mfma_f32_16x16x32_bf16 v[172:175], a[180:183], v[240:243], v[172:175]\n"
"s_waitcnt lgkmcnt(4)\n"
"v_exp_f32 v133, v133\n"
"v_mfma_f32_16x16x32_bf16 v[108:111], a[184:187], v[120:123], v[108:111]\n"
"v_exp_f32 v134, v134\n"
"v_mfma_f32_16x16x32_bf16 v[124:127], a[184:187], v[152:155], v[124:127]\n"
"ds_read_b64_tr_b16 a[204:205], v38 offset:49664\n"
"v_exp_f32 v135, v135\n"
"v_mfma_f32_16x16x32_bf16 v[140:143], a[184:187], v[184:187], v[140:143]\n"
"ds_read_b64_tr_b16 a[206:207], v38 offset:53760\n"
"v_cvt_pk_bf16_f32 v196, v132, v133\n"
"v_cvt_pk_bf16_f32 v197, v134, v135\n"
"v_mfma_f32_16x16x32_bf16 v[156:159], a[184:187], v[216:219], v[156:159]\n"
"v_exp_f32 v148, v148\n"
"v_mfma_f32_16x16x32_bf16 v[172:175], a[184:187], v[248:251], v[172:175]\n"
"v_exp_f32 v149, v149\n"
"v_mfma_f32_16x16x32_bf16 a[160:163], v[60:63], v[212:215], a[160:163]\n"
"ds_read_b64_tr_b16 a[208:209], v39 offset:49664\n"
"v_exp_f32 v150, v150\n"
"v_mfma_f32_16x16x32_bf16 a[164:167], v[60:63], v[220:223], a[164:167]\n"
"ds_read_b64_tr_b16 a[210:211], v39 offset:53760\n"
"v_exp_f32 v151, v151\n"
"v_mfma_f32_16x16x32_bf16 a[168:171], v[60:63], v[228:231], a[168:171]\n"
"v_cvt_pk_bf16_f32 v204, v148, v149\n"
"v_cvt_pk_bf16_f32 v205, v150, v151\n"
"v_mfma_f32_16x16x32_bf16 a[172:175], v[60:63], v[236:239], a[172:175]\n"
"s_waitcnt lgkmcnt(6)\n"
"v_exp_f32 v164, v164\n"
"v_mfma_f32_16x16x32_bf16 a[0:3], a[220:223], v[212:215], a[0:3]\n"
"ds_read_b64_tr_b16 a[212:213], v38 offset:50176\n"
"v_exp_f32 v165, v165\n"
"v_mfma_f32_16x16x32_bf16 a[32:35], a[220:223], v[220:223], a[32:35]\n"
"ds_read_b64_tr_b16 a[214:215], v38 offset:54272\n"
"v_exp_f32 v166, v166\n"
"v_mfma_f32_16x16x32_bf16 a[64:67], a[220:223], v[228:231], a[64:67]\n"
"v_exp_f32 v167, v167\n"
"v_mfma_f32_16x16x32_bf16 a[96:99], a[220:223], v[236:239], a[96:99]\n"
"s_waitcnt lgkmcnt(6)\n"
"v_cvt_pk_bf16_f32 v244, v164, v165\n"
"v_cvt_pk_bf16_f32 v245, v166, v167\n"
"v_mfma_f32_16x16x32_bf16 a[4:7], a[224:227], v[212:215], a[4:7]\n"
"ds_read_b64_tr_b16 a[216:217], v39 offset:50176\n"
"v_exp_f32 v108, v108\n"
"v_mfma_f32_16x16x32_bf16 a[36:39], a[224:227], v[220:223], a[36:39]\n"
"ds_read_b64_tr_b16 a[218:219], v39 offset:54272\n"
"v_exp_f32 v109, v109\n"
"v_mfma_f32_16x16x32_bf16 a[68:71], a[224:227], v[228:231], a[68:71]\n"
"v_exp_f32 v110, v110\n"
"v_mfma_f32_16x16x32_bf16 a[100:103], a[224:227], v[236:239], a[100:103]\n"
"s_waitcnt lgkmcnt(6)\n"
"v_exp_f32 v111, v111\n"
"v_mfma_f32_16x16x32_bf16 a[8:11], a[204:207], v[212:215], a[8:11]\n"
"ds_read_b64_tr_b16 a[220:221], v38 offset:50688\n"
"v_cvt_pk_bf16_f32 v182, v108, v109\n"
"v_cvt_pk_bf16_f32 v183, v110, v111\n"
"v_mfma_f32_16x16x32_bf16 a[40:43], a[204:207], v[220:223], a[40:43]\n"
"ds_read_b64_tr_b16 a[222:223], v38 offset:54784\n"
"v_exp_f32 v124, v124\n"
"v_mfma_f32_16x16x32_bf16 a[72:75], a[204:207], v[228:231], a[72:75]\n"
"v_exp_f32 v125, v125\n"
"v_mfma_f32_16x16x32_bf16 a[104:107], a[204:207], v[236:239], a[104:107]\n"
"s_waitcnt lgkmcnt(6)\n"
"v_exp_f32 v126, v126\n"
"v_mfma_f32_16x16x32_bf16 a[12:15], a[208:211], v[212:215], a[12:15]\n"
"ds_read_b64_tr_b16 a[224:225], v39 offset:50688\n"
"v_exp_f32 v127, v127\n"
"v_mfma_f32_16x16x32_bf16 a[44:47], a[208:211], v[220:223], a[44:47]\n"
"ds_read_b64_tr_b16 a[226:227], v39 offset:54784\n"
"v_cvt_pk_bf16_f32 v190, v124, v125\n"
"v_cvt_pk_bf16_f32 v191, v126, v127\n"
"v_mfma_f32_16x16x32_bf16 a[76:79], a[208:211], v[228:231], a[76:79]\n"
"v_exp_f32 v140, v140\n"
"v_mfma_f32_16x16x32_bf16 a[108:111], a[208:211], v[236:239], a[108:111]\n"
"s_waitcnt lgkmcnt(6)\n"
"v_exp_f32 v141, v141\n"
"v_mfma_f32_16x16x32_bf16 a[16:19], a[212:215], v[212:215], a[16:19]\n"
"ds_read_b128 a[188:191], v32 offset:0\n"
"v_exp_f32 v142, v142\n"
"v_mfma_f32_16x16x32_bf16 a[48:51], a[212:215], v[220:223], a[48:51]\n"
"v_exp_f32 v143, v143\n"
"v_mfma_f32_16x16x32_bf16 a[80:83], a[212:215], v[228:231], a[80:83]\n"
"v_cvt_pk_bf16_f32 v198, v140, v141\n"
"v_cvt_pk_bf16_f32 v199, v142, v143\n"
"v_mfma_f32_16x16x32_bf16 a[112:115], a[212:215], v[236:239], a[112:115]\n"
"s_waitcnt lgkmcnt(5)\n"
"v_exp_f32 v156, v156\n"
"v_mfma_f32_16x16x32_bf16 a[20:23], a[216:219], v[212:215], a[20:23]\n"
"ds_read_b128 a[192:195], v33 offset:0\n"
"v_exp_f32 v157, v157\n"
"v_mfma_f32_16x16x32_bf16 a[52:55], a[216:219], v[220:223], a[52:55]\n"
"v_exp_f32 v158, v158\n"
"v_mfma_f32_16x16x32_bf16 a[84:87], a[216:219], v[228:231], a[84:87]\n"
"v_exp_f32 v159, v159\n"
"v_mfma_f32_16x16x32_bf16 a[116:119], a[216:219], v[236:239], a[116:119]\n"
"s_waitcnt lgkmcnt(4)\n"
"v_cvt_pk_bf16_f32 v206, v156, v157\n"
"v_cvt_pk_bf16_f32 v207, v158, v159\n"
"v_mfma_f32_16x16x32_bf16 a[24:27], a[220:223], v[212:215], a[24:27]\n"
"ds_read_b128 a[196:199], v34 offset:0\n"
"v_exp_f32 v172, v172\n"
"v_mfma_f32_16x16x32_bf16 a[56:59], a[220:223], v[220:223], a[56:59]\n"
"v_exp_f32 v173, v173\n"
"v_mfma_f32_16x16x32_bf16 a[88:91], a[220:223], v[228:231], a[88:91]\n"
"v_exp_f32 v174, v174\n"
"v_mfma_f32_16x16x32_bf16 a[120:123], a[220:223], v[236:239], a[120:123]\n"
"s_waitcnt lgkmcnt(3)\n"
"v_exp_f32 v175, v175\n"
"v_mfma_f32_16x16x32_bf16 a[28:31], a[224:227], v[212:215], a[28:31]\n"
"ds_read_b128 a[200:203], v35 offset:0\n"
"v_cvt_pk_bf16_f32 v246, v172, v173\n"
"v_cvt_pk_bf16_f32 v247, v174, v175\n"
"v_mfma_f32_16x16x32_bf16 a[60:63], a[224:227], v[220:223], a[60:63]\n"
"v_mfma_f32_16x16x32_bf16 a[92:95], a[224:227], v[228:231], a[92:95]\n"
"v_mfma_f32_16x16x32_bf16 a[124:127], a[224:227], v[236:239], a[124:127]\n"
"s_waitcnt lgkmcnt(3)\n"
"v_mfma_f32_16x16x32_bf16 v[100:103], a[188:191], v[96:99], v[40:43]\n"
"ds_read_b128 a[180:183], v32 offset:4096\n"
"v_mfma_f32_16x16x32_bf16 v[116:119], a[188:191], v[128:131], v[44:47]\n"
"s_add_u32 m0, s61, 49152\n"
"s_mov_b32 s65, 245760\n"
"v_mfma_f32_16x16x32_bf16 v[132:135], a[188:191], v[160:163], v[48:51]\n"
"buffer_load_dwordx4 v24, s[48:51], s65 offen nt lds\n"
"v_mfma_f32_16x16x32_bf16 v[148:151], a[188:191], v[192:195], v[52:55]\n"
"s_waitcnt lgkmcnt(3)\n"
"v_mfma_f32_16x16x32_bf16 v[100:103], a[192:195], v[104:107], v[100:103]\n"
"ds_read_b128 a[184:187], v33 offset:4096\n"
"v_mfma_f32_16x16x32_bf16 v[116:119], a[192:195], v[136:139], v[116:119]\n"
"v_mfma_f32_16x16x32_bf16 v[132:135], a[192:195], v[168:171], v[132:135]\n"
"buffer_load_dwordx4 v25, s[48:51], s65 offen offset:1024 nt lds\n"
"v_mfma_f32_16x16x32_bf16 v[148:151], a[192:195], v[200:203], v[148:151]\n"
"s_waitcnt lgkmcnt(3)\n"
"v_mfma_f32_16x16x32_bf16 v[100:103], a[196:199], v[112:115], v[100:103]\n"
"ds_read_b128 a[188:191], v34 offset:4096\n"
"v_mfma_f32_16x16x32_bf16 v[116:119], a[196:199], v[144:147], v[116:119]\n"
"v_mfma_f32_16x16x32_bf16 v[132:135], a[196:199], v[176:179], v[132:135]\n"
"buffer_load_dwordx4 v93, s[48:51], s65 offen offset:2048 nt lds\n"
"v_mfma_f32_16x16x32_bf16 v[148:151], a[196:199], v[208:211], v[148:151]\n"
"s_waitcnt lgkmcnt(3)\n"
"v_mfma_f32_16x16x32_bf16 v[100:103], a[200:203], v[120:123], v[100:103]\n"
"ds_read_b128 a[192:195], v35 offset:4096\n"
"v_mfma_f32_16x16x32_bf16 v[116:119], a[200:203], v[152:155], v[116:119]\n"
"v_mfma_f32_16x16x32_bf16 v[132:135], a[200:203], v[184:187], v[132:135]\n"
"buffer_load_dwordx4 v94, s[48:51], s65 offen offset:3072 nt lds\n"
"v_mfma_f32_16x16x32_bf16 v[148:151], a[200:203], v[216:219], v[148:151]\n"
"s_waitcnt lgkmcnt(3)\n"
"v_mfma_f32_16x16x32_bf16 v[108:111], a[180:183], v[96:99], v[40:43]\n"
"v_mfma_f32_16x16x32_bf16 v[124:127], a[180:183], v[128:131], v[44:47]\n"
"ds_read_b64_tr_b16 a[204:205], v36 offset:0\n"
"v_mfma_f32_16x16x32_bf16 v[140:143], a[180:183], v[160:163], v[48:51]\n"
"ds_read_b64_tr_b16 a[206:207], v36 offset:4096\n"
"v_mfma_f32_16x16x32_bf16 v[156:159], a[180:183], v[192:195], v[52:55]\n"
"s_waitcnt lgkmcnt(4)\n"
"v_mfma_f32_16x16x32_bf16 v[108:111], a[184:187], v[104:107], v[108:111]\n"
"v_exp_f32 v100, v100\n"
"v_mfma_f32_16x16x32_bf16 v[124:127], a[184:187], v[136:139], v[124:127]\n"
"ds_read_b64_tr_b16 a[210:211], v37 offset:4096\n"
"v_exp_f32 v101, v101\n"
"v_mfma_f32_16x16x32_bf16 v[140:143], a[184:187], v[168:171], v[140:143]\n"
"ds_read_b64_tr_b16 a[208:209], v37 offset:0\n"
"v_exp_f32 v102, v102\n"
"v_mfma_f32_16x16x32_bf16 v[156:159], a[184:187], v[200:203], v[156:159]\n"
"s_waitcnt lgkmcnt(5)\n"
"v_exp_f32 v103, v103\n"
"v_mfma_f32_16x16x32_bf16 v[108:111], a[188:191], v[112:115], v[108:111]\n"
"v_cvt_pk_bf16_f32 v212, v100, v101\n"
"v_cvt_pk_bf16_f32 v213, v102, v103\n"
"v_mfma_f32_16x16x32_bf16 v[124:127], a[188:191], v[144:147], v[124:127]\n"
"v_exp_f32 v116, v116\n"
"v_mfma_f32_16x16x32_bf16 v[140:143], a[188:191], v[176:179], v[140:143]\n"
"ds_read_b64_tr_b16 a[212:213], v36 offset:512\n"
"v_exp_f32 v117, v117\n"
"v_mfma_f32_16x16x32_bf16 v[156:159], a[188:191], v[208:211], v[156:159]\n"
"s_waitcnt lgkmcnt(5)\n"
"v_exp_f32 v118, v118\n"
"v_mfma_f32_16x16x32_bf16 v[108:111], a[192:195], v[120:123], v[108:111]\n"
"ds_read_b64_tr_b16 a[214:215], v36 offset:4608\n"
"v_exp_f32 v119, v119\n"
"v_mfma_f32_16x16x32_bf16 v[124:127], a[192:195], v[152:155], v[124:127]\n"
"v_cvt_pk_bf16_f32 v220, v116, v117\n"
"v_cvt_pk_bf16_f32 v221, v118, v119\n"
"v_mfma_f32_16x16x32_bf16 v[140:143], a[192:195], v[184:187], v[140:143]\n"
"v_exp_f32 v132, v132\n"
"v_mfma_f32_16x16x32_bf16 v[156:159], a[192:195], v[216:219], v[156:159]\n"
"s_waitcnt vmcnt(12)\n"
"s_barrier\n"
"v_exp_f32 v133, v133\n"
"v_mfma_f32_16x16x32_bf16 a[160:163], v[60:63], v[180:183], a[160:163]\n"
"ds_read_b64_tr_b16 a[216:217], v37 offset:512\n"
"v_exp_f32 v134, v134\n"
"v_mfma_f32_16x16x32_bf16 a[164:167], v[60:63], v[188:191], a[164:167]\n"
"ds_read_b64_tr_b16 a[218:219], v37 offset:4608\n"
"v_exp_f32 v135, v135\n"
"v_mfma_f32_16x16x32_bf16 a[168:171], v[60:63], v[196:199], a[168:171]\n"
"v_cvt_pk_bf16_f32 v228, v132, v133\n"
"v_cvt_pk_bf16_f32 v229, v134, v135\n"
"v_mfma_f32_16x16x32_bf16 a[172:175], v[60:63], v[204:207], a[172:175]\n"
"v_exp_f32 v148, v148\n"
"v_mfma_f32_16x16x32_bf16 a[176:179], v[60:63], v[244:247], a[176:179]\n"
"s_waitcnt lgkmcnt(6)\n"
"v_exp_f32 v149, v149\n"
"v_mfma_f32_16x16x32_bf16 a[0:3], a[204:207], v[180:183], a[0:3]\n"
"ds_read_b64_tr_b16 a[220:221], v36 offset:1024\n"
"v_exp_f32 v150, v150\n"
"v_mfma_f32_16x16x32_bf16 a[32:35], a[204:207], v[188:191], a[32:35]\n"
"ds_read_b64_tr_b16 a[222:223], v36 offset:5120\n"
"v_exp_f32 v151, v151\n"
"v_mfma_f32_16x16x32_bf16 a[64:67], a[204:207], v[196:199], a[64:67]\n"
"v_cvt_pk_bf16_f32 v236, v148, v149\n"
"v_cvt_pk_bf16_f32 v237, v150, v151\n"
"v_mfma_f32_16x16x32_bf16 a[96:99], a[204:207], v[204:207], a[96:99]\n"
"v_exp_f32 v108, v108\n"
"v_mfma_f32_16x16x32_bf16 a[128:131], a[204:207], v[244:247], a[128:131]\n"
"s_waitcnt lgkmcnt(6)\n"
"v_exp_f32 v109, v109\n"
"v_mfma_f32_16x16x32_bf16 a[4:7], a[208:211], v[180:183], a[4:7]\n"
"ds_read_b64_tr_b16 a[224:225], v37 offset:1024\n"
"v_exp_f32 v110, v110\n"
"v_mfma_f32_16x16x32_bf16 a[36:39], a[208:211], v[188:191], a[36:39]\n"
"ds_read_b64_tr_b16 a[226:227], v37 offset:5120\n"
"v_exp_f32 v111, v111\n"
"v_mfma_f32_16x16x32_bf16 a[68:71], a[208:211], v[196:199], a[68:71]\n"
"v_cvt_pk_bf16_f32 v214, v108, v109\n"
"v_cvt_pk_bf16_f32 v215, v110, v111\n"
"v_mfma_f32_16x16x32_bf16 a[100:103], a[208:211], v[204:207], a[100:103]\n"
"v_exp_f32 v124, v124\n"
"v_mfma_f32_16x16x32_bf16 a[132:135], a[208:211], v[244:247], a[132:135]\n"
"s_waitcnt lgkmcnt(6)\n"
"v_exp_f32 v125, v125\n"
"v_mfma_f32_16x16x32_bf16 a[8:11], a[212:215], v[180:183], a[8:11]\n"
"ds_read_b64_tr_b16 a[204:205], v36 offset:1536\n"
"v_exp_f32 v126, v126\n"
"v_mfma_f32_16x16x32_bf16 a[40:43], a[212:215], v[188:191], a[40:43]\n"
"ds_read_b64_tr_b16 a[206:207], v36 offset:5632\n"
"v_exp_f32 v127, v127\n"
"v_mfma_f32_16x16x32_bf16 a[72:75], a[212:215], v[196:199], a[72:75]\n"
"v_cvt_pk_bf16_f32 v222, v124, v125\n"
"v_cvt_pk_bf16_f32 v223, v126, v127\n"
"v_mfma_f32_16x16x32_bf16 a[104:107], a[212:215], v[204:207], a[104:107]\n"
"v_exp_f32 v140, v140\n"
"v_mfma_f32_16x16x32_bf16 a[136:139], a[212:215], v[244:247], a[136:139]\n"
"s_waitcnt lgkmcnt(6)\n"
"v_exp_f32 v141, v141\n"
"v_mfma_f32_16x16x32_bf16 a[12:15], a[216:219], v[180:183], a[12:15]\n"
"ds_read_b64_tr_b16 a[208:209], v37 offset:1536\n"
"v_exp_f32 v142, v142\n"
"v_mfma_f32_16x16x32_bf16 a[44:47], a[216:219], v[188:191], a[44:47]\n"
"ds_read_b64_tr_b16 a[210:211], v37 offset:5632\n"
"v_exp_f32 v143, v143\n"
"v_mfma_f32_16x16x32_bf16 a[76:79], a[216:219], v[196:199], a[76:79]\n"
"v_cvt_pk_bf16_f32 v230, v140, v141\n"
"v_cvt_pk_bf16_f32 v231, v142, v143\n"
"v_mfma_f32_16x16x32_bf16 a[108:111], a[216:219], v[204:207], a[108:111]\n"
"v_exp_f32 v156, v156\n"
"v_mfma_f32_16x16x32_bf16 a[140:143], a[216:219], v[244:247], a[140:143]\n"
"s_waitcnt lgkmcnt(6)\n"
"v_exp_f32 v157, v157\n"
"v_mfma_f32_16x16x32_bf16 a[16:19], a[220:223], v[180:183], a[16:19]\n"
"ds_read_b128 a[196:199], v28 offset:16384\n"
"v_exp_f32 v158, v158\n"
"v_mfma_f32_16x16x32_bf16 a[48:51], a[220:223], v[188:191], a[48:51]\n"
"v_exp_f32 v159, v159\n"
"v_mfma_f32_16x16x32_bf16 a[80:83], a[220:223], v[196:199], a[80:83]\n"
"v_cvt_pk_bf16_f32 v238, v156, v157\n"
"v_cvt_pk_bf16_f32 v239, v158, v159\n"
"v_mfma_f32_16x16x32_bf16 a[112:115], a[220:223], v[204:207], a[112:115]\n"
"v_mfma_f32_16x16x32_bf16 a[144:147], a[220:223], v[244:247], a[144:147]\n"
"s_waitcnt lgkmcnt(5)\n"
"v_mfma_f32_16x16x32_bf16 a[20:23], a[224:227], v[180:183], a[20:23]\n"
"ds_read_b128 a[200:203], v29 offset:16384\n"
"v_mfma_f32_16x16x32_bf16 a[52:55], a[224:227], v[188:191], a[52:55]\n"
"v_mfma_f32_16x16x32_bf16 a[84:87], a[224:227], v[196:199], a[84:87]\n"
"v_mfma_f32_16x16x32_bf16 a[116:119], a[224:227], v[204:207], a[116:119]\n"
"v_mfma_f32_16x16x32_bf16 a[148:151], a[224:227], v[244:247], a[148:151]\n"
"s_waitcnt lgkmcnt(4)\n"
"v_mfma_f32_16x16x32_bf16 a[24:27], a[204:207], v[180:183], a[24:27]\n"
"ds_read_b128 a[180:183], v30 offset:16384\n"
"v_mfma_f32_16x16x32_bf16 a[56:59], a[204:207], v[188:191], a[56:59]\n"
"v_mfma_f32_16x16x32_bf16 a[88:91], a[204:207], v[196:199], a[88:91]\n"
"v_mfma_f32_16x16x32_bf16 a[120:123], a[204:207], v[204:207], a[120:123]\n"
"v_mfma_f32_16x16x32_bf16 a[152:155], a[204:207], v[244:247], a[152:155]\n"
"s_waitcnt lgkmcnt(3)\n"
"v_mfma_f32_16x16x32_bf16 a[28:31], a[208:211], v[180:183], a[28:31]\n"
"ds_read_b128 a[184:187], v31 offset:16384\n"
"v_mfma_f32_16x16x32_bf16 a[60:63], a[208:211], v[188:191], a[60:63]\n"
"v_mfma_f32_16x16x32_bf16 a[92:95], a[208:211], v[196:199], a[92:95]\n"
"v_mfma_f32_16x16x32_bf16 a[124:127], a[208:211], v[204:207], a[124:127]\n"
"v_mfma_f32_16x16x32_bf16 a[156:159], a[208:211], v[244:247], a[156:159]\n"
"s_waitcnt lgkmcnt(3)\n"
"v_mfma_f32_16x16x32_bf16 v[100:103], a[196:199], v[96:99], v[40:43]\n"
"ds_read_b128 a[188:191], v28 offset:20480\n"
"v_mfma_f32_16x16x32_bf16 v[116:119], a[196:199], v[128:131], v[44:47]\n"
"s_add_u32 m0, s61, 114688\n"
"s_mov_b32 s66, 245760\n"
"v_mfma_f32_16x16x32_bf16 v[132:135], a[196:199], v[160:163], v[48:51]\n"
"buffer_load_dwordx4 v95, s[52:55], s66 offen nt lds\n"
"v_mfma_f32_16x16x32_bf16 v[148:151], a[196:199], v[192:195], v[52:55]\n"
"v_mfma_f32_16x16x32_bf16 v[164:167], a[196:199], v[224:227], v[56:59]\n"
"s_waitcnt lgkmcnt(3)\n"
"v_mfma_f32_16x16x32_bf16 v[100:103], a[200:203], v[104:107], v[100:103]\n"
"ds_read_b128 a[192:195], v29 offset:20480\n"
"v_mfma_f32_16x16x32_bf16 v[116:119], a[200:203], v[136:139], v[116:119]\n"
"buffer_load_dwordx4 v27, s[52:55], s66 offen offset:1024 nt lds\n"
"v_mfma_f32_16x16x32_bf16 v[132:135], a[200:203], v[168:171], v[132:135]\n"
"v_mfma_f32_16x16x32_bf16 v[148:151], a[200:203], v[200:203], v[148:151]\n"
"v_mfma_f32_16x16x32_bf16 v[164:167], a[200:203], v[232:235], v[164:167]\n"
"s_waitcnt lgkmcnt(3)\n"
"v_mfma_f32_16x16x32_bf16 v[100:103], a[180:183], v[112:115], v[100:103]\n"
"ds_read_b128 a[196:199], v30 offset:20480\n"
"v_mfma_f32_16x16x32_bf16 v[116:119], a[180:183], v[144:147], v[116:119]\n"
"buffer_load_dwordx4 v95, s[52:55], s66 offen offset:2048 nt lds\n"
"v_mfma_f32_16x16x32_bf16 v[132:135], a[180:183], v[176:179], v[132:135]\n"
"v_mfma_f32_16x16x32_bf16 v[148:151], a[180:183], v[208:211], v[148:151]\n"
"v_mfma_f32_16x16x32_bf16 v[164:167], a[180:183], v[240:243], v[164:167]\n"
"s_waitcnt lgkmcnt(3)\n"
"v_mfma_f32_16x16x32_bf16 v[100:103], a[184:187], v[120:123], v[100:103]\n"
"ds_read_b128 a[200:203], v31 offset:20480\n"
"v_mfma_f32_16x16x32_bf16 v[116:119], a[184:187], v[152:155], v[116:119]\n"
"buffer_load_dwordx4 v27, s[52:55], s66 offen offset:3072 nt lds\n"
"v_mfma_f32_16x16x32_bf16 v[132:135], a[184:187], v[184:187], v[132:135]\n"
"v_mfma_f32_16x16x32_bf16 v[148:151], a[184:187], v[216:219], v[148:151]\n"
"v_mfma_f32_16x16x32_bf16 v[164:167], a[184:187], v[248:251], v[164:167]\n"
"s_waitcnt lgkmcnt(3)\n"
"v_mfma_f32_16x16x32_bf16 v[108:111], a[188:191], v[96:99], v[40:43]\n"
"v_mfma_f32_16x16x32_bf16 v[124:127], a[188:191], v[128:131], v[44:47]\n"
"v_mfma_f32_16x16x32_bf16 v[140:143], a[188:191], v[160:163], v[48:51]\n"
"v_mfma_f32_16x16x32_bf16 v[156:159], a[188:191], v[192:195], v[52:55]\n"
"v_exp_f32 v100, v100\n"
"v_mfma_f32_16x16x32_bf16 v[172:175], a[188:191], v[224:227], v[56:59]\n"
"s_waitcnt lgkmcnt(2)\n"
"v_exp_f32 v101, v101\n"
"v_mfma_f32_16x16x32_bf16 v[108:111], a[192:195], v[104:107], v[108:111]\n"
"v_exp_f32 v102, v102\n"
"v_mfma_f32_16x16x32_bf16 v[124:127], a[192:195], v[136:139], v[124:127]\n"
"v_exp_f32 v103, v103\n"
"v_mfma_f32_16x16x32_bf16 v[140:143], a[192:195], v[168:171], v[140:143]\n"
"ds_read_b64_tr_b16 a[214:215], v38 offset:4096\n"
"v_cvt_pk_bf16_f32 v180, v100, v101\n"
"v_cvt_pk_bf16_f32 v181, v102, v103\n"
"v_mfma_f32_16x16x32_bf16 v[156:159], a[192:195], v[200:203], v[156:159]\n"
"ds_read_b64_tr_b16 a[212:213], v38 offset:0\n"
"v_exp_f32 v116, v116\n"
"v_mfma_f32_16x16x32_bf16 v[172:175], a[192:195], v[232:235], v[172:175]\n"
"s_waitcnt lgkmcnt(3)\n"
"v_exp_f32 v117, v117\n"
"v_mfma_f32_16x16x32_bf16 v[108:111], a[196:199], v[112:115], v[108:111]\n"
"v_exp_f32 v118, v118\n"
"v_mfma_f32_16x16x32_bf16 v[124:127], a[196:199], v[144:147], v[124:127]\n"
"v_exp_f32 v119, v119\n"
"v_mfma_f32_16x16x32_bf16 v[140:143], a[196:199], v[176:179], v[140:143]\n"
"ds_read_b64_tr_b16 a[216:217], v39 offset:0\n"
"v_cvt_pk_bf16_f32 v188, v116, v117\n"
"v_cvt_pk_bf16_f32 v189, v118, v119\n"
"v_mfma_f32_16x16x32_bf16 v[156:159], a[196:199], v[208:211], v[156:159]\n"
"ds_read_b64_tr_b16 a[218:219], v39 offset:4096\n"
"v_exp_f32 v132, v132\n"
"v_mfma_f32_16x16x32_bf16 v[172:175], a[196:199], v[240:243], v[172:175]\n"
"s_waitcnt lgkmcnt(4)\n"
"v_exp_f32 v133, v133\n"
"v_mfma_f32_16x16x32_bf16 v[108:111], a[200:203], v[120:123], v[108:111]\n"
"v_exp_f32 v134, v134\n"
"v_mfma_f32_16x16x32_bf16 v[124:127], a[200:203], v[152:155], v[124:127]\n"
"ds_read_b64_tr_b16 a[220:221], v38 offset:512\n"
"v_exp_f32 v135, v135\n"
"v_mfma_f32_16x16x32_bf16 v[140:143], a[200:203], v[184:187], v[140:143]\n"
"ds_read_b64_tr_b16 a[222:223], v38 offset:4608\n"
"v_cvt_pk_bf16_f32 v196, v132, v133\n"
"v_cvt_pk_bf16_f32 v197, v134, v135\n"
"v_mfma_f32_16x16x32_bf16 v[156:159], a[200:203], v[216:219], v[156:159]\n"
"v_exp_f32 v148, v148\n"
"v_mfma_f32_16x16x32_bf16 v[172:175], a[200:203], v[248:251], v[172:175]\n"
"v_exp_f32 v149, v149\n"
"v_mfma_f32_16x16x32_bf16 a[160:163], v[60:63], v[212:215], a[160:163]\n"
"ds_read_b64_tr_b16 a[224:225], v39 offset:512\n"
"v_exp_f32 v150, v150\n"
"v_mfma_f32_16x16x32_bf16 a[164:167], v[60:63], v[220:223], a[164:167]\n"
"ds_read_b64_tr_b16 a[226:227], v39 offset:4608\n"
"v_exp_f32 v151, v151\n"
"v_mfma_f32_16x16x32_bf16 a[168:171], v[60:63], v[228:231], a[168:171]\n"
"v_cvt_pk_bf16_f32 v204, v148, v149\n"
"v_cvt_pk_bf16_f32 v205, v150, v151\n"
"v_mfma_f32_16x16x32_bf16 a[172:175], v[60:63], v[236:239], a[172:175]\n"
"s_waitcnt lgkmcnt(6)\n"
"v_exp_f32 v164, v164\n"
"v_mfma_f32_16x16x32_bf16 a[0:3], a[212:215], v[212:215], a[0:3]\n"
"ds_read_b64_tr_b16 a[204:205], v38 offset:1024\n"
"v_exp_f32 v165, v165\n"
"v_mfma_f32_16x16x32_bf16 a[32:35], a[212:215], v[220:223], a[32:35]\n"
"ds_read_b64_tr_b16 a[206:207], v38 offset:5120\n"
"v_exp_f32 v166, v166\n"
"v_mfma_f32_16x16x32_bf16 a[64:67], a[212:215], v[228:231], a[64:67]\n"
"v_exp_f32 v167, v167\n"
"v_mfma_f32_16x16x32_bf16 a[96:99], a[212:215], v[236:239], a[96:99]\n"
"s_waitcnt lgkmcnt(6)\n"
"v_cvt_pk_bf16_f32 v244, v164, v165\n"
"v_cvt_pk_bf16_f32 v245, v166, v167\n"
"v_mfma_f32_16x16x32_bf16 a[4:7], a[216:219], v[212:215], a[4:7]\n"
"ds_read_b64_tr_b16 a[208:209], v39 offset:1024\n"
"v_exp_f32 v108, v108\n"
"v_mfma_f32_16x16x32_bf16 a[36:39], a[216:219], v[220:223], a[36:39]\n"
"ds_read_b64_tr_b16 a[210:211], v39 offset:5120\n"
"v_exp_f32 v109, v109\n"
"v_mfma_f32_16x16x32_bf16 a[68:71], a[216:219], v[228:231], a[68:71]\n"
"v_exp_f32 v110, v110\n"
"v_mfma_f32_16x16x32_bf16 a[100:103], a[216:219], v[236:239], a[100:103]\n"
"s_waitcnt lgkmcnt(6)\n"
"v_exp_f32 v111, v111\n"
"v_mfma_f32_16x16x32_bf16 a[8:11], a[220:223], v[212:215], a[8:11]\n"
"ds_read_b64_tr_b16 a[212:213], v38 offset:1536\n"
"v_cvt_pk_bf16_f32 v182, v108, v109\n"
"v_cvt_pk_bf16_f32 v183, v110, v111\n"
"v_mfma_f32_16x16x32_bf16 a[40:43], a[220:223], v[220:223], a[40:43]\n"
"ds_read_b64_tr_b16 a[214:215], v38 offset:5632\n"
"v_exp_f32 v124, v124\n"
"v_mfma_f32_16x16x32_bf16 a[72:75], a[220:223], v[228:231], a[72:75]\n"
"v_exp_f32 v125, v125\n"
"v_mfma_f32_16x16x32_bf16 a[104:107], a[220:223], v[236:239], a[104:107]\n"
"s_waitcnt lgkmcnt(6)\n"
"v_exp_f32 v126, v126\n"
"v_mfma_f32_16x16x32_bf16 a[12:15], a[224:227], v[212:215], a[12:15]\n"
"ds_read_b64_tr_b16 a[216:217], v39 offset:1536\n"
"v_exp_f32 v127, v127\n"
"v_mfma_f32_16x16x32_bf16 a[44:47], a[224:227], v[220:223], a[44:47]\n"
"ds_read_b64_tr_b16 a[218:219], v39 offset:5632\n"
"v_cvt_pk_bf16_f32 v190, v124, v125\n"
"v_cvt_pk_bf16_f32 v191, v126, v127\n"
"v_mfma_f32_16x16x32_bf16 a[76:79], a[224:227], v[228:231], a[76:79]\n"
"v_exp_f32 v140, v140\n"
"v_mfma_f32_16x16x32_bf16 a[108:111], a[224:227], v[236:239], a[108:111]\n"
"s_waitcnt lgkmcnt(6)\n"
"v_exp_f32 v141, v141\n"
"v_mfma_f32_16x16x32_bf16 a[16:19], a[204:207], v[212:215], a[16:19]\n"
"ds_read_b128 a[180:183], v32 offset:16384\n"
"v_exp_f32 v142, v142\n"
"v_mfma_f32_16x16x32_bf16 a[48:51], a[204:207], v[220:223], a[48:51]\n"
"v_exp_f32 v143, v143\n"
"v_mfma_f32_16x16x32_bf16 a[80:83], a[204:207], v[228:231], a[80:83]\n"
"v_cvt_pk_bf16_f32 v198, v140, v141\n"
"v_cvt_pk_bf16_f32 v199, v142, v143\n"
"v_mfma_f32_16x16x32_bf16 a[112:115], a[204:207], v[236:239], a[112:115]\n"
"s_waitcnt lgkmcnt(5)\n"
"v_exp_f32 v156, v156\n"
"v_mfma_f32_16x16x32_bf16 a[20:23], a[208:211], v[212:215], a[20:23]\n"
"ds_read_b128 a[184:187], v33 offset:16384\n"
"v_exp_f32 v157, v157\n"
"v_mfma_f32_16x16x32_bf16 a[52:55], a[208:211], v[220:223], a[52:55]\n"
"v_exp_f32 v158, v158\n"
"v_mfma_f32_16x16x32_bf16 a[84:87], a[208:211], v[228:231], a[84:87]\n"
"v_exp_f32 v159, v159\n"
"v_mfma_f32_16x16x32_bf16 a[116:119], a[208:211], v[236:239], a[116:119]\n"
"s_waitcnt lgkmcnt(4)\n"
"v_cvt_pk_bf16_f32 v206, v156, v157\n"
"v_cvt_pk_bf16_f32 v207, v158, v159\n"
"v_mfma_f32_16x16x32_bf16 a[24:27], a[212:215], v[212:215], a[24:27]\n"
"ds_read_b128 a[188:191], v34 offset:16384\n"
"v_exp_f32 v172, v172\n"
"v_mfma_f32_16x16x32_bf16 a[56:59], a[212:215], v[220:223], a[56:59]\n"
"v_exp_f32 v173, v173\n"
"v_mfma_f32_16x16x32_bf16 a[88:91], a[212:215], v[228:231], a[88:91]\n"
"v_exp_f32 v174, v174\n"
"v_mfma_f32_16x16x32_bf16 a[120:123], a[212:215], v[236:239], a[120:123]\n"
"s_waitcnt lgkmcnt(3)\n"
"v_exp_f32 v175, v175\n"
"v_mfma_f32_16x16x32_bf16 a[28:31], a[216:219], v[212:215], a[28:31]\n"
"ds_read_b128 a[192:195], v35 offset:16384\n"
"v_cvt_pk_bf16_f32 v246, v172, v173\n"
"v_cvt_pk_bf16_f32 v247, v174, v175\n"
"v_mfma_f32_16x16x32_bf16 a[60:63], a[216:219], v[220:223], a[60:63]\n"
"v_mfma_f32_16x16x32_bf16 a[92:95], a[216:219], v[228:231], a[92:95]\n"
"v_mfma_f32_16x16x32_bf16 a[124:127], a[216:219], v[236:239], a[124:127]\n"
"s_waitcnt lgkmcnt(3)\n"
"v_mfma_f32_16x16x32_bf16 v[100:103], a[180:183], v[96:99], v[40:43]\n"
"ds_read_b128 a[196:199], v32 offset:20480\n"
"v_mfma_f32_16x16x32_bf16 v[116:119], a[180:183], v[128:131], v[44:47]\n"
"v_mfma_f32_16x16x32_bf16 v[132:135], a[180:183], v[160:163], v[48:51]\n"
"v_mfma_f32_16x16x32_bf16 v[148:151], a[180:183], v[192:195], v[52:55]\n"
"s_waitcnt lgkmcnt(3)\n"
"v_mfma_f32_16x16x32_bf16 v[100:103], a[184:187], v[104:107], v[100:103]\n"
"ds_read_b128 a[200:203], v33 offset:20480\n"
"v_mfma_f32_16x16x32_bf16 v[116:119], a[184:187], v[136:139], v[116:119]\n"
"v_mfma_f32_16x16x32_bf16 v[132:135], a[184:187], v[168:171], v[132:135]\n"
"v_mfma_f32_16x16x32_bf16 v[148:151], a[184:187], v[200:203], v[148:151]\n"
"s_waitcnt lgkmcnt(3)\n"
"v_mfma_f32_16x16x32_bf16 v[100:103], a[188:191], v[112:115], v[100:103]\n"
"ds_read_b128 a[180:183], v34 offset:20480\n"
"v_mfma_f32_16x16x32_bf16 v[116:119], a[188:191], v[144:147], v[116:119]\n"
"v_mfma_f32_16x16x32_bf16 v[132:135], a[188:191], v[176:179], v[132:135]\n"
"v_mfma_f32_16x16x32_bf16 v[148:151], a[188:191], v[208:211], v[148:151]\n"
"s_waitcnt lgkmcnt(3)\n"
"v_mfma_f32_16x16x32_bf16 v[100:103], a[192:195], v[120:123], v[100:103]\n"
"ds_read_b128 a[184:187], v35 offset:20480\n"
"v_mfma_f32_16x16x32_bf16 v[116:119], a[192:195], v[152:155], v[116:119]\n"
"v_mfma_f32_16x16x32_bf16 v[132:135], a[192:195], v[184:187], v[132:135]\n"
"v_mfma_f32_16x16x32_bf16 v[148:151], a[192:195], v[216:219], v[148:151]\n"
"s_waitcnt lgkmcnt(3)\n"
"v_mfma_f32_16x16x32_bf16 v[108:111], a[196:199], v[96:99], v[40:43]\n"
"v_mfma_f32_16x16x32_bf16 v[124:127], a[196:199], v[128:131], v[44:47]\n"
"ds_read_b64_tr_b16 a[220:221], v36 offset:16384\n"
"v_mfma_f32_16x16x32_bf16 v[140:143], a[196:199], v[160:163], v[48:51]\n"
"ds_read_b64_tr_b16 a[222:223], v36 offset:20480\n"
"v_mfma_f32_16x16x32_bf16 v[156:159], a[196:199], v[192:195], v[52:55]\n"
"s_waitcnt lgkmcnt(4)\n"
"v_mfma_f32_16x16x32_bf16 v[108:111], a[200:203], v[104:107], v[108:111]\n"
"v_exp_f32 v100, v100\n"
"v_mfma_f32_16x16x32_bf16 v[124:127], a[200:203], v[136:139], v[124:127]\n"
"ds_read_b64_tr_b16 a[226:227], v37 offset:20480\n"
"v_exp_f32 v101, v101\n"
"v_mfma_f32_16x16x32_bf16 v[140:143], a[200:203], v[168:171], v[140:143]\n"
"ds_read_b64_tr_b16 a[224:225], v37 offset:16384\n"
"v_exp_f32 v102, v102\n"
"v_mfma_f32_16x16x32_bf16 v[156:159], a[200:203], v[200:203], v[156:159]\n"
"s_waitcnt lgkmcnt(5)\n"
"v_exp_f32 v103, v103\n"
"v_mfma_f32_16x16x32_bf16 v[108:111], a[180:183], v[112:115], v[108:111]\n"
"v_cvt_pk_bf16_f32 v212, v100, v101\n"
"v_cvt_pk_bf16_f32 v213, v102, v103\n"
"v_mfma_f32_16x16x32_bf16 v[124:127], a[180:183], v[144:147], v[124:127]\n"
"v_exp_f32 v116, v116\n"
"v_mfma_f32_16x16x32_bf16 v[140:143], a[180:183], v[176:179], v[140:143]\n"
"ds_read_b64_tr_b16 a[204:205], v36 offset:16896\n"
"v_exp_f32 v117, v117\n"
"v_mfma_f32_16x16x32_bf16 v[156:159], a[180:183], v[208:211], v[156:159]\n"
"s_waitcnt lgkmcnt(5)\n"
"v_exp_f32 v118, v118\n"
"v_mfma_f32_16x16x32_bf16 v[108:111], a[184:187], v[120:123], v[108:111]\n"
"ds_read_b64_tr_b16 a[206:207], v36 offset:20992\n"
"v_exp_f32 v119, v119\n"
"v_mfma_f32_16x16x32_bf16 v[124:127], a[184:187], v[152:155], v[124:127]\n"
"v_cvt_pk_bf16_f32 v220, v116, v117\n"
"v_cvt_pk_bf16_f32 v221, v118, v119\n"
"v_mfma_f32_16x16x32_bf16 v[140:143], a[184:187], v[184:187], v[140:143]\n"
"v_exp_f32 v132, v132\n"
"v_mfma_f32_16x16x32_bf16 v[156:159], a[184:187], v[216:219], v[156:159]\n"
"s_waitcnt vmcnt(8)\n"
"s_barrier\n"
"v_exp_f32 v133, v133\n"
"v_mfma_f32_16x16x32_bf16 a[160:163], v[60:63], v[180:183], a[160:163]\n"
"ds_read_b64_tr_b16 a[208:209], v37 offset:16896\n"
"v_exp_f32 v134, v134\n"
"v_mfma_f32_16x16x32_bf16 a[164:167], v[60:63], v[188:191], a[164:167]\n"
"ds_read_b64_tr_b16 a[210:211], v37 offset:20992\n"
"v_exp_f32 v135, v135\n"
"v_mfma_f32_16x16x32_bf16 a[168:171], v[60:63], v[196:199], a[168:171]\n"
"v_cvt_pk_bf16_f32 v228, v132, v133\n"
"v_cvt_pk_bf16_f32 v229, v134, v135\n"
"v_mfma_f32_16x16x32_bf16 a[172:175], v[60:63], v[204:207], a[172:175]\n"
"v_exp_f32 v148, v148\n"
"v_mfma_f32_16x16x32_bf16 a[176:179], v[60:63], v[244:247], a[176:179]\n"
"s_waitcnt lgkmcnt(6)\n"
"v_exp_f32 v149, v149\n"
"v_mfma_f32_16x16x32_bf16 a[0:3], a[220:223], v[180:183], a[0:3]\n"
"ds_read_b64_tr_b16 a[212:213], v36 offset:17408\n"
"v_exp_f32 v150, v150\n"
"v_mfma_f32_16x16x32_bf16 a[32:35], a[220:223], v[188:191], a[32:35]\n"
"ds_read_b64_tr_b16 a[214:215], v36 offset:21504\n"
"v_exp_f32 v151, v151\n"
"v_mfma_f32_16x16x32_bf16 a[64:67], a[220:223], v[196:199], a[64:67]\n"
"v_cvt_pk_bf16_f32 v236, v148, v149\n"
"v_cvt_pk_bf16_f32 v237, v150, v151\n"
"v_mfma_f32_16x16x32_bf16 a[96:99], a[220:223], v[204:207], a[96:99]\n"
"v_exp_f32 v108, v108\n"
"v_mfma_f32_16x16x32_bf16 a[128:131], a[220:223], v[244:247], a[128:131]\n"
"s_waitcnt lgkmcnt(6)\n"
"v_exp_f32 v109, v109\n"
"v_mfma_f32_16x16x32_bf16 a[4:7], a[224:227], v[180:183], a[4:7]\n"
"ds_read_b64_tr_b16 a[216:217], v37 offset:17408\n"
"v_exp_f32 v110, v110\n"
"v_mfma_f32_16x16x32_bf16 a[36:39], a[224:227], v[188:191], a[36:39]\n"
"ds_read_b64_tr_b16 a[218:219], v37 offset:21504\n"
"v_exp_f32 v111, v111\n"
"v_mfma_f32_16x16x32_bf16 a[68:71], a[224:227], v[196:199], a[68:71]\n"
"v_cvt_pk_bf16_f32 v214, v108, v109\n"
"v_cvt_pk_bf16_f32 v215, v110, v111\n"
"v_mfma_f32_16x16x32_bf16 a[100:103], a[224:227], v[204:207], a[100:103]\n"
"v_exp_f32 v124, v124\n"
"v_mfma_f32_16x16x32_bf16 a[132:135], a[224:227], v[244:247], a[132:135]\n"
"s_waitcnt lgkmcnt(6)\n"
"v_exp_f32 v125, v125\n"
"v_mfma_f32_16x16x32_bf16 a[8:11], a[204:207], v[180:183], a[8:11]\n"
"ds_read_b64_tr_b16 a[220:221], v36 offset:17920\n"
"v_exp_f32 v126, v126\n"
"v_mfma_f32_16x16x32_bf16 a[40:43], a[204:207], v[188:191], a[40:43]\n"
"ds_read_b64_tr_b16 a[222:223], v36 offset:22016\n"
"v_exp_f32 v127, v127\n"
"v_mfma_f32_16x16x32_bf16 a[72:75], a[204:207], v[196:199], a[72:75]\n"
"v_cvt_pk_bf16_f32 v222, v124, v125\n"
"v_cvt_pk_bf16_f32 v223, v126, v127\n"
"v_mfma_f32_16x16x32_bf16 a[104:107], a[204:207], v[204:207], a[104:107]\n"
"v_exp_f32 v140, v140\n"
"v_mfma_f32_16x16x32_bf16 a[136:139], a[204:207], v[244:247], a[136:139]\n"
"s_waitcnt lgkmcnt(6)\n"
"v_exp_f32 v141, v141\n"
"v_mfma_f32_16x16x32_bf16 a[12:15], a[208:211], v[180:183], a[12:15]\n"
"ds_read_b64_tr_b16 a[224:225], v37 offset:17920\n"
"v_exp_f32 v142, v142\n"
"v_mfma_f32_16x16x32_bf16 a[44:47], a[208:211], v[188:191], a[44:47]\n"
"ds_read_b64_tr_b16 a[226:227], v37 offset:22016\n"
"v_exp_f32 v143, v143\n"
"v_mfma_f32_16x16x32_bf16 a[76:79], a[208:211], v[196:199], a[76:79]\n"
"v_cvt_pk_bf16_f32 v230, v140, v141\n"
"v_cvt_pk_bf16_f32 v231, v142, v143\n"
"v_mfma_f32_16x16x32_bf16 a[108:111], a[208:211], v[204:207], a[108:111]\n"
"v_exp_f32 v156, v156\n"
"v_mfma_f32_16x16x32_bf16 a[140:143], a[208:211], v[244:247], a[140:143]\n"
"s_waitcnt lgkmcnt(6)\n"
"v_exp_f32 v157, v157\n"
"v_mfma_f32_16x16x32_bf16 a[16:19], a[212:215], v[180:183], a[16:19]\n"
"ds_read_b128 a[188:191], v28 offset:32768\n"
"v_exp_f32 v158, v158\n"
"v_mfma_f32_16x16x32_bf16 a[48:51], a[212:215], v[188:191], a[48:51]\n"
"v_exp_f32 v159, v159\n"
"v_mfma_f32_16x16x32_bf16 a[80:83], a[212:215], v[196:199], a[80:83]\n"
"v_cvt_pk_bf16_f32 v238, v156, v157\n"
"v_cvt_pk_bf16_f32 v239, v158, v159\n"
"v_mfma_f32_16x16x32_bf16 a[112:115], a[212:215], v[204:207], a[112:115]\n"
"v_mfma_f32_16x16x32_bf16 a[144:147], a[212:215], v[244:247], a[144:147]\n"
"s_waitcnt lgkmcnt(5)\n"
"v_mfma_f32_16x16x32_bf16 a[20:23], a[216:219], v[180:183], a[20:23]\n"
"ds_read_b128 a[192:195], v29 offset:32768\n"
"v_mfma_f32_16x16x32_bf16 a[52:55], a[216:219], v[188:191], a[52:55]\n"
"v_mfma_f32_16x16x32_bf16 a[84:87], a[216:219], v[196:199], a[84:87]\n"
"v_mfma_f32_16x16x32_bf16 a[116:119], a[216:219], v[204:207], a[116:119]\n"
"v_mfma_f32_16x16x32_bf16 a[148:151], a[216:219], v[244:247], a[148:151]\n"
"s_waitcnt lgkmcnt(4)\n"
"v_mfma_f32_16x16x32_bf16 a[24:27], a[220:223], v[180:183], a[24:27]\n"
"ds_read_b128 a[196:199], v30 offset:32768\n"
"v_mfma_f32_16x16x32_bf16 a[56:59], a[220:223], v[188:191], a[56:59]\n"
"v_mfma_f32_16x16x32_bf16 a[88:91], a[220:223], v[196:199], a[88:91]\n"
"v_mfma_f32_16x16x32_bf16 a[120:123], a[220:223], v[204:207], a[120:123]\n"
"v_mfma_f32_16x16x32_bf16 a[152:155], a[220:223], v[244:247], a[152:155]\n"
"s_waitcnt lgkmcnt(3)\n"
"v_mfma_f32_16x16x32_bf16 a[28:31], a[224:227], v[180:183], a[28:31]\n"
"ds_read_b128 a[200:203], v31 offset:32768\n"
"v_mfma_f32_16x16x32_bf16 a[60:63], a[224:227], v[188:191], a[60:63]\n"
"v_mfma_f32_16x16x32_bf16 a[92:95], a[224:227], v[196:199], a[92:95]\n"
"v_mfma_f32_16x16x32_bf16 a[124:127], a[224:227], v[204:207], a[124:127]\n"
"v_mfma_f32_16x16x32_bf16 a[156:159], a[224:227], v[244:247], a[156:159]\n"
"s_waitcnt lgkmcnt(3)\n"
"v_mfma_f32_16x16x32_bf16 v[100:103], a[188:191], v[96:99], v[40:43]\n"
"ds_read_b128 a[180:183], v28 offset:36864\n"
"v_mfma_f32_16x16x32_bf16 v[116:119], a[188:191], v[128:131], v[44:47]\n"
"v_mfma_f32_16x16x32_bf16 v[132:135], a[188:191], v[160:163], v[48:51]\n"
"v_mfma_f32_16x16x32_bf16 v[148:151], a[188:191], v[192:195], v[52:55]\n"
"v_mfma_f32_16x16x32_bf16 v[164:167], a[188:191], v[224:227], v[56:59]\n"
"s_waitcnt lgkmcnt(3)\n"
"v_mfma_f32_16x16x32_bf16 v[100:103], a[192:195], v[104:107], v[100:103]\n"
"ds_read_b128 a[184:187], v29 offset:36864\n"
"v_mfma_f32_16x16x32_bf16 v[116:119], a[192:195], v[136:139], v[116:119]\n"
"v_mfma_f32_16x16x32_bf16 v[132:135], a[192:195], v[168:171], v[132:135]\n"
"v_mfma_f32_16x16x32_bf16 v[148:151], a[192:195], v[200:203], v[148:151]\n"
"v_mfma_f32_16x16x32_bf16 v[164:167], a[192:195], v[232:235], v[164:167]\n"
"s_waitcnt lgkmcnt(3)\n"
"v_mfma_f32_16x16x32_bf16 v[100:103], a[196:199], v[112:115], v[100:103]\n"
"ds_read_b128 a[188:191], v30 offset:36864\n"
"v_mfma_f32_16x16x32_bf16 v[116:119], a[196:199], v[144:147], v[116:119]\n"
"v_mfma_f32_16x16x32_bf16 v[132:135], a[196:199], v[176:179], v[132:135]\n"
"v_mfma_f32_16x16x32_bf16 v[148:151], a[196:199], v[208:211], v[148:151]\n"
"v_mfma_f32_16x16x32_bf16 v[164:167], a[196:199], v[240:243], v[164:167]\n"
"s_waitcnt lgkmcnt(3)\n"
"v_mfma_f32_16x16x32_bf16 v[100:103], a[200:203], v[120:123], v[100:103]\n"
"ds_read_b128 a[192:195], v31 offset:36864\n"
"v_mfma_f32_16x16x32_bf16 v[116:119], a[200:203], v[152:155], v[116:119]\n"
"v_mfma_f32_16x16x32_bf16 v[132:135], a[200:203], v[184:187], v[132:135]\n"
"v_mfma_f32_16x16x32_bf16 v[148:151], a[200:203], v[216:219], v[148:151]\n"
"v_mfma_f32_16x16x32_bf16 v[164:167], a[200:203], v[248:251], v[164:167]\n"
"s_waitcnt lgkmcnt(3)\n"
"v_mfma_f32_16x16x32_bf16 v[108:111], a[180:183], v[96:99], v[40:43]\n"
"v_mfma_f32_16x16x32_bf16 v[124:127], a[180:183], v[128:131], v[44:47]\n"
"v_mfma_f32_16x16x32_bf16 v[140:143], a[180:183], v[160:163], v[48:51]\n"
"v_mfma_f32_16x16x32_bf16 v[156:159], a[180:183], v[192:195], v[52:55]\n"
"v_exp_f32 v100, v100\n"
"v_mfma_f32_16x16x32_bf16 v[172:175], a[180:183], v[224:227], v[56:59]\n"
"s_waitcnt lgkmcnt(2)\n"
"v_exp_f32 v101, v101\n"
"v_mfma_f32_16x16x32_bf16 v[108:111], a[184:187], v[104:107], v[108:111]\n"
"v_exp_f32 v102, v102\n"
"v_mfma_f32_16x16x32_bf16 v[124:127], a[184:187], v[136:139], v[124:127]\n"
"v_exp_f32 v103, v103\n"
"v_mfma_f32_16x16x32_bf16 v[140:143], a[184:187], v[168:171], v[140:143]\n"
"ds_read_b64_tr_b16 a[206:207], v38 offset:20480\n"
"v_cvt_pk_bf16_f32 v180, v100, v101\n"
"v_cvt_pk_bf16_f32 v181, v102, v103\n"
"v_mfma_f32_16x16x32_bf16 v[156:159], a[184:187], v[200:203], v[156:159]\n"
"ds_read_b64_tr_b16 a[204:205], v38 offset:16384\n"
"v_exp_f32 v116, v116\n"
"v_mfma_f32_16x16x32_bf16 v[172:175], a[184:187], v[232:235], v[172:175]\n"
"s_waitcnt lgkmcnt(3)\n"
"v_exp_f32 v117, v117\n"
"v_mfma_f32_16x16x32_bf16 v[108:111], a[188:191], v[112:115], v[108:111]\n"
"v_exp_f32 v118, v118\n"
"v_mfma_f32_16x16x32_bf16 v[124:127], a[188:191], v[144:147], v[124:127]\n"
"v_exp_f32 v119, v119\n"
"v_mfma_f32_16x16x32_bf16 v[140:143], a[188:191], v[176:179], v[140:143]\n"
"ds_read_b64_tr_b16 a[208:209], v39 offset:16384\n"
"v_cvt_pk_bf16_f32 v188, v116, v117\n"
"v_cvt_pk_bf16_f32 v189, v118, v119\n"
"v_mfma_f32_16x16x32_bf16 v[156:159], a[188:191], v[208:211], v[156:159]\n"
"ds_read_b64_tr_b16 a[210:211], v39 offset:20480\n"
"v_exp_f32 v132, v132\n"
"v_mfma_f32_16x16x32_bf16 v[172:175], a[188:191], v[240:243], v[172:175]\n"
"s_waitcnt lgkmcnt(4)\n"
"v_exp_f32 v133, v133\n"
"v_mfma_f32_16x16x32_bf16 v[108:111], a[192:195], v[120:123], v[108:111]\n"
"v_exp_f32 v134, v134\n"
"v_mfma_f32_16x16x32_bf16 v[124:127], a[192:195], v[152:155], v[124:127]\n"
"ds_read_b64_tr_b16 a[212:213], v38 offset:16896\n"
"v_exp_f32 v135, v135\n"
"v_mfma_f32_16x16x32_bf16 v[140:143], a[192:195], v[184:187], v[140:143]\n"
"ds_read_b64_tr_b16 a[214:215], v38 offset:20992\n"
"v_cvt_pk_bf16_f32 v196, v132, v133\n"
"v_cvt_pk_bf16_f32 v197, v134, v135\n"
"v_mfma_f32_16x16x32_bf16 v[156:159], a[192:195], v[216:219], v[156:159]\n"
"v_exp_f32 v148, v148\n"
"v_mfma_f32_16x16x32_bf16 v[172:175], a[192:195], v[248:251], v[172:175]\n"
"v_exp_f32 v149, v149\n"
"v_mfma_f32_16x16x32_bf16 a[160:163], v[60:63], v[212:215], a[160:163]\n"
"ds_read_b64_tr_b16 a[216:217], v39 offset:16896\n"
"v_exp_f32 v150, v150\n"
"v_mfma_f32_16x16x32_bf16 a[164:167], v[60:63], v[220:223], a[164:167]\n"
"ds_read_b64_tr_b16 a[218:219], v39 offset:20992\n"
"v_exp_f32 v151, v151\n"
"v_mfma_f32_16x16x32_bf16 a[168:171], v[60:63], v[228:231], a[168:171]\n"
"v_cvt_pk_bf16_f32 v204, v148, v149\n"
"v_cvt_pk_bf16_f32 v205, v150, v151\n"
"v_mfma_f32_16x16x32_bf16 a[172:175], v[60:63], v[236:239], a[172:175]\n"
"s_waitcnt lgkmcnt(6)\n"
"v_exp_f32 v164, v164\n"
"v_mfma_f32_16x16x32_bf16 a[0:3], a[204:207], v[212:215], a[0:3]\n"
"ds_read_b64_tr_b16 a[220:221], v38 offset:17408\n"
"v_exp_f32 v165, v165\n"
"v_mfma_f32_16x16x32_bf16 a[32:35], a[204:207], v[220:223], a[32:35]\n"
"ds_read_b64_tr_b16 a[222:223], v38 offset:21504\n"
"v_exp_f32 v166, v166\n"
"v_mfma_f32_16x16x32_bf16 a[64:67], a[204:207], v[228:231], a[64:67]\n"
"v_exp_f32 v167, v167\n"
"v_mfma_f32_16x16x32_bf16 a[96:99], a[204:207], v[236:239], a[96:99]\n"
"s_waitcnt lgkmcnt(6)\n"
"v_cvt_pk_bf16_f32 v244, v164, v165\n"
"v_cvt_pk_bf16_f32 v245, v166, v167\n"
"v_mfma_f32_16x16x32_bf16 a[4:7], a[208:211], v[212:215], a[4:7]\n"
"ds_read_b64_tr_b16 a[224:225], v39 offset:17408\n"
"v_exp_f32 v108, v108\n"
"v_mfma_f32_16x16x32_bf16 a[36:39], a[208:211], v[220:223], a[36:39]\n"
"ds_read_b64_tr_b16 a[226:227], v39 offset:21504\n"
"v_exp_f32 v109, v109\n"
"v_mfma_f32_16x16x32_bf16 a[68:71], a[208:211], v[228:231], a[68:71]\n"
"v_exp_f32 v110, v110\n"
"v_mfma_f32_16x16x32_bf16 a[100:103], a[208:211], v[236:239], a[100:103]\n"
"s_waitcnt lgkmcnt(6)\n"
"v_exp_f32 v111, v111\n"
"v_mfma_f32_16x16x32_bf16 a[8:11], a[212:215], v[212:215], a[8:11]\n"
"ds_read_b64_tr_b16 a[204:205], v38 offset:17920\n"
"v_cvt_pk_bf16_f32 v182, v108, v109\n"
"v_cvt_pk_bf16_f32 v183, v110, v111\n"
"v_mfma_f32_16x16x32_bf16 a[40:43], a[212:215], v[220:223], a[40:43]\n"
"ds_read_b64_tr_b16 a[206:207], v38 offset:22016\n"
"v_exp_f32 v124, v124\n"
"v_mfma_f32_16x16x32_bf16 a[72:75], a[212:215], v[228:231], a[72:75]\n"
"v_exp_f32 v125, v125\n"
"v_mfma_f32_16x16x32_bf16 a[104:107], a[212:215], v[236:239], a[104:107]\n"
"s_waitcnt lgkmcnt(6)\n"
"v_exp_f32 v126, v126\n"
"v_mfma_f32_16x16x32_bf16 a[12:15], a[216:219], v[212:215], a[12:15]\n"
"ds_read_b64_tr_b16 a[208:209], v39 offset:17920\n"
"v_exp_f32 v127, v127\n"
"v_mfma_f32_16x16x32_bf16 a[44:47], a[216:219], v[220:223], a[44:47]\n"
"ds_read_b64_tr_b16 a[210:211], v39 offset:22016\n"
"v_cvt_pk_bf16_f32 v190, v124, v125\n"
"v_cvt_pk_bf16_f32 v191, v126, v127\n"
"v_mfma_f32_16x16x32_bf16 a[76:79], a[216:219], v[228:231], a[76:79]\n"
"v_exp_f32 v140, v140\n"
"v_mfma_f32_16x16x32_bf16 a[108:111], a[216:219], v[236:239], a[108:111]\n"
"s_waitcnt lgkmcnt(6)\n"
"v_exp_f32 v141, v141\n"
"v_mfma_f32_16x16x32_bf16 a[16:19], a[220:223], v[212:215], a[16:19]\n"
"ds_read_b128 a[196:199], v32 offset:32768\n"
"v_exp_f32 v142, v142\n"
"v_mfma_f32_16x16x32_bf16 a[48:51], a[220:223], v[220:223], a[48:51]\n"
"v_exp_f32 v143, v143\n"
"v_mfma_f32_16x16x32_bf16 a[80:83], a[220:223], v[228:231], a[80:83]\n"
"v_cvt_pk_bf16_f32 v198, v140, v141\n"
"v_cvt_pk_bf16_f32 v199, v142, v143\n"
"v_mfma_f32_16x16x32_bf16 a[112:115], a[220:223], v[236:239], a[112:115]\n"
"s_waitcnt lgkmcnt(5)\n"
"v_exp_f32 v156, v156\n"
"v_mfma_f32_16x16x32_bf16 a[20:23], a[224:227], v[212:215], a[20:23]\n"
"ds_read_b128 a[200:203], v33 offset:32768\n"
"v_exp_f32 v157, v157\n"
"v_mfma_f32_16x16x32_bf16 a[52:55], a[224:227], v[220:223], a[52:55]\n"
"v_exp_f32 v158, v158\n"
"v_mfma_f32_16x16x32_bf16 a[84:87], a[224:227], v[228:231], a[84:87]\n"
"v_exp_f32 v159, v159\n"
"v_mfma_f32_16x16x32_bf16 a[116:119], a[224:227], v[236:239], a[116:119]\n"
"s_waitcnt lgkmcnt(4)\n"
"v_cvt_pk_bf16_f32 v206, v156, v157\n"
"v_cvt_pk_bf16_f32 v207, v158, v159\n"
"v_mfma_f32_16x16x32_bf16 a[24:27], a[204:207], v[212:215], a[24:27]\n"
"ds_read_b128 a[180:183], v34 offset:32768\n"
"v_exp_f32 v172, v172\n"
"v_mfma_f32_16x16x32_bf16 a[56:59], a[204:207], v[220:223], a[56:59]\n"
"v_exp_f32 v173, v173\n"
"v_mfma_f32_16x16x32_bf16 a[88:91], a[204:207], v[228:231], a[88:91]\n"
"v_exp_f32 v174, v174\n"
"v_mfma_f32_16x16x32_bf16 a[120:123], a[204:207], v[236:239], a[120:123]\n"
"s_waitcnt lgkmcnt(3)\n"
"v_exp_f32 v175, v175\n"
"v_mfma_f32_16x16x32_bf16 a[28:31], a[208:211], v[212:215], a[28:31]\n"
"ds_read_b128 a[184:187], v35 offset:32768\n"
"v_cvt_pk_bf16_f32 v246, v172, v173\n"
"v_cvt_pk_bf16_f32 v247, v174, v175\n"
"v_mfma_f32_16x16x32_bf16 a[60:63], a[208:211], v[220:223], a[60:63]\n"
"v_mfma_f32_16x16x32_bf16 a[92:95], a[208:211], v[228:231], a[92:95]\n"
"v_mfma_f32_16x16x32_bf16 a[124:127], a[208:211], v[236:239], a[124:127]\n"
"s_waitcnt lgkmcnt(3)\n"
"v_mfma_f32_16x16x32_bf16 v[100:103], a[196:199], v[96:99], v[40:43]\n"
"ds_read_b128 a[188:191], v32 offset:36864\n"
"v_mfma_f32_16x16x32_bf16 v[116:119], a[196:199], v[128:131], v[44:47]\n"
"v_mfma_f32_16x16x32_bf16 v[132:135], a[196:199], v[160:163], v[48:51]\n"
"v_mfma_f32_16x16x32_bf16 v[148:151], a[196:199], v[192:195], v[52:55]\n"
"s_waitcnt lgkmcnt(3)\n"
"v_mfma_f32_16x16x32_bf16 v[100:103], a[200:203], v[104:107], v[100:103]\n"
"ds_read_b128 a[192:195], v33 offset:36864\n"
"v_mfma_f32_16x16x32_bf16 v[116:119], a[200:203], v[136:139], v[116:119]\n"
"v_mfma_f32_16x16x32_bf16 v[132:135], a[200:203], v[168:171], v[132:135]\n"
"v_mfma_f32_16x16x32_bf16 v[148:151], a[200:203], v[200:203], v[148:151]\n"
"s_waitcnt lgkmcnt(3)\n"
"v_mfma_f32_16x16x32_bf16 v[100:103], a[180:183], v[112:115], v[100:103]\n"
"ds_read_b128 a[196:199], v34 offset:36864\n"
"v_mfma_f32_16x16x32_bf16 v[116:119], a[180:183], v[144:147], v[116:119]\n"
"v_mfma_f32_16x16x32_bf16 v[132:135], a[180:183], v[176:179], v[132:135]\n"
"v_mfma_f32_16x16x32_bf16 v[148:151], a[180:183], v[208:211], v[148:151]\n"
"s_waitcnt lgkmcnt(3)\n"
"v_mfma_f32_16x16x32_bf16 v[100:103], a[184:187], v[120:123], v[100:103]\n"
"ds_read_b128 a[200:203], v35 offset:36864\n"
"v_mfma_f32_16x16x32_bf16 v[116:119], a[184:187], v[152:155], v[116:119]\n"
"v_mfma_f32_16x16x32_bf16 v[132:135], a[184:187], v[184:187], v[132:135]\n"
"v_mfma_f32_16x16x32_bf16 v[148:151], a[184:187], v[216:219], v[148:151]\n"
"s_waitcnt lgkmcnt(3)\n"
"v_mfma_f32_16x16x32_bf16 v[108:111], a[188:191], v[96:99], v[40:43]\n"
"v_mfma_f32_16x16x32_bf16 v[124:127], a[188:191], v[128:131], v[44:47]\n"
"ds_read_b64_tr_b16 a[212:213], v36 offset:32768\n"
"v_mfma_f32_16x16x32_bf16 v[140:143], a[188:191], v[160:163], v[48:51]\n"
"ds_read_b64_tr_b16 a[214:215], v36 offset:36864\n"
"v_mfma_f32_16x16x32_bf16 v[156:159], a[188:191], v[192:195], v[52:55]\n"
"s_waitcnt lgkmcnt(4)\n"
"v_mfma_f32_16x16x32_bf16 v[108:111], a[192:195], v[104:107], v[108:111]\n"
"v_exp_f32 v100, v100\n"
"v_mfma_f32_16x16x32_bf16 v[124:127], a[192:195], v[136:139], v[124:127]\n"
"ds_read_b64_tr_b16 a[218:219], v37 offset:36864\n"
"v_exp_f32 v101, v101\n"
"v_mfma_f32_16x16x32_bf16 v[140:143], a[192:195], v[168:171], v[140:143]\n"
"ds_read_b64_tr_b16 a[216:217], v37 offset:32768\n"
"v_exp_f32 v102, v102\n"
"v_mfma_f32_16x16x32_bf16 v[156:159], a[192:195], v[200:203], v[156:159]\n"
"s_waitcnt lgkmcnt(5)\n"
"v_exp_f32 v103, v103\n"
"v_mfma_f32_16x16x32_bf16 v[108:111], a[196:199], v[112:115], v[108:111]\n"
"v_cvt_pk_bf16_f32 v212, v100, v101\n"
"v_cvt_pk_bf16_f32 v213, v102, v103\n"
"v_mfma_f32_16x16x32_bf16 v[124:127], a[196:199], v[144:147], v[124:127]\n"
"v_exp_f32 v116, v116\n"
"v_mfma_f32_16x16x32_bf16 v[140:143], a[196:199], v[176:179], v[140:143]\n"
"ds_read_b64_tr_b16 a[220:221], v36 offset:33280\n"
"v_exp_f32 v117, v117\n"
"v_mfma_f32_16x16x32_bf16 v[156:159], a[196:199], v[208:211], v[156:159]\n"
"s_waitcnt lgkmcnt(5)\n"
"v_exp_f32 v118, v118\n"
"v_mfma_f32_16x16x32_bf16 v[108:111], a[200:203], v[120:123], v[108:111]\n"
"ds_read_b64_tr_b16 a[222:223], v36 offset:37376\n"
"v_exp_f32 v119, v119\n"
"v_mfma_f32_16x16x32_bf16 v[124:127], a[200:203], v[152:155], v[124:127]\n"
"v_cvt_pk_bf16_f32 v220, v116, v117\n"
"v_cvt_pk_bf16_f32 v221, v118, v119\n"
"v_mfma_f32_16x16x32_bf16 v[140:143], a[200:203], v[184:187], v[140:143]\n"
"v_exp_f32 v132, v132\n"
"v_mfma_f32_16x16x32_bf16 v[156:159], a[200:203], v[216:219], v[156:159]\n"
"s_waitcnt vmcnt(0)\n"
"s_barrier\n"
"v_exp_f32 v133, v133\n"
"v_mfma_f32_16x16x32_bf16 a[160:163], v[60:63], v[180:183], a[160:163]\n"
"ds_read_b64_tr_b16 a[224:225], v37 offset:33280\n"
"v_exp_f32 v134, v134\n"
"v_mfma_f32_16x16x32_bf16 a[164:167], v[60:63], v[188:191], a[164:167]\n"
"ds_read_b64_tr_b16 a[226:227], v37 offset:37376\n"
"v_exp_f32 v135, v135\n"
"v_mfma_f32_16x16x32_bf16 a[168:171], v[60:63], v[196:199], a[168:171]\n"
"v_cvt_pk_bf16_f32 v228, v132, v133\n"
"v_cvt_pk_bf16_f32 v229, v134, v135\n"
"v_mfma_f32_16x16x32_bf16 a[172:175], v[60:63], v[204:207], a[172:175]\n"
"v_exp_f32 v148, v148\n"
"v_mfma_f32_16x16x32_bf16 a[176:179], v[60:63], v[244:247], a[176:179]\n"
"s_waitcnt lgkmcnt(6)\n"
"v_exp_f32 v149, v149\n"
"v_mfma_f32_16x16x32_bf16 a[0:3], a[212:215], v[180:183], a[0:3]\n"
"ds_read_b64_tr_b16 a[204:205], v36 offset:33792\n"
"v_exp_f32 v150, v150\n"
"v_mfma_f32_16x16x32_bf16 a[32:35], a[212:215], v[188:191], a[32:35]\n"
"ds_read_b64_tr_b16 a[206:207], v36 offset:37888\n"
"v_exp_f32 v151, v151\n"
"v_mfma_f32_16x16x32_bf16 a[64:67], a[212:215], v[196:199], a[64:67]\n"
"v_cvt_pk_bf16_f32 v236, v148, v149\n"
"v_cvt_pk_bf16_f32 v237, v150, v151\n"
"v_mfma_f32_16x16x32_bf16 a[96:99], a[212:215], v[204:207], a[96:99]\n"
"v_exp_f32 v108, v108\n"
"v_mfma_f32_16x16x32_bf16 a[128:131], a[212:215], v[244:247], a[128:131]\n"
"s_waitcnt lgkmcnt(6)\n"
"v_exp_f32 v109, v109\n"
"v_mfma_f32_16x16x32_bf16 a[4:7], a[216:219], v[180:183], a[4:7]\n"
"ds_read_b64_tr_b16 a[208:209], v37 offset:33792\n"
"v_exp_f32 v110, v110\n"
"v_mfma_f32_16x16x32_bf16 a[36:39], a[216:219], v[188:191], a[36:39]\n"
"ds_read_b64_tr_b16 a[210:211], v37 offset:37888\n"
"v_exp_f32 v111, v111\n"
"v_mfma_f32_16x16x32_bf16 a[68:71], a[216:219], v[196:199], a[68:71]\n"
"v_cvt_pk_bf16_f32 v214, v108, v109\n"
"v_cvt_pk_bf16_f32 v215, v110, v111\n"
"v_mfma_f32_16x16x32_bf16 a[100:103], a[216:219], v[204:207], a[100:103]\n"
"v_exp_f32 v124, v124\n"
"v_mfma_f32_16x16x32_bf16 a[132:135], a[216:219], v[244:247], a[132:135]\n"
"s_waitcnt lgkmcnt(6)\n"
"v_exp_f32 v125, v125\n"
"v_mfma_f32_16x16x32_bf16 a[8:11], a[220:223], v[180:183], a[8:11]\n"
"ds_read_b64_tr_b16 a[212:213], v36 offset:34304\n"
"v_exp_f32 v126, v126\n"
"v_mfma_f32_16x16x32_bf16 a[40:43], a[220:223], v[188:191], a[40:43]\n"
"ds_read_b64_tr_b16 a[214:215], v36 offset:38400\n"
"v_exp_f32 v127, v127\n"
"v_mfma_f32_16x16x32_bf16 a[72:75], a[220:223], v[196:199], a[72:75]\n"
"v_cvt_pk_bf16_f32 v222, v124, v125\n"
"v_cvt_pk_bf16_f32 v223, v126, v127\n"
"v_mfma_f32_16x16x32_bf16 a[104:107], a[220:223], v[204:207], a[104:107]\n"
"v_exp_f32 v140, v140\n"
"v_mfma_f32_16x16x32_bf16 a[136:139], a[220:223], v[244:247], a[136:139]\n"
"s_waitcnt lgkmcnt(6)\n"
"v_exp_f32 v141, v141\n"
"v_mfma_f32_16x16x32_bf16 a[12:15], a[224:227], v[180:183], a[12:15]\n"
"ds_read_b64_tr_b16 a[216:217], v37 offset:34304\n"
"v_exp_f32 v142, v142\n"
"v_mfma_f32_16x16x32_bf16 a[44:47], a[224:227], v[188:191], a[44:47]\n"
"ds_read_b64_tr_b16 a[218:219], v37 offset:38400\n"
"v_exp_f32 v143, v143\n"
"v_mfma_f32_16x16x32_bf16 a[76:79], a[224:227], v[196:199], a[76:79]\n"
"v_cvt_pk_bf16_f32 v230, v140, v141\n"
"v_cvt_pk_bf16_f32 v231, v142, v143\n"
"v_mfma_f32_16x16x32_bf16 a[108:111], a[224:227], v[204:207], a[108:111]\n"
"v_exp_f32 v156, v156\n"
"v_mfma_f32_16x16x32_bf16 a[140:143], a[224:227], v[244:247], a[140:143]\n"
"s_waitcnt lgkmcnt(6)\n"
"v_exp_f32 v157, v157\n"
"v_mfma_f32_16x16x32_bf16 a[16:19], a[204:207], v[180:183], a[16:19]\n"
"ds_read_b128 a[180:183], v28 offset:49152\n"
"v_exp_f32 v158, v158\n"
"v_mfma_f32_16x16x32_bf16 a[48:51], a[204:207], v[188:191], a[48:51]\n"
"v_exp_f32 v159, v159\n"
"v_mfma_f32_16x16x32_bf16 a[80:83], a[204:207], v[196:199], a[80:83]\n"
"v_cvt_pk_bf16_f32 v238, v156, v157\n"
"v_cvt_pk_bf16_f32 v239, v158, v159\n"
"v_mfma_f32_16x16x32_bf16 a[112:115], a[204:207], v[204:207], a[112:115]\n"
"v_mfma_f32_16x16x32_bf16 a[144:147], a[204:207], v[244:247], a[144:147]\n"
"s_waitcnt lgkmcnt(5)\n"
"v_mfma_f32_16x16x32_bf16 a[20:23], a[208:211], v[180:183], a[20:23]\n"
"ds_read_b128 a[184:187], v29 offset:49152\n"
"v_mfma_f32_16x16x32_bf16 a[52:55], a[208:211], v[188:191], a[52:55]\n"
"v_mfma_f32_16x16x32_bf16 a[84:87], a[208:211], v[196:199], a[84:87]\n"
"v_mfma_f32_16x16x32_bf16 a[116:119], a[208:211], v[204:207], a[116:119]\n"
"v_mfma_f32_16x16x32_bf16 a[148:151], a[208:211], v[244:247], a[148:151]\n"
"s_waitcnt lgkmcnt(4)\n"
"v_mfma_f32_16x16x32_bf16 a[24:27], a[212:215], v[180:183], a[24:27]\n"
"ds_read_b128 a[188:191], v30 offset:49152\n"
"v_mfma_f32_16x16x32_bf16 a[56:59], a[212:215], v[188:191], a[56:59]\n"
"v_mfma_f32_16x16x32_bf16 a[88:91], a[212:215], v[196:199], a[88:91]\n"
"v_mfma_f32_16x16x32_bf16 a[120:123], a[212:215], v[204:207], a[120:123]\n"
"v_mfma_f32_16x16x32_bf16 a[152:155], a[212:215], v[244:247], a[152:155]\n"
"s_waitcnt lgkmcnt(3)\n"
"v_mfma_f32_16x16x32_bf16 a[28:31], a[216:219], v[180:183], a[28:31]\n"
"ds_read_b128 a[192:195], v31 offset:49152\n"
"v_mfma_f32_16x16x32_bf16 a[60:63], a[216:219], v[188:191], a[60:63]\n"
"v_mfma_f32_16x16x32_bf16 a[92:95], a[216:219], v[196:199], a[92:95]\n"
"v_mfma_f32_16x16x32_bf16 a[124:127], a[216:219], v[204:207], a[124:127]\n"
"v_mfma_f32_16x16x32_bf16 a[156:159], a[216:219], v[244:247], a[156:159]\n"
"s_waitcnt lgkmcnt(3)\n"
"v_mfma_f32_16x16x32_bf16 v[100:103], a[180:183], v[96:99], v[40:43]\n"
"ds_read_b128 a[196:199], v28 offset:53248\n"
"v_mfma_f32_16x16x32_bf16 v[116:119], a[180:183], v[128:131], v[44:47]\n"
"v_mfma_f32_16x16x32_bf16 v[132:135], a[180:183], v[160:163], v[48:51]\n"
"v_mfma_f32_16x16x32_bf16 v[148:151], a[180:183], v[192:195], v[52:55]\n"
"v_mfma_f32_16x16x32_bf16 v[164:167], a[180:183], v[224:227], v[56:59]\n"
"s_waitcnt lgkmcnt(3)\n"
"v_mfma_f32_16x16x32_bf16 v[100:103], a[184:187], v[104:107], v[100:103]\n"
"ds_read_b128 a[200:203], v29 offset:53248\n"
"v_mfma_f32_16x16x32_bf16 v[116:119], a[184:187], v[136:139], v[116:119]\n"
"v_mfma_f32_16x16x32_bf16 v[132:135], a[184:187], v[168:171], v[132:135]\n"
"v_mfma_f32_16x16x32_bf16 v[148:151], a[184:187], v[200:203], v[148:151]\n"
"v_mfma_f32_16x16x32_bf16 v[164:167], a[184:187], v[232:235], v[164:167]\n"
"s_waitcnt lgkmcnt(3)\n"
"v_mfma_f32_16x16x32_bf16 v[100:103], a[188:191], v[112:115], v[100:103]\n"
"ds_read_b128 a[180:183], v30 offset:53248\n"
"v_mfma_f32_16x16x32_bf16 v[116:119], a[188:191], v[144:147], v[116:119]\n"
"v_mfma_f32_16x16x32_bf16 v[132:135], a[188:191], v[176:179], v[132:135]\n"
"v_mfma_f32_16x16x32_bf16 v[148:151], a[188:191], v[208:211], v[148:151]\n"
"v_mfma_f32_16x16x32_bf16 v[164:167], a[188:191], v[240:243], v[164:167]\n"
"s_waitcnt lgkmcnt(3)\n"
"v_mfma_f32_16x16x32_bf16 v[100:103], a[192:195], v[120:123], v[100:103]\n"
"ds_read_b128 a[184:187], v31 offset:53248\n"
"v_mfma_f32_16x16x32_bf16 v[116:119], a[192:195], v[152:155], v[116:119]\n"
"v_mfma_f32_16x16x32_bf16 v[132:135], a[192:195], v[184:187], v[132:135]\n"
"v_mfma_f32_16x16x32_bf16 v[148:151], a[192:195], v[216:219], v[148:151]\n"
"v_mfma_f32_16x16x32_bf16 v[164:167], a[192:195], v[248:251], v[164:167]\n"
"s_waitcnt lgkmcnt(3)\n"
"v_mfma_f32_16x16x32_bf16 v[108:111], a[196:199], v[96:99], v[40:43]\n"
"v_mfma_f32_16x16x32_bf16 v[124:127], a[196:199], v[128:131], v[44:47]\n"
"v_mfma_f32_16x16x32_bf16 v[140:143], a[196:199], v[160:163], v[48:51]\n"
"v_mfma_f32_16x16x32_bf16 v[156:159], a[196:199], v[192:195], v[52:55]\n"
"v_exp_f32 v100, v100\n"
"v_mfma_f32_16x16x32_bf16 v[172:175], a[196:199], v[224:227], v[56:59]\n"
"s_waitcnt lgkmcnt(2)\n"
"v_exp_f32 v101, v101\n"
"v_mfma_f32_16x16x32_bf16 v[108:111], a[200:203], v[104:107], v[108:111]\n"
"v_exp_f32 v102, v102\n"
"v_mfma_f32_16x16x32_bf16 v[124:127], a[200:203], v[136:139], v[124:127]\n"
"v_exp_f32 v103, v103\n"
"v_mfma_f32_16x16x32_bf16 v[140:143], a[200:203], v[168:171], v[140:143]\n"
"ds_read_b64_tr_b16 a[222:223], v38 offset:36864\n"
"v_cvt_pk_bf16_f32 v180, v100, v101\n"
"v_cvt_pk_bf16_f32 v181, v102, v103\n"
"v_mfma_f32_16x16x32_bf16 v[156:159], a[200:203], v[200:203], v[156:159]\n"
"ds_read_b64_tr_b16 a[220:221], v38 offset:32768\n"
"v_exp_f32 v116, v116\n"
"v_mfma_f32_16x16x32_bf16 v[172:175], a[200:203], v[232:235], v[172:175]\n"
"s_waitcnt lgkmcnt(3)\n"
"v_exp_f32 v117, v117\n"
"v_mfma_f32_16x16x32_bf16 v[108:111], a[180:183], v[112:115], v[108:111]\n"
"v_exp_f32 v118, v118\n"
"v_mfma_f32_16x16x32_bf16 v[124:127], a[180:183], v[144:147], v[124:127]\n"
"v_exp_f32 v119, v119\n"
"v_mfma_f32_16x16x32_bf16 v[140:143], a[180:183], v[176:179], v[140:143]\n"
"ds_read_b64_tr_b16 a[224:225], v39 offset:32768\n"
"v_cvt_pk_bf16_f32 v188, v116, v117\n"
"v_cvt_pk_bf16_f32 v189, v118, v119\n"
"v_mfma_f32_16x16x32_bf16 v[156:159], a[180:183], v[208:211], v[156:159]\n"
"ds_read_b64_tr_b16 a[226:227], v39 offset:36864\n"
"v_exp_f32 v132, v132\n"
"v_mfma_f32_16x16x32_bf16 v[172:175], a[180:183], v[240:243], v[172:175]\n"
"s_waitcnt lgkmcnt(4)\n"
"v_exp_f32 v133, v133\n"
"v_mfma_f32_16x16x32_bf16 v[108:111], a[184:187], v[120:123], v[108:111]\n"
"v_exp_f32 v134, v134\n"
"v_mfma_f32_16x16x32_bf16 v[124:127], a[184:187], v[152:155], v[124:127]\n"
"ds_read_b64_tr_b16 a[204:205], v38 offset:33280\n"
"v_exp_f32 v135, v135\n"
"v_mfma_f32_16x16x32_bf16 v[140:143], a[184:187], v[184:187], v[140:143]\n"
"ds_read_b64_tr_b16 a[206:207], v38 offset:37376\n"
"v_cvt_pk_bf16_f32 v196, v132, v133\n"
"v_cvt_pk_bf16_f32 v197, v134, v135\n"
"v_mfma_f32_16x16x32_bf16 v[156:159], a[184:187], v[216:219], v[156:159]\n"
"v_exp_f32 v148, v148\n"
"v_mfma_f32_16x16x32_bf16 v[172:175], a[184:187], v[248:251], v[172:175]\n"
"v_exp_f32 v149, v149\n"
"v_mfma_f32_16x16x32_bf16 a[160:163], v[60:63], v[212:215], a[160:163]\n"
"ds_read_b64_tr_b16 a[208:209], v39 offset:33280\n"
"v_exp_f32 v150, v150\n"
"v_mfma_f32_16x16x32_bf16 a[164:167], v[60:63], v[220:223], a[164:167]\n"
"ds_read_b64_tr_b16 a[210:211], v39 offset:37376\n"
"v_exp_f32 v151, v151\n"
"v_mfma_f32_16x16x32_bf16 a[168:171], v[60:63], v[228:231], a[168:171]\n"
"v_cvt_pk_bf16_f32 v204, v148, v149\n"
"v_cvt_pk_bf16_f32 v205, v150, v151\n"
"v_mfma_f32_16x16x32_bf16 a[172:175], v[60:63], v[236:239], a[172:175]\n"
"s_waitcnt lgkmcnt(6)\n"
"v_exp_f32 v164, v164\n"
"v_mfma_f32_16x16x32_bf16 a[0:3], a[220:223], v[212:215], a[0:3]\n"
"ds_read_b64_tr_b16 a[212:213], v38 offset:33792\n"
"v_exp_f32 v165, v165\n"
"v_mfma_f32_16x16x32_bf16 a[32:35], a[220:223], v[220:223], a[32:35]\n"
"ds_read_b64_tr_b16 a[214:215], v38 offset:37888\n"
"v_exp_f32 v166, v166\n"
"v_mfma_f32_16x16x32_bf16 a[64:67], a[220:223], v[228:231], a[64:67]\n"
"v_exp_f32 v167, v167\n"
"v_mfma_f32_16x16x32_bf16 a[96:99], a[220:223], v[236:239], a[96:99]\n"
"s_waitcnt lgkmcnt(6)\n"
"v_cvt_pk_bf16_f32 v244, v164, v165\n"
"v_cvt_pk_bf16_f32 v245, v166, v167\n"
"v_mfma_f32_16x16x32_bf16 a[4:7], a[224:227], v[212:215], a[4:7]\n"
"ds_read_b64_tr_b16 a[216:217], v39 offset:33792\n"
"v_exp_f32 v108, v108\n"
"v_mfma_f32_16x16x32_bf16 a[36:39], a[224:227], v[220:223], a[36:39]\n"
"ds_read_b64_tr_b16 a[218:219], v39 offset:37888\n"
"v_exp_f32 v109, v109\n"
"v_mfma_f32_16x16x32_bf16 a[68:71], a[224:227], v[228:231], a[68:71]\n"
"v_exp_f32 v110, v110\n"
"v_mfma_f32_16x16x32_bf16 a[100:103], a[224:227], v[236:239], a[100:103]\n"
"s_waitcnt lgkmcnt(6)\n"
"v_exp_f32 v111, v111\n"
"v_mfma_f32_16x16x32_bf16 a[8:11], a[204:207], v[212:215], a[8:11]\n"
"ds_read_b64_tr_b16 a[220:221], v38 offset:34304\n"
"v_cvt_pk_bf16_f32 v182, v108, v109\n"
"v_cvt_pk_bf16_f32 v183, v110, v111\n"
"v_mfma_f32_16x16x32_bf16 a[40:43], a[204:207], v[220:223], a[40:43]\n"
"ds_read_b64_tr_b16 a[222:223], v38 offset:38400\n"
"v_exp_f32 v124, v124\n"
"v_mfma_f32_16x16x32_bf16 a[72:75], a[204:207], v[228:231], a[72:75]\n"
"v_exp_f32 v125, v125\n"
"v_mfma_f32_16x16x32_bf16 a[104:107], a[204:207], v[236:239], a[104:107]\n"
"s_waitcnt lgkmcnt(6)\n"
"v_exp_f32 v126, v126\n"
"v_mfma_f32_16x16x32_bf16 a[12:15], a[208:211], v[212:215], a[12:15]\n"
"ds_read_b64_tr_b16 a[224:225], v39 offset:34304\n"
"v_exp_f32 v127, v127\n"
"v_mfma_f32_16x16x32_bf16 a[44:47], a[208:211], v[220:223], a[44:47]\n"
"ds_read_b64_tr_b16 a[226:227], v39 offset:38400\n"
"v_cvt_pk_bf16_f32 v190, v124, v125\n"
"v_cvt_pk_bf16_f32 v191, v126, v127\n"
"v_mfma_f32_16x16x32_bf16 a[76:79], a[208:211], v[228:231], a[76:79]\n"
"v_exp_f32 v140, v140\n"
"v_mfma_f32_16x16x32_bf16 a[108:111], a[208:211], v[236:239], a[108:111]\n"
"s_waitcnt lgkmcnt(6)\n"
"v_exp_f32 v141, v141\n"
"v_mfma_f32_16x16x32_bf16 a[16:19], a[212:215], v[212:215], a[16:19]\n"
"ds_read_b128 a[188:191], v32 offset:49152\n"
"v_exp_f32 v142, v142\n"
"v_mfma_f32_16x16x32_bf16 a[48:51], a[212:215], v[220:223], a[48:51]\n"
"v_exp_f32 v143, v143\n"
"v_mfma_f32_16x16x32_bf16 a[80:83], a[212:215], v[228:231], a[80:83]\n"
"v_cvt_pk_bf16_f32 v198, v140, v141\n"
"v_cvt_pk_bf16_f32 v199, v142, v143\n"
"v_mfma_f32_16x16x32_bf16 a[112:115], a[212:215], v[236:239], a[112:115]\n"
"s_waitcnt lgkmcnt(5)\n"
"v_exp_f32 v156, v156\n"
"v_mfma_f32_16x16x32_bf16 a[20:23], a[216:219], v[212:215], a[20:23]\n"
"ds_read_b128 a[192:195], v33 offset:49152\n"
"v_exp_f32 v157, v157\n"
"v_mfma_f32_16x16x32_bf16 a[52:55], a[216:219], v[220:223], a[52:55]\n"
"v_exp_f32 v158, v158\n"
"v_mfma_f32_16x16x32_bf16 a[84:87], a[216:219], v[228:231], a[84:87]\n"
"v_exp_f32 v159, v159\n"
"v_mfma_f32_16x16x32_bf16 a[116:119], a[216:219], v[236:239], a[116:119]\n"
"s_waitcnt lgkmcnt(4)\n"
"v_cvt_pk_bf16_f32 v206, v156, v157\n"
"v_cvt_pk_bf16_f32 v207, v158, v159\n"
"v_mfma_f32_16x16x32_bf16 a[24:27], a[220:223], v[212:215], a[24:27]\n"
"ds_read_b128 a[196:199], v34 offset:49152\n"
"v_exp_f32 v172, v172\n"
"v_mfma_f32_16x16x32_bf16 a[56:59], a[220:223], v[220:223], a[56:59]\n"
"v_exp_f32 v173, v173\n"
"v_mfma_f32_16x16x32_bf16 a[88:91], a[220:223], v[228:231], a[88:91]\n"
"v_exp_f32 v174, v174\n"
"v_mfma_f32_16x16x32_bf16 a[120:123], a[220:223], v[236:239], a[120:123]\n"
"s_waitcnt lgkmcnt(3)\n"
"v_exp_f32 v175, v175\n"
"v_mfma_f32_16x16x32_bf16 a[28:31], a[224:227], v[212:215], a[28:31]\n"
"ds_read_b128 a[200:203], v35 offset:49152\n"
"v_cvt_pk_bf16_f32 v246, v172, v173\n"
"v_cvt_pk_bf16_f32 v247, v174, v175\n"
"v_mfma_f32_16x16x32_bf16 a[60:63], a[224:227], v[220:223], a[60:63]\n"
"v_mfma_f32_16x16x32_bf16 a[92:95], a[224:227], v[228:231], a[92:95]\n"
"v_mfma_f32_16x16x32_bf16 a[124:127], a[224:227], v[236:239], a[124:127]\n"
"s_waitcnt lgkmcnt(3)\n"
"v_mfma_f32_16x16x32_bf16 v[100:103], a[188:191], v[96:99], v[40:43]\n"
"ds_read_b128 a[180:183], v32 offset:53248\n"
"v_mfma_f32_16x16x32_bf16 v[116:119], a[188:191], v[128:131], v[44:47]\n"
"v_mfma_f32_16x16x32_bf16 v[132:135], a[188:191], v[160:163], v[48:51]\n"
"v_mfma_f32_16x16x32_bf16 v[148:151], a[188:191], v[192:195], v[52:55]\n"
"s_waitcnt lgkmcnt(3)\n"
"v_mfma_f32_16x16x32_bf16 v[100:103], a[192:195], v[104:107], v[100:103]\n"
"ds_read_b128 a[184:187], v33 offset:53248\n"
"v_mfma_f32_16x16x32_bf16 v[116:119], a[192:195], v[136:139], v[116:119]\n"
"v_mfma_f32_16x16x32_bf16 v[132:135], a[192:195], v[168:171], v[132:135]\n"
"v_mfma_f32_16x16x32_bf16 v[148:151], a[192:195], v[200:203], v[148:151]\n"
"s_waitcnt lgkmcnt(3)\n"
"v_mfma_f32_16x16x32_bf16 v[100:103], a[196:199], v[112:115], v[100:103]\n"
"ds_read_b128 a[188:191], v34 offset:53248\n"
"v_mfma_f32_16x16x32_bf16 v[116:119], a[196:199], v[144:147], v[116:119]\n"
"v_mfma_f32_16x16x32_bf16 v[132:135], a[196:199], v[176:179], v[132:135]\n"
"v_mfma_f32_16x16x32_bf16 v[148:151], a[196:199], v[208:211], v[148:151]\n"
"s_waitcnt lgkmcnt(3)\n"
"v_mfma_f32_16x16x32_bf16 v[100:103], a[200:203], v[120:123], v[100:103]\n"
"ds_read_b128 a[192:195], v35 offset:53248\n"
"v_mfma_f32_16x16x32_bf16 v[116:119], a[200:203], v[152:155], v[116:119]\n"
"v_mfma_f32_16x16x32_bf16 v[132:135], a[200:203], v[184:187], v[132:135]\n"
"v_mfma_f32_16x16x32_bf16 v[148:151], a[200:203], v[216:219], v[148:151]\n"
"s_waitcnt lgkmcnt(3)\n"
"v_mfma_f32_16x16x32_bf16 v[108:111], a[180:183], v[96:99], v[40:43]\n"
"v_mfma_f32_16x16x32_bf16 v[124:127], a[180:183], v[128:131], v[44:47]\n"
"ds_read_b64_tr_b16 a[204:205], v36 offset:49152\n"
"v_mfma_f32_16x16x32_bf16 v[140:143], a[180:183], v[160:163], v[48:51]\n"
"ds_read_b64_tr_b16 a[206:207], v36 offset:53248\n"
"v_mfma_f32_16x16x32_bf16 v[156:159], a[180:183], v[192:195], v[52:55]\n"
"s_waitcnt lgkmcnt(4)\n"
"v_mfma_f32_16x16x32_bf16 v[108:111], a[184:187], v[104:107], v[108:111]\n"
"v_exp_f32 v100, v100\n"
"v_mfma_f32_16x16x32_bf16 v[124:127], a[184:187], v[136:139], v[124:127]\n"
"ds_read_b64_tr_b16 a[210:211], v37 offset:53248\n"
"v_exp_f32 v101, v101\n"
"v_mfma_f32_16x16x32_bf16 v[140:143], a[184:187], v[168:171], v[140:143]\n"
"ds_read_b64_tr_b16 a[208:209], v37 offset:49152\n"
"v_exp_f32 v102, v102\n"
"v_mfma_f32_16x16x32_bf16 v[156:159], a[184:187], v[200:203], v[156:159]\n"
"s_waitcnt lgkmcnt(5)\n"
"v_exp_f32 v103, v103\n"
"v_mfma_f32_16x16x32_bf16 v[108:111], a[188:191], v[112:115], v[108:111]\n"
"v_cvt_pk_bf16_f32 v212, v100, v101\n"
"v_cvt_pk_bf16_f32 v213, v102, v103\n"
"v_mfma_f32_16x16x32_bf16 v[124:127], a[188:191], v[144:147], v[124:127]\n"
"v_exp_f32 v116, v116\n"
"v_mfma_f32_16x16x32_bf16 v[140:143], a[188:191], v[176:179], v[140:143]\n"
"ds_read_b64_tr_b16 a[212:213], v36 offset:49664\n"
"v_exp_f32 v117, v117\n"
"v_mfma_f32_16x16x32_bf16 v[156:159], a[188:191], v[208:211], v[156:159]\n"
"s_waitcnt lgkmcnt(5)\n"
"v_exp_f32 v118, v118\n"
"v_mfma_f32_16x16x32_bf16 v[108:111], a[192:195], v[120:123], v[108:111]\n"
"ds_read_b64_tr_b16 a[214:215], v36 offset:53760\n"
"v_exp_f32 v119, v119\n"
"v_mfma_f32_16x16x32_bf16 v[124:127], a[192:195], v[152:155], v[124:127]\n"
"v_cvt_pk_bf16_f32 v220, v116, v117\n"
"v_cvt_pk_bf16_f32 v221, v118, v119\n"
"v_mfma_f32_16x16x32_bf16 v[140:143], a[192:195], v[184:187], v[140:143]\n"
"v_exp_f32 v132, v132\n"
"v_mfma_f32_16x16x32_bf16 v[156:159], a[192:195], v[216:219], v[156:159]\n"
"s_barrier\n"
"v_exp_f32 v133, v133\n"
"v_mfma_f32_16x16x32_bf16 a[160:163], v[60:63], v[180:183], a[160:163]\n"
"ds_read_b64_tr_b16 a[216:217], v37 offset:49664\n"
"v_exp_f32 v134, v134\n"
"v_mfma_f32_16x16x32_bf16 a[164:167], v[60:63], v[188:191], a[164:167]\n"
"ds_read_b64_tr_b16 a[218:219], v37 offset:53760\n"
"v_exp_f32 v135, v135\n"
"v_mfma_f32_16x16x32_bf16 a[168:171], v[60:63], v[196:199], a[168:171]\n"
"v_cvt_pk_bf16_f32 v228, v132, v133\n"
"v_cvt_pk_bf16_f32 v229, v134, v135\n"
"v_mfma_f32_16x16x32_bf16 a[172:175], v[60:63], v[204:207], a[172:175]\n"
"v_exp_f32 v148, v148\n"
"v_mfma_f32_16x16x32_bf16 a[176:179], v[60:63], v[244:247], a[176:179]\n"
"s_waitcnt lgkmcnt(6)\n"
"v_exp_f32 v149, v149\n"
"v_mfma_f32_16x16x32_bf16 a[0:3], a[204:207], v[180:183], a[0:3]\n"
"ds_read_b64_tr_b16 a[220:221], v36 offset:50176\n"
"v_exp_f32 v150, v150\n"
"v_mfma_f32_16x16x32_bf16 a[32:35], a[204:207], v[188:191], a[32:35]\n"
"ds_read_b64_tr_b16 a[222:223], v36 offset:54272\n"
"v_exp_f32 v151, v151\n"
"v_mfma_f32_16x16x32_bf16 a[64:67], a[204:207], v[196:199], a[64:67]\n"
"v_cvt_pk_bf16_f32 v236, v148, v149\n"
"v_cvt_pk_bf16_f32 v237, v150, v151\n"
"v_mfma_f32_16x16x32_bf16 a[96:99], a[204:207], v[204:207], a[96:99]\n"
"v_exp_f32 v108, v108\n"
"v_mfma_f32_16x16x32_bf16 a[128:131], a[204:207], v[244:247], a[128:131]\n"
"s_waitcnt lgkmcnt(6)\n"
"v_exp_f32 v109, v109\n"
"v_mfma_f32_16x16x32_bf16 a[4:7], a[208:211], v[180:183], a[4:7]\n"
"ds_read_b64_tr_b16 a[224:225], v37 offset:50176\n"
"v_exp_f32 v110, v110\n"
"v_mfma_f32_16x16x32_bf16 a[36:39], a[208:211], v[188:191], a[36:39]\n"
"ds_read_b64_tr_b16 a[226:227], v37 offset:54272\n"
"v_exp_f32 v111, v111\n"
"v_mfma_f32_16x16x32_bf16 a[68:71], a[208:211], v[196:199], a[68:71]\n"
"v_cvt_pk_bf16_f32 v214, v108, v109\n"
"v_cvt_pk_bf16_f32 v215, v110, v111\n"
"v_mfma_f32_16x16x32_bf16 a[100:103], a[208:211], v[204:207], a[100:103]\n"
"v_exp_f32 v124, v124\n"
"v_mfma_f32_16x16x32_bf16 a[132:135], a[208:211], v[244:247], a[132:135]\n"
"s_waitcnt lgkmcnt(6)\n"
"v_exp_f32 v125, v125\n"
"v_mfma_f32_16x16x32_bf16 a[8:11], a[212:215], v[180:183], a[8:11]\n"
"ds_read_b64_tr_b16 a[204:205], v36 offset:50688\n"
"v_exp_f32 v126, v126\n"
"v_mfma_f32_16x16x32_bf16 a[40:43], a[212:215], v[188:191], a[40:43]\n"
"ds_read_b64_tr_b16 a[206:207], v36 offset:54784\n"
"v_exp_f32 v127, v127\n"
"v_mfma_f32_16x16x32_bf16 a[72:75], a[212:215], v[196:199], a[72:75]\n"
"v_cvt_pk_bf16_f32 v222, v124, v125\n"
"v_cvt_pk_bf16_f32 v223, v126, v127\n"
"v_mfma_f32_16x16x32_bf16 a[104:107], a[212:215], v[204:207], a[104:107]\n"
"v_exp_f32 v140, v140\n"
"v_mfma_f32_16x16x32_bf16 a[136:139], a[212:215], v[244:247], a[136:139]\n"
"s_waitcnt lgkmcnt(6)\n"
"v_exp_f32 v141, v141\n"
"v_mfma_f32_16x16x32_bf16 a[12:15], a[216:219], v[180:183], a[12:15]\n"
"ds_read_b64_tr_b16 a[208:209], v37 offset:50688\n"
"v_exp_f32 v142, v142\n"
"v_mfma_f32_16x16x32_bf16 a[44:47], a[216:219], v[188:191], a[44:47]\n"
"ds_read_b64_tr_b16 a[210:211], v37 offset:54784\n"
"v_exp_f32 v143, v143\n"
"v_mfma_f32_16x16x32_bf16 a[76:79], a[216:219], v[196:199], a[76:79]\n"
"v_cvt_pk_bf16_f32 v230, v140, v141\n"
"v_cvt_pk_bf16_f32 v231, v142, v143\n"
"v_mfma_f32_16x16x32_bf16 a[108:111], a[216:219], v[204:207], a[108:111]\n"
"v_exp_f32 v156, v156\n"
"v_mfma_f32_16x16x32_bf16 a[140:143], a[216:219], v[244:247], a[140:143]\n"
"s_waitcnt lgkmcnt(6)\n"
"v_exp_f32 v157, v157\n"
"v_mfma_f32_16x16x32_bf16 a[16:19], a[220:223], v[180:183], a[16:19]\n"
"v_exp_f32 v158, v158\n"
"v_mfma_f32_16x16x32_bf16 a[48:51], a[220:223], v[188:191], a[48:51]\n"
"v_exp_f32 v159, v159\n"
"v_mfma_f32_16x16x32_bf16 a[80:83], a[220:223], v[196:199], a[80:83]\n"
"v_cvt_pk_bf16_f32 v238, v156, v157\n"
"v_cvt_pk_bf16_f32 v239, v158, v159\n"
"v_mfma_f32_16x16x32_bf16 a[112:115], a[220:223], v[204:207], a[112:115]\n"
"v_mfma_f32_16x16x32_bf16 a[144:147], a[220:223], v[244:247], a[144:147]\n"
"s_waitcnt lgkmcnt(4)\n"
"v_mfma_f32_16x16x32_bf16 a[20:23], a[224:227], v[180:183], a[20:23]\n"
"v_mfma_f32_16x16x32_bf16 a[52:55], a[224:227], v[188:191], a[52:55]\n"
"v_mfma_f32_16x16x32_bf16 a[84:87], a[224:227], v[196:199], a[84:87]\n"
"ds_read_b64_tr_b16 a[214:215], v38 offset:53248\n"
"v_mfma_f32_16x16x32_bf16 a[116:119], a[224:227], v[204:207], a[116:119]\n"
"ds_read_b64_tr_b16 a[212:213], v38 offset:49152\n"
"v_mfma_f32_16x16x32_bf16 a[148:151], a[224:227], v[244:247], a[148:151]\n"
"s_waitcnt lgkmcnt(4)\n"
"v_mfma_f32_16x16x32_bf16 a[24:27], a[204:207], v[180:183], a[24:27]\n"
"v_mfma_f32_16x16x32_bf16 a[56:59], a[204:207], v[188:191], a[56:59]\n"
"v_mfma_f32_16x16x32_bf16 a[88:91], a[204:207], v[196:199], a[88:91]\n"
"ds_read_b64_tr_b16 a[216:217], v39 offset:49152\n"
"v_mfma_f32_16x16x32_bf16 a[120:123], a[204:207], v[204:207], a[120:123]\n"
"ds_read_b64_tr_b16 a[218:219], v39 offset:53248\n"
"v_mfma_f32_16x16x32_bf16 a[152:155], a[204:207], v[244:247], a[152:155]\n"
"s_waitcnt lgkmcnt(4)\n"
"v_mfma_f32_16x16x32_bf16 a[28:31], a[208:211], v[180:183], a[28:31]\n"
"v_mfma_f32_16x16x32_bf16 a[60:63], a[208:211], v[188:191], a[60:63]\n"
"ds_read_b64_tr_b16 a[220:221], v38 offset:49664\n"
"v_mfma_f32_16x16x32_bf16 a[92:95], a[208:211], v[196:199], a[92:95]\n"
"ds_read_b64_tr_b16 a[222:223], v38 offset:53760\n"
"v_mfma_f32_16x16x32_bf16 a[124:127], a[208:211], v[204:207], a[124:127]\n"
"v_mfma_f32_16x16x32_bf16 a[156:159], a[208:211], v[244:247], a[156:159]\n"
"v_mfma_f32_16x16x32_bf16 a[160:163], v[60:63], v[212:215], a[160:163]\n"
"ds_read_b64_tr_b16 a[224:225], v39 offset:49664\n"
"v_mfma_f32_16x16x32_bf16 a[164:167], v[60:63], v[220:223], a[164:167]\n"
"ds_read_b64_tr_b16 a[226:227], v39 offset:53760\n"
"v_mfma_f32_16x16x32_bf16 a[168:171], v[60:63], v[228:231], a[168:171]\n"
"v_mfma_f32_16x16x32_bf16 a[172:175], v[60:63], v[236:239], a[172:175]\n"
"s_waitcnt lgkmcnt(6)\n"
"v_mfma_f32_16x16x32_bf16 a[0:3], a[212:215], v[212:215], a[0:3]\n"
"ds_read_b64_tr_b16 a[204:205], v38 offset:50176\n"
"v_mfma_f32_16x16x32_bf16 a[32:35], a[212:215], v[220:223], a[32:35]\n"
"ds_read_b64_tr_b16 a[206:207], v38 offset:54272\n"
"v_mfma_f32_16x16x32_bf16 a[64:67], a[212:215], v[228:231], a[64:67]\n"
"v_mfma_f32_16x16x32_bf16 a[96:99], a[212:215], v[236:239], a[96:99]\n"
"s_waitcnt lgkmcnt(6)\n"
"v_mfma_f32_16x16x32_bf16 a[4:7], a[216:219], v[212:215], a[4:7]\n"
"ds_read_b64_tr_b16 a[208:209], v39 offset:50176\n"
"v_mfma_f32_16x16x32_bf16 a[36:39], a[216:219], v[220:223], a[36:39]\n"
"ds_read_b64_tr_b16 a[210:211], v39 offset:54272\n"
"v_mfma_f32_16x16x32_bf16 a[68:71], a[216:219], v[228:231], a[68:71]\n"
"v_mfma_f32_16x16x32_bf16 a[100:103], a[216:219], v[236:239], a[100:103]\n"
"s_waitcnt lgkmcnt(6)\n"
"v_mfma_f32_16x16x32_bf16 a[8:11], a[220:223], v[212:215], a[8:11]\n"
"ds_read_b64_tr_b16 a[212:213], v38 offset:50688\n"
"v_mfma_f32_16x16x32_bf16 a[40:43], a[220:223], v[220:223], a[40:43]\n"
"ds_read_b64_tr_b16 a[214:215], v38 offset:54784\n"
"v_mfma_f32_16x16x32_bf16 a[72:75], a[220:223], v[228:231], a[72:75]\n"
"v_mfma_f32_16x16x32_bf16 a[104:107], a[220:223], v[236:239], a[104:107]\n"
"s_waitcnt lgkmcnt(6)\n"
"v_mfma_f32_16x16x32_bf16 a[12:15], a[224:227], v[212:215], a[12:15]\n"
"ds_read_b64_tr_b16 a[216:217], v39 offset:50688\n"
"v_mfma_f32_16x16x32_bf16 a[44:47], a[224:227], v[220:223], a[44:47]\n"
"ds_read_b64_tr_b16 a[218:219], v39 offset:54784\n"
"v_mfma_f32_16x16x32_bf16 a[76:79], a[224:227], v[228:231], a[76:79]\n"
"v_mfma_f32_16x16x32_bf16 a[108:111], a[224:227], v[236:239], a[108:111]\n"
"s_waitcnt lgkmcnt(6)\n"
"v_mfma_f32_16x16x32_bf16 a[16:19], a[204:207], v[212:215], a[16:19]\n"
"v_mfma_f32_16x16x32_bf16 a[48:51], a[204:207], v[220:223], a[48:51]\n"
"v_mfma_f32_16x16x32_bf16 a[80:83], a[204:207], v[228:231], a[80:83]\n"
"v_mfma_f32_16x16x32_bf16 a[112:115], a[204:207], v[236:239], a[112:115]\n"
"s_waitcnt lgkmcnt(4)\n"
"v_mfma_f32_16x16x32_bf16 a[20:23], a[208:211], v[212:215], a[20:23]\n"
"v_mfma_f32_16x16x32_bf16 a[52:55], a[208:211], v[220:223], a[52:55]\n"
"v_mfma_f32_16x16x32_bf16 a[84:87], a[208:211], v[228:231], a[84:87]\n"
"v_mfma_f32_16x16x32_bf16 a[116:119], a[208:211], v[236:239], a[116:119]\n"
"s_waitcnt lgkmcnt(2)\n"
"v_mfma_f32_16x16x32_bf16 a[24:27], a[212:215], v[212:215], a[24:27]\n"
"v_mfma_f32_16x16x32_bf16 a[56:59], a[212:215], v[220:223], a[56:59]\n"
"v_mfma_f32_16x16x32_bf16 a[88:91], a[212:215], v[228:231], a[88:91]\n"
"v_mfma_f32_16x16x32_bf16 a[120:123], a[212:215], v[236:239], a[120:123]\n"
"s_waitcnt lgkmcnt(0)\n"
"v_mfma_f32_16x16x32_bf16 a[28:31], a[216:219], v[212:215], a[28:31]\n"
"v_mfma_f32_16x16x32_bf16 a[60:63], a[216:219], v[220:223], a[60:63]\n"
"v_mfma_f32_16x16x32_bf16 a[92:95], a[216:219], v[228:231], a[92:95]\n"
"v_mfma_f32_16x16x32_bf16 a[124:127], a[216:219], v[236:239], a[124:127]\n"
"v_mov_b32 v95, s78\n"
"s_lshl_b32 s79, s71, 14\n"
"s_add_u32 s79, s79, s77\n"
"s_nop 7\n"
"v_accvgpr_read_b32 v64, a160\n"
"v_cmp_nle_f32 vcc, v64, v95\n"
"v_rcp_f32 v65, v64\n"
"s_or_b64 s[74:75], s[74:75], vcc\n"
"s_add_u32 s41, s79, 0\n"
"v_accvgpr_read_b32 v68, a0\n"
"v_accvgpr_read_b32 v69, a1\n"
"v_accvgpr_read_b32 v70, a2\n"
"v_accvgpr_read_b32 v71, a3\n"
"v_accvgpr_read_b32 v72, a4\n"
"v_accvgpr_read_b32 v73, a5\n"
"v_accvgpr_read_b32 v74, a6\n"
"v_accvgpr_read_b32 v75, a7\n"
"v_mul_f32 v68, v68, v65\n"
"v_mul_f32 v69, v69, v65\n"
"v_mul_f32 v70, v70, v65\n"
"v_mul_f32 v71, v71, v65\n"
"v_mul_f32 v72, v72, v65\n"
"v_mul_f32 v73, v73, v65\n"
"v_mul_f32 v74, v74, v65\n"
"v_mul_f32 v75, v75, v65\n"
"v_cvt_pk_bf16_f32 v76, v68, v69\n"
"v_cvt_pk_bf16_f32 v77, v70, v71\n"
"v_cvt_pk_bf16_f32 v78, v72, v73\n"
"v_cvt_pk_bf16_f32 v79, v74, v75\n"
"buffer_store_dwordx4 v[76:79], %16, s[56:59], s41 offen sc1\n"
"v_accvgpr_read_b32 v80, a8\n"
"v_accvgpr_read_b32 v81, a9\n"
"v_accvgpr_read_b32 v82, a10\n"
"v_accvgpr_read_b32 v83, a11\n"
"v_accvgpr_read_b32 v84, a12\n"
"v_accvgpr_read_b32 v85, a13\n"
"v_accvgpr_read_b32 v86, a14\n"
"v_accvgpr_read_b32 v87, a15\n"
"v_mul_f32 v80, v80, v65\n"
"v_mul_f32 v81, v81, v65\n"
"v_mul_f32 v82, v82, v65\n"
"v_mul_f32 v83, v83, v65\n"
"v_mul_f32 v84, v84, v65\n"
"v_mul_f32 v85, v85, v65\n"
"v_mul_f32 v86, v86, v65\n"
"v_mul_f32 v87, v87, v65\n"
"v_cvt_pk_bf16_f32 v88, v80, v81\n"
"v_cvt_pk_bf16_f32 v89, v82, v83\n"
"v_cvt_pk_bf16_f32 v90, v84, v85\n"
"v_cvt_pk_bf16_f32 v91, v86, v87\n"
"buffer_store_dwordx4 v[88:91], %16, s[56:59], s41 offen offset:1024 sc1\n"
"v_accvgpr_read_b32 v68, a16\n"
"v_accvgpr_read_b32 v69, a17\n"
"v_accvgpr_read_b32 v70, a18\n"
"v_accvgpr_read_b32 v71, a19\n"
"v_accvgpr_read_b32 v72, a20\n"
"v_accvgpr_read_b32 v73, a21\n"
"v_accvgpr_read_b32 v74, a22\n"
"v_accvgpr_read_b32 v75, a23\n"
"v_mul_f32 v68, v68, v65\n"
"v_mul_f32 v69, v69, v65\n"
"v_mul_f32 v70, v70, v65\n"
"v_mul_f32 v71, v71, v65\n"
"v_mul_f32 v72, v72, v65\n"
"v_mul_f32 v73, v73, v65\n"
"v_mul_f32 v74, v74, v65\n"
"v_mul_f32 v75, v75, v65\n"
"v_cvt_pk_bf16_f32 v76, v68, v69\n"
"v_cvt_pk_bf16_f32 v77, v70, v71\n"
"v_cvt_pk_bf16_f32 v78, v72, v73\n"
"v_cvt_pk_bf16_f32 v79, v74, v75\n"
"buffer_store_dwordx4 v[76:79], %16, s[56:59], s41 offen offset:2048 sc1\n"
"v_accvgpr_read_b32 v80, a24\n"
"v_accvgpr_read_b32 v81, a25\n"
"v_accvgpr_read_b32 v82, a26\n"
"v_accvgpr_read_b32 v83, a27\n"
"v_accvgpr_read_b32 v84, a28\n"
"v_accvgpr_read_b32 v85, a29\n"
"v_accvgpr_read_b32 v86, a30\n"
"v_accvgpr_read_b32 v87, a31\n"
"v_mul_f32 v80, v80, v65\n"
"v_mul_f32 v81, v81, v65\n"
"v_mul_f32 v82, v82, v65\n"
"v_mul_f32 v83, v83, v65\n"
"v_mul_f32 v84, v84, v65\n"
"v_mul_f32 v85, v85, v65\n"
"v_mul_f32 v86, v86, v65\n"
"v_mul_f32 v87, v87, v65\n"
"v_cvt_pk_bf16_f32 v88, v80, v81\n"
"v_cvt_pk_bf16_f32 v89, v82, v83\n"
"v_cvt_pk_bf16_f32 v90, v84, v85\n"
"v_cvt_pk_bf16_f32 v91, v86, v87\n"
"buffer_store_dwordx4 v[88:91], %16, s[56:59], s41 offen offset:3072 sc1\n"
"v_accvgpr_read_b32 v64, a164\n"
"v_cmp_nle_f32 vcc, v64, v95\n"
"v_rcp_f32 v65, v64\n"
"s_or_b64 s[74:75], s[74:75], vcc\n"
"s_add_u32 s42, s79, 4096\n"
"v_accvgpr_read_b32 v68, a32\n"
"v_accvgpr_read_b32 v69, a33\n"
"v_accvgpr_read_b32 v70, a34\n"
"v_accvgpr_read_b32 v71, a35\n"
"v_accvgpr_read_b32 v72, a36\n"
"v_accvgpr_read_b32 v73, a37\n"
"v_accvgpr_read_b32 v74, a38\n"
"v_accvgpr_read_b32 v75, a39\n"
"v_mul_f32 v68, v68, v65\n"
"v_mul_f32 v69, v69, v65\n"
"v_mul_f32 v70, v70, v65\n"
"v_mul_f32 v71, v71, v65\n"
"v_mul_f32 v72, v72, v65\n"
"v_mul_f32 v73, v73, v65\n"
"v_mul_f32 v74, v74, v65\n"
"v_mul_f32 v75, v75, v65\n"
"v_cvt_pk_bf16_f32 v76, v68, v69\n"
"v_cvt_pk_bf16_f32 v77, v70, v71\n"
"v_cvt_pk_bf16_f32 v78, v72, v73\n"
"v_cvt_pk_bf16_f32 v79, v74, v75\n"
"buffer_store_dwordx4 v[76:79], %16, s[56:59], s42 offen sc1\n"
"v_accvgpr_read_b32 v80, a40\n"
"v_accvgpr_read_b32 v81, a41\n"
"v_accvgpr_read_b32 v82, a42\n"
"v_accvgpr_read_b32 v83, a43\n"
"v_accvgpr_read_b32 v84, a44\n"
"v_accvgpr_read_b32 v85, a45\n"
"v_accvgpr_read_b32 v86, a46\n"
"v_accvgpr_read_b32 v87, a47\n"
"v_mul_f32 v80, v80, v65\n"
"v_mul_f32 v81, v81, v65\n"
"v_mul_f32 v82, v82, v65\n"
"v_mul_f32 v83, v83, v65\n"
"v_mul_f32 v84, v84, v65\n"
"v_mul_f32 v85, v85, v65\n"
"v_mul_f32 v86, v86, v65\n"
"v_mul_f32 v87, v87, v65\n"
"v_cvt_pk_bf16_f32 v88, v80, v81\n"
"v_cvt_pk_bf16_f32 v89, v82, v83\n"
"v_cvt_pk_bf16_f32 v90, v84, v85\n"
"v_cvt_pk_bf16_f32 v91, v86, v87\n"
"buffer_store_dwordx4 v[88:91], %16, s[56:59], s42 offen offset:1024 sc1\n"
"v_accvgpr_read_b32 v68, a48\n"
"v_accvgpr_read_b32 v69, a49\n"
"v_accvgpr_read_b32 v70, a50\n"
"v_accvgpr_read_b32 v71, a51\n"
"v_accvgpr_read_b32 v72, a52\n"
"v_accvgpr_read_b32 v73, a53\n"
"v_accvgpr_read_b32 v74, a54\n"
"v_accvgpr_read_b32 v75, a55\n"
"v_mul_f32 v68, v68, v65\n"
"v_mul_f32 v69, v69, v65\n"
"v_mul_f32 v70, v70, v65\n"
"v_mul_f32 v71, v71, v65\n"
"v_mul_f32 v72, v72, v65\n"
"v_mul_f32 v73, v73, v65\n"
"v_mul_f32 v74, v74, v65\n"
"v_mul_f32 v75, v75, v65\n"
"v_cvt_pk_bf16_f32 v76, v68, v69\n"
"v_cvt_pk_bf16_f32 v77, v70, v71\n"
"v_cvt_pk_bf16_f32 v78, v72, v73\n"
"v_cvt_pk_bf16_f32 v79, v74, v75\n"
"buffer_store_dwordx4 v[76:79], %16, s[56:59], s42 offen offset:2048 sc1\n"
"v_accvgpr_read_b32 v80, a56\n"
"v_accvgpr_read_b32 v81, a57\n"
"v_accvgpr_read_b32 v82, a58\n"
"v_accvgpr_read_b32 v83, a59\n"
"v_accvgpr_read_b32 v84, a60\n"
"v_accvgpr_read_b32 v85, a61\n"
"v_accvgpr_read_b32 v86, a62\n"
"v_accvgpr_read_b32 v87, a63\n"
"v_mul_f32 v80, v80, v65\n"
"v_mul_f32 v81, v81, v65\n"
"v_mul_f32 v82, v82, v65\n"
"v_mul_f32 v83, v83, v65\n"
"v_mul_f32 v84, v84, v65\n"
"v_mul_f32 v85, v85, v65\n"
"v_mul_f32 v86, v86, v65\n"
"v_mul_f32 v87, v87, v65\n"
"v_cvt_pk_bf16_f32 v88, v80, v81\n"
"v_cvt_pk_bf16_f32 v89, v82, v83\n"
"v_cvt_pk_bf16_f32 v90, v84, v85\n"
"v_cvt_pk_bf16_f32 v91, v86, v87\n"
"buffer_store_dwordx4 v[88:91], %16, s[56:59], s42 offen offset:3072 sc1\n"
"v_accvgpr_read_b32 v64, a168\n"
"v_cmp_nle_f32 vcc, v64, v95\n"
"v_rcp_f32 v65, v64\n"
"s_or_b64 s[74:75], s[74:75], vcc\n"
"s_add_u32 s43, s79, 8192\n"
"v_accvgpr_read_b32 v68, a64\n"
"v_accvgpr_read_b32 v69, a65\n"
"v_accvgpr_read_b32 v70, a66\n"
"v_accvgpr_read_b32 v71, a67\n"
"v_accvgpr_read_b32 v72, a68\n"
"v_accvgpr_read_b32 v73, a69\n"
"v_accvgpr_read_b32 v74, a70\n"
"v_accvgpr_read_b32 v75, a71\n"
"v_mul_f32 v68, v68, v65\n"
"v_mul_f32 v69, v69, v65\n"
"v_mul_f32 v70, v70, v65\n"
"v_mul_f32 v71, v71, v65\n"
"v_mul_f32 v72, v72, v65\n"
"v_mul_f32 v73, v73, v65\n"
"v_mul_f32 v74, v74, v65\n"
"v_mul_f32 v75, v75, v65\n"
"v_cvt_pk_bf16_f32 v76, v68, v69\n"
"v_cvt_pk_bf16_f32 v77, v70, v71\n"
"v_cvt_pk_bf16_f32 v78, v72, v73\n"
"v_cvt_pk_bf16_f32 v79, v74, v75\n"
"buffer_store_dwordx4 v[76:79], %16, s[56:59], s43 offen sc1\n"
"v_accvgpr_read_b32 v80, a72\n"
"v_accvgpr_read_b32 v81, a73\n"
"v_accvgpr_read_b32 v82, a74\n"
"v_accvgpr_read_b32 v83, a75\n"
"v_accvgpr_read_b32 v84, a76\n"
"v_accvgpr_read_b32 v85, a77\n"
"v_accvgpr_read_b32 v86, a78\n"
"v_accvgpr_read_b32 v87, a79\n"
"v_mul_f32 v80, v80, v65\n"
"v_mul_f32 v81, v81, v65\n"
"v_mul_f32 v82, v82, v65\n"
"v_mul_f32 v83, v83, v65\n"
"v_mul_f32 v84, v84, v65\n"
"v_mul_f32 v85, v85, v65\n"
"v_mul_f32 v86, v86, v65\n"
"v_mul_f32 v87, v87, v65\n"
"v_cvt_pk_bf16_f32 v88, v80, v81\n"
"v_cvt_pk_bf16_f32 v89, v82, v83\n"
"v_cvt_pk_bf16_f32 v90, v84, v85\n"
"v_cvt_pk_bf16_f32 v91, v86, v87\n"
"buffer_store_dwordx4 v[88:91], %16, s[56:59], s43 offen offset:1024 sc1\n"
"v_accvgpr_read_b32 v68, a80\n"
"v_accvgpr_read_b32 v69, a81\n"
"v_accvgpr_read_b32 v70, a82\n"
"v_accvgpr_read_b32 v71, a83\n"
"v_accvgpr_read_b32 v72, a84\n"
"v_accvgpr_read_b32 v73, a85\n"
"v_accvgpr_read_b32 v74, a86\n"
"v_accvgpr_read_b32 v75, a87\n"
"v_mul_f32 v68, v68, v65\n"
"v_mul_f32 v69, v69, v65\n"
"v_mul_f32 v70, v70, v65\n"
"v_mul_f32 v71, v71, v65\n"
"v_mul_f32 v72, v72, v65\n"
"v_mul_f32 v73, v73, v65\n"
"v_mul_f32 v74, v74, v65\n"
"v_mul_f32 v75, v75, v65\n"
"v_cvt_pk_bf16_f32 v76, v68, v69\n"
"v_cvt_pk_bf16_f32 v77, v70, v71\n"
"v_cvt_pk_bf16_f32 v78, v72, v73\n"
"v_cvt_pk_bf16_f32 v79, v74, v75\n"
"buffer_store_dwordx4 v[76:79], %16, s[56:59], s43 offen offset:2048 sc1\n"
"v_accvgpr_read_b32 v80, a88\n"
"v_accvgpr_read_b32 v81, a89\n"
"v_accvgpr_read_b32 v82, a90\n"
"v_accvgpr_read_b32 v83, a91\n"
"v_accvgpr_read_b32 v84, a92\n"
"v_accvgpr_read_b32 v85, a93\n"
"v_accvgpr_read_b32 v86, a94\n"
"v_accvgpr_read_b32 v87, a95\n"
"v_mul_f32 v80, v80, v65\n"
"v_mul_f32 v81, v81, v65\n"
"v_mul_f32 v82, v82, v65\n"
"v_mul_f32 v83, v83, v65\n"
"v_mul_f32 v84, v84, v65\n"
"v_mul_f32 v85, v85, v65\n"
"v_mul_f32 v86, v86, v65\n"
"v_mul_f32 v87, v87, v65\n"
"v_cvt_pk_bf16_f32 v88, v80, v81\n"
"v_cvt_pk_bf16_f32 v89, v82, v83\n"
"v_cvt_pk_bf16_f32 v90, v84, v85\n"
"v_cvt_pk_bf16_f32 v91, v86, v87\n"
"buffer_store_dwordx4 v[88:91], %16, s[56:59], s43 offen offset:3072 sc1\n"
"v_accvgpr_read_b32 v64, a172\n"
"v_cmp_nle_f32 vcc, v64, v95\n"
"v_rcp_f32 v65, v64\n"
"s_or_b64 s[74:75], s[74:75], vcc\n"
"s_add_u32 s40, s79, 12288\n"
"v_accvgpr_read_b32 v68, a96\n"
"v_accvgpr_read_b32 v69, a97\n"
"v_accvgpr_read_b32 v70, a98\n"
"v_accvgpr_read_b32 v71, a99\n"
"v_accvgpr_read_b32 v72, a100\n"
"v_accvgpr_read_b32 v73, a101\n"
"v_accvgpr_read_b32 v74, a102\n"
"v_accvgpr_read_b32 v75, a103\n"
"v_mul_f32 v68, v68, v65\n"
"v_mul_f32 v69, v69, v65\n"
"v_mul_f32 v70, v70, v65\n"
"v_mul_f32 v71, v71, v65\n"
"v_mul_f32 v72, v72, v65\n"
"v_mul_f32 v73, v73, v65\n"
"v_mul_f32 v74, v74, v65\n"
"v_mul_f32 v75, v75, v65\n"
"v_cvt_pk_bf16_f32 v76, v68, v69\n"
"v_cvt_pk_bf16_f32 v77, v70, v71\n"
"v_cvt_pk_bf16_f32 v78, v72, v73\n"
"v_cvt_pk_bf16_f32 v79, v74, v75\n"
"buffer_store_dwordx4 v[76:79], %16, s[56:59], s40 offen sc1\n"
"v_accvgpr_read_b32 v80, a104\n"
"v_accvgpr_read_b32 v81, a105\n"
"v_accvgpr_read_b32 v82, a106\n"
"v_accvgpr_read_b32 v83, a107\n"
"v_accvgpr_read_b32 v84, a108\n"
"v_accvgpr_read_b32 v85, a109\n"
"v_accvgpr_read_b32 v86, a110\n"
"v_accvgpr_read_b32 v87, a111\n"
"v_mul_f32 v80, v80, v65\n"
"v_mul_f32 v81, v81, v65\n"
"v_mul_f32 v82, v82, v65\n"
"v_mul_f32 v83, v83, v65\n"
"v_mul_f32 v84, v84, v65\n"
"v_mul_f32 v85, v85, v65\n"
"v_mul_f32 v86, v86, v65\n"
"v_mul_f32 v87, v87, v65\n"
"v_cvt_pk_bf16_f32 v88, v80, v81\n"
"v_cvt_pk_bf16_f32 v89, v82, v83\n"
"v_cvt_pk_bf16_f32 v90, v84, v85\n"
"v_cvt_pk_bf16_f32 v91, v86, v87\n"
"buffer_store_dwordx4 v[88:91], %16, s[56:59], s40 offen offset:1024 sc1\n"
"v_accvgpr_read_b32 v68, a112\n"
"v_accvgpr_read_b32 v69, a113\n"
"v_accvgpr_read_b32 v70, a114\n"
"v_accvgpr_read_b32 v71, a115\n"
"v_accvgpr_read_b32 v72, a116\n"
"v_accvgpr_read_b32 v73, a117\n"
"v_accvgpr_read_b32 v74, a118\n"
"v_accvgpr_read_b32 v75, a119\n"
"v_mul_f32 v68, v68, v65\n"
"v_mul_f32 v69, v69, v65\n"
"v_mul_f32 v70, v70, v65\n"
"v_mul_f32 v71, v71, v65\n"
"v_mul_f32 v72, v72, v65\n"
"v_mul_f32 v73, v73, v65\n"
"v_mul_f32 v74, v74, v65\n"
"v_mul_f32 v75, v75, v65\n"
"v_cvt_pk_bf16_f32 v76, v68, v69\n"
"v_cvt_pk_bf16_f32 v77, v70, v71\n"
"v_cvt_pk_bf16_f32 v78, v72, v73\n"
"v_cvt_pk_bf16_f32 v79, v74, v75\n"
"buffer_store_dwordx4 v[76:79], %16, s[56:59], s40 offen offset:2048 sc1\n"
"v_accvgpr_read_b32 v80, a120\n"
"v_accvgpr_read_b32 v81, a121\n"
"v_accvgpr_read_b32 v82, a122\n"
"v_accvgpr_read_b32 v83, a123\n"
"v_accvgpr_read_b32 v84, a124\n"
"v_accvgpr_read_b32 v85, a125\n"
"v_accvgpr_read_b32 v86, a126\n"
"v_accvgpr_read_b32 v87, a127\n"
"v_mul_f32 v80, v80, v65\n"
"v_mul_f32 v81, v81, v65\n"
"v_mul_f32 v82, v82, v65\n"
"v_mul_f32 v83, v83, v65\n"
"v_mul_f32 v84, v84, v65\n"
"v_mul_f32 v85, v85, v65\n"
"v_mul_f32 v86, v86, v65\n"
"v_mul_f32 v87, v87, v65\n"
"v_cvt_pk_bf16_f32 v88, v80, v81\n"
"v_cvt_pk_bf16_f32 v89, v82, v83\n"
"v_cvt_pk_bf16_f32 v90, v84, v85\n"
"v_cvt_pk_bf16_f32 v91, v86, v87\n"
"buffer_store_dwordx4 v[88:91], %16, s[56:59], s40 offen offset:3072 sc1\n"
"v_accvgpr_read_b32 v64, a176\n"
"v_cmp_nle_f32 vcc, v64, v95\n"
"s_lshl_b32 s40, s72, 14\n"
"s_or_b64 s[74:75], s[74:75], vcc\n"
"v_add_u32 v92, s40, %16\n"
"s_cmp_lg_u64 s[74:75], 0\n"
"s_cselect_b32 s41, 1, 0\n"
"s_lshr_b32 s42, s60, 8\n"
"v_mov_b32 v93, s42\n"
"v_mov_b32 v94, s41\n"
"s_barrier\n"
"ds_write_b32 v93, v94 offset:49152\n"
"s_cmp_eq_u32 s71, 0\n"
"s_cbranch_scc1 .Lk1_p0_%=\n"
"ds_write_b128 v92, a[128:131] offset:0\n"
"ds_write_b128 v92, a[132:135] offset:1024\n"
"ds_write_b128 v92, a[136:139] offset:2048\n"
"ds_write_b128 v92, a[140:143] offset:3072\n"
"ds_write_b128 v92, a[144:147] offset:4096\n"
"ds_write_b128 v92, a[148:151] offset:5120\n"
"ds_write_b128 v92, a[152:155] offset:6144\n"
"ds_write_b128 v92, a[156:159] offset:7168\n"
"v_mov_b32 v68, v64\n"
"v_mov_b32 v69, v56\n"
"v_mov_b32 v70, 0\n"
"v_mov_b32 v71, 0\n"
"ds_write_b128 v92, v[68:71] offset:8192\n"
"s_waitcnt lgkmcnt(0)\n"
"s_barrier\n"
"s_branch .Lk1_join_%=\n"
".Lk1_p0_%=:\n"
"s_waitcnt lgkmcnt(0)\n"
"s_barrier\n"
"ds_read_b128 v[68:71], v92 offset:8192\n"
"ds_read_b128 v[96:99], v92 offset:0\n"
"ds_read_b128 v[100:103], v92 offset:1024\n"
"ds_read_b128 v[104:107], v92 offset:2048\n"
"ds_read_b128 v[108:111], v92 offset:3072\n"
"ds_read_b128 v[112:115], v92 offset:4096\n"
"ds_read_b128 v[116:119], v92 offset:5120\n"
"ds_read_b128 v[120:123], v92 offset:6144\n"
"ds_read_b128 v[124:127], v92 offset:7168\n"
"s_waitcnt lgkmcnt(8)\n"
"v_min_f32 v72, v56, v69\n"
"v_sub_f32 v73, v72, v56\n"
"v_sub_f32 v74, v72, v69\n"
"v_exp_f32 v73, v73\n"
"v_exp_f32 v74, v74\n"
"s_nop 0\n"
"v_mul_f32 v75, v64, v73\n"
"v_fma_f32 v75, v68, v74, v75\n"
"v_rcp_f32 v76, v75\n"
"s_nop 0\n"
"v_mul_f32 v73, v73, v76\n"
"v_mul_f32 v74, v74, v76\n"
"s_add_u32 s43, s77, 32768\n"
"s_waitcnt lgkmcnt(0)\n"
"v_accvgpr_read_b32 v78, a128\n"
"v_accvgpr_read_b32 v79, a129\n"
"v_accvgpr_read_b32 v80, a130\n"
"v_accvgpr_read_b32 v81, a131\n"
"v_accvgpr_read_b32 v82, a132\n"
"v_accvgpr_read_b32 v83, a133\n"
"v_accvgpr_read_b32 v84, a134\n"
"v_accvgpr_read_b32 v85, a135\n"
"v_mul_f32 v78, v78, v73\n"
"v_mul_f32 v79, v79, v73\n"
"v_mul_f32 v80, v80, v73\n"
"v_mul_f32 v81, v81, v73\n"
"v_mul_f32 v82, v82, v73\n"
"v_mul_f32 v83, v83, v73\n"
"v_mul_f32 v84, v84, v73\n"
"v_mul_f32 v85, v85, v73\n"
"v_fma_f32 v78, v96, v74, v78\n"
"v_fma_f32 v79, v97, v74, v79\n"
"v_fma_f32 v80, v98, v74, v80\n"
"v_fma_f32 v81, v99, v74, v81\n"
"v_fma_f32 v82, v100, v74, v82\n"
"v_fma_f32 v83, v101, v74, v83\n"
"v_fma_f32 v84, v102, v74, v84\n"
"v_fma_f32 v85, v103, v74, v85\n"
"v_cvt_pk_bf16_f32 v128, v78, v79\n"
"v_cvt_pk_bf16_f32 v129, v80, v81\n"
"v_cvt_pk_bf16_f32 v130, v82, v83\n"
"v_cvt_pk_bf16_f32 v131, v84, v85\n"
"buffer_store_dwordx4 v[128:131], %16, s[56:59], s43 offen sc1\n"
"v_accvgpr_read_b32 v78, a136\n"
"v_accvgpr_read_b32 v79, a137\n"
"v_accvgpr_read_b32 v80, a138\n"
"v_accvgpr_read_b32 v81, a139\n"
"v_accvgpr_read_b32 v82, a140\n"
"v_accvgpr_read_b32 v83, a141\n"
"v_accvgpr_read_b32 v84, a142\n"
"v_accvgpr_read_b32 v85, a143\n"
"v_mul_f32 v78, v78, v73\n"
"v_mul_f32 v79, v79, v73\n"
"v_mul_f32 v80, v80, v73\n"
"v_mul_f32 v81, v81, v73\n"
"v_mul_f32 v82, v82, v73\n"
"v_mul_f32 v83, v83, v73\n"
"v_mul_f32 v84, v84, v73\n"
"v_mul_f32 v85, v85, v73\n"
"v_fma_f32 v78, v104, v74, v78\n"
"v_fma_f32 v79, v105, v74, v79\n"
"v_fma_f32 v80, v106, v74, v80\n"
"v_fma_f32 v81, v107, v74, v81\n"
"v_fma_f32 v82, v108, v74, v82\n"
"v_fma_f32 v83, v109, v74, v83\n"
"v_fma_f32 v84, v110, v74, v84\n"
"v_fma_f32 v85, v111, v74, v85\n"
"v_cvt_pk_bf16_f32 v132, v78, v79\n"
"v_cvt_pk_bf16_f32 v133, v80, v81\n"
"v_cvt_pk_bf16_f32 v134, v82, v83\n"
"v_cvt_pk_bf16_f32 v135, v84, v85\n"
"buffer_store_dwordx4 v[132:135], %16, s[56:59], s43 offen offset:1024 sc1\n"
"v_accvgpr_read_b32 v78, a144\n"
"v_accvgpr_read_b32 v79, a145\n"
"v_accvgpr_read_b32 v80, a146\n"
"v_accvgpr_read_b32 v81, a147\n"
"v_accvgpr_read_b32 v82, a148\n"
"v_accvgpr_read_b32 v83, a149\n"
"v_accvgpr_read_b32 v84, a150\n"
"v_accvgpr_read_b32 v85, a151\n"
"v_mul_f32 v78, v78, v73\n"
"v_mul_f32 v79, v79, v73\n"
"v_mul_f32 v80, v80, v73\n"
"v_mul_f32 v81, v81, v73\n"
"v_mul_f32 v82, v82, v73\n"
"v_mul_f32 v83, v83, v73\n"
"v_mul_f32 v84, v84, v73\n"
"v_mul_f32 v85, v85, v73\n"
"v_fma_f32 v78, v112, v74, v78\n"
"v_fma_f32 v79, v113, v74, v79\n"
"v_fma_f32 v80, v114, v74, v80\n"
"v_fma_f32 v81, v115, v74, v81\n"
"v_fma_f32 v82, v116, v74, v82\n"
"v_fma_f32 v83, v117, v74, v83\n"
"v_fma_f32 v84, v118, v74, v84\n"
"v_fma_f32 v85, v119, v74, v85\n"
"v_cvt_pk_bf16_f32 v136, v78, v79\n"
"v_cvt_pk_bf16_f32 v137, v80, v81\n"
"v_cvt_pk_bf16_f32 v138, v82, v83\n"
"v_cvt_pk_bf16_f32 v139, v84, v85\n"
"buffer_store_dwordx4 v[136:139], %16, s[56:59], s43 offen offset:2048 sc1\n"
"v_accvgpr_read_b32 v78, a152\n"
"v_accvgpr_read_b32 v79, a153\n"
"v_accvgpr_read_b32 v80, a154\n"
"v_accvgpr_read_b32 v81, a155\n"
"v_accvgpr_read_b32 v82, a156\n"
"v_accvgpr_read_b32 v83, a157\n"
"v_accvgpr_read_b32 v84, a158\n"
"v_accvgpr_read_b32 v85, a159\n"
"v_mul_f32 v78, v78, v73\n"
"v_mul_f32 v79, v79, v73\n"
"v_mul_f32 v80, v80, v73\n"
"v_mul_f32 v81, v81, v73\n"
"v_mul_f32 v82, v82, v73\n"
"v_mul_f32 v83, v83, v73\n"
"v_mul_f32 v84, v84, v73\n"
"v_mul_f32 v85, v85, v73\n"
"v_fma_f32 v78, v120, v74, v78\n"
"v_fma_f32 v79, v121, v74, v79\n"
"v_fma_f32 v80, v122, v74, v80\n"
"v_fma_f32 v81, v123, v74, v81\n"
"v_fma_f32 v82, v124, v74, v82\n"
"v_fma_f32 v83, v125, v74, v83\n"
"v_fma_f32 v84, v126, v74, v84\n"
"v_fma_f32 v85, v127, v74, v85\n"
"v_cvt_pk_bf16_f32 v140, v78, v79\n"
"v_cvt_pk_bf16_f32 v141, v80, v81\n"
"v_cvt_pk_bf16_f32 v142, v82, v83\n"
"v_cvt_pk_bf16_f32 v143, v84, v85\n"
"buffer_store_dwordx4 v[140:143], %16, s[56:59], s43 offen offset:3072 sc1\n"
".Lk1_join_%=:\n"
"v_mov_b32 v77, 0\n"
"ds_read_b128 v[68:71], v77 offset:49152\n"
"s_waitcnt lgkmcnt(0)\n"
"v_or3_b32 v77, v68, v69, v70\n"
"v_or_b32 v77, v77, v71\n"
"s_nop 0\n"
"v_readfirstlane_b32 %0, v77\n"
"s_waitcnt vmcnt(0)\n"
".Lk1_end_%=:\n"

      : "=&s"(flag)
      : "s"(x), "s"(wq), "s"(kc), "s"((const char*)vc - 1024), "s"(o), "s"(w), "v"(vK00), "v"(vV00), "v"(vW0), "v"(vX0), "v"(vXt0),
        "v"(offK), "v"(offV), "v"(offW), "v"(offX), "v"(lane16), "s"(dbg),
        "v"(offXt), "s"(pfC), "s"(wo), "s"(pfO), "s"(rot)
      : K1_CLOBBERS);
  if (flag) {
    __syncthreads();
    attn_slow_path(x, wq, kc, vc, o);
  }
}

#define K2_ROT 0u
#define K2_CLOBBERS "v16", "v17", "v18", "v19", "v20", "v21", "v22", "v23", "v24", "v25", "v26", "v27", "v28", "v29", "v30", "v31", "v32", "v33", "v34", "v35", "v36", "v37", "v38", "v39", "v40", "v41", "v42", "v43", "v44", "v45", "v46", "v47", "v48", "v49", "v50", "v51", "v52", "v53", "v54", "v55", "v56", "v57", "v58", "v59", "v60", "v61", "v62", "v63", "v64", "v65", "v66", "v67", "v68", "v69", "v70", "v71", "v72", "v73", "v74", "v75", "v76", "v77", "v78", "v79", "v80", "v81", "v82", "v83", "v84", "v85", "v86", "v87", "v88", "v89", "v90", "v91", "v92", "v93", "v94", "v95", "v96", "v97", "v98", "v99", "v100", "v101", "v102", "v103", "v104", "v105", "v106", "v107", "v108", "v109", "v110", "v111", "v112", "v113", "v114", "v115", "v116", "v117", "v118", "v119", "v120", "v121", "v122", "v123", "v124", "v125", "v126", "v127", "v128", "v129", "v130", "v131", "v132", "v133", "v134", "v135", "v136", "v137", "v138", "v139", "v140", "v141", "v142", "v143", "v144", "v145", "v146", "v147", "v148", "v149", "v150", "v151", "v152", "v153", "v154", "v155", "v156", "v157", "v158", "v159", "s40", "s41", "s42", "s43", "s44", "s45", "s46", "s47", "s48", "s49", "s50", "s51", "s52", "s53", "s54", "s55", "s56", "s57", "s58", "s59", "s60", "s61", "s62", "s63", "s64", "s65", "s66", "s67", "s68", "s69", "s70", "s71", "s72", "s73", "s74", "s75", "s76", "s77", "s78", "s79", "vcc", "scc", "m0", "memory"

__global__ __launch_bounds__(512, 1) void oproj_kernel(const unsigned char* __restrict__ o, const u16* __restrict__ wo0,
                                                       const u16* __restrict__ wo1, u16* __restrict__ out) {
  const unsigned blk = blockIdx.x;
  const unsigned xcd = blk & 7, j = blk >> 3;
  const unsigned p = xcd >> 2;
  const unsigned qg = (xcd & 3) * 3 + j / 9, ng = j % 9;
  const u16* wo = p ? wo1 : wo0;
  const unsigned tid = threadIdx.x, lane = tid & 63;
  const unsigned w = __builtin_amdgcn_readfirstlane(tid >> 6);
  const unsigned q = lane & 15, g = lane >> 4, qq = (lane & 15) >> 2, p4 = lane & 3, wa = (w & 3) >> 1, wc = w & 1;
  const unsigned lw = w & 3;
  unsigned vO[3];
#pragma unroll
  for (int m = 0; m < 3; ++m) {
    unsigned piece = 3 * lw + m, Q = qg * 6 + (piece >> 1), b = Q / 9, t = Q % 9;
    vO[m] = lane * 16 + (p * NB + b) * HQ * 36864u + t * 4096 + (piece & 1) * 1024 + 4096 - 1024 * m;
  }
  const unsigned rowl = (lane >> 2) & 7;
  const unsigned vW0 = rowl * 1152 + (lane >> 5) * 64 + 16 * ((lane & 3) ^ (2 * (rowl >> 2))) + ng * 128 + 2 * lw * 8 * 1152;
  const unsigned vB = lane * 16 + wa * 6144;
  const unsigned vA0 = 12288 + (g >> 1) * 1024 + wc * 512 + (4 * (g & 1) + qq) * 64 + 32 * (g & 1) + 8 * p4;
  const unsigned vOut = q * 2304 + 8 * g + p * 1152 + ng * 128 + wc * 64 + (qg * 6 + 3 * wa) * 16 * 2304;
  const unsigned rot2 = K2_ROT;
  const unsigned char* dbg = o + (66u << 20) + (size_t)blk * 4096;
  asm volatile(
"s_cmp_ge_u32 %3, 4\n"
"s_cbranch_scc1 .Lk2_ld_%=\n"
"s_mov_b64 s[48:49], %0\n"
"s_and_b32 s49, s49, 0xffff\n"
"s_mov_b32 s50, 2654208\n"
"s_mov_b32 s51, 131072\n"
"v_add_u32 v16, 0, %8\n"
"v_add_u32 v19, 0, %9\n"
"v_xor_b32 v22, 32, v19\n"
"v_add_u32 v17, 61440, %8\n"
"v_add_u32 v20, 61440, %9\n"
"v_xor_b32 v23, 32, v20\n"
"v_add_u32 v18, 122880, %8\n"
"v_add_u32 v21, 122880, %9\n"
"v_xor_b32 v24, 32, v21\n"
"s_barrier\n"
"ds_read_b64_tr_b16 v[56:57], v19 offset:0\n"
"ds_read_b64_tr_b16 v[58:59], v19 offset:2048\n"
"ds_read_b64_tr_b16 v[64:65], v22 offset:0\n"
"ds_read_b64_tr_b16 v[66:67], v22 offset:2048\n"
"ds_read_b128 v[32:35], v16 offset:0\n"
"ds_read_b128 v[40:43], v16 offset:2048\n"
"ds_read_b128 v[48:51], v16 offset:4096\n"
"ds_read_b64_tr_b16 v[60:61], v19 offset:4096\n"
"ds_read_b64_tr_b16 v[62:63], v19 offset:6144\n"
"ds_read_b64_tr_b16 v[68:69], v22 offset:4096\n"
"ds_read_b64_tr_b16 v[70:71], v22 offset:6144\n"
"ds_read_b128 v[36:39], v16 offset:1024\n"
"ds_read_b128 v[44:47], v16 offset:3072\n"
"ds_read_b128 v[52:55], v16 offset:5120\n"
"s_waitcnt lgkmcnt(0)\n"
"s_barrier\n"
"v_mfma_f32_16x16x32_bf16 v[112:115], v[56:59], v[32:35], 0\n"
"ds_read_b64_tr_b16 v[96:97], v19 offset:20480\n"
"ds_read_b64_tr_b16 v[98:99], v19 offset:22528\n"
"v_mfma_f32_16x16x32_bf16 v[120:123], v[56:59], v[40:43], 0\n"
"ds_read_b64_tr_b16 v[104:105], v22 offset:20480\n"
"ds_read_b64_tr_b16 v[106:107], v22 offset:22528\n"
"v_mfma_f32_16x16x32_bf16 v[128:131], v[56:59], v[48:51], 0\n"
"ds_read_b128 v[72:75], v16 offset:20480\n"
"ds_read_b128 v[80:83], v16 offset:22528\n"
"v_mfma_f32_16x16x32_bf16 v[116:119], v[64:67], v[32:35], 0\n"
"ds_read_b128 v[88:91], v16 offset:24576\n"
"ds_read_b64_tr_b16 v[100:101], v19 offset:24576\n"
"v_mfma_f32_16x16x32_bf16 v[124:127], v[64:67], v[40:43], 0\n"
"ds_read_b64_tr_b16 v[102:103], v19 offset:26624\n"
"v_mfma_f32_16x16x32_bf16 v[132:135], v[64:67], v[48:51], 0\n"
"ds_read_b64_tr_b16 v[108:109], v22 offset:24576\n"
"v_mfma_f32_16x16x32_bf16 v[112:115], v[60:63], v[36:39], v[112:115]\n"
"ds_read_b64_tr_b16 v[110:111], v22 offset:26624\n"
"v_mfma_f32_16x16x32_bf16 v[120:123], v[60:63], v[44:47], v[120:123]\n"
"ds_read_b128 v[76:79], v16 offset:21504\n"
"v_mfma_f32_16x16x32_bf16 v[128:131], v[60:63], v[52:55], v[128:131]\n"
"ds_read_b128 v[84:87], v16 offset:23552\n"
"v_mfma_f32_16x16x32_bf16 v[116:119], v[68:71], v[36:39], v[116:119]\n"
"ds_read_b128 v[92:95], v16 offset:25600\n"
"v_mfma_f32_16x16x32_bf16 v[124:127], v[68:71], v[44:47], v[124:127]\n"
"v_mfma_f32_16x16x32_bf16 v[132:135], v[68:71], v[52:55], v[132:135]\n"
"s_waitcnt lgkmcnt(0)\n"
"s_barrier\n"
"v_mfma_f32_16x16x32_bf16 v[112:115], v[96:99], v[72:75], v[112:115]\n"
"ds_read_b64_tr_b16 v[56:57], v19 offset:40960\n"
"ds_read_b64_tr_b16 v[58:59], v19 offset:43008\n"
"v_mfma_f32_16x16x32_bf16 v[120:123], v[96:99], v[80:83], v[120:123]\n"
"ds_read_b64_tr_b16 v[64:65], v22 offset:40960\n"
"ds_read_b64_tr_b16 v[66:67], v22 offset:43008\n"
"v_mfma_f32_16x16x32_bf16 v[128:131], v[96:99], v[88:91], v[128:131]\n"
"ds_read_b128 v[32:35], v16 offset:40960\n"
"ds_read_b128 v[40:43], v16 offset:43008\n"
"v_mfma_f32_16x16x32_bf16 v[116:119], v[104:107], v[72:75], v[116:119]\n"
"ds_read_b128 v[48:51], v16 offset:45056\n"
"ds_read_b64_tr_b16 v[60:61], v19 offset:45056\n"
"v_mfma_f32_16x16x32_bf16 v[124:127], v[104:107], v[80:83], v[124:127]\n"
"ds_read_b64_tr_b16 v[62:63], v19 offset:47104\n"
"v_mfma_f32_16x16x32_bf16 v[132:135], v[104:107], v[88:91], v[132:135]\n"
"ds_read_b64_tr_b16 v[68:69], v22 offset:45056\n"
"v_mfma_f32_16x16x32_bf16 v[112:115], v[100:103], v[76:79], v[112:115]\n"
"ds_read_b64_tr_b16 v[70:71], v22 offset:47104\n"
"v_mfma_f32_16x16x32_bf16 v[120:123], v[100:103], v[84:87], v[120:123]\n"
"ds_read_b128 v[36:39], v16 offset:41984\n"
"v_mfma_f32_16x16x32_bf16 v[128:131], v[100:103], v[92:95], v[128:131]\n"
"ds_read_b128 v[44:47], v16 offset:44032\n"
"v_mfma_f32_16x16x32_bf16 v[116:119], v[108:111], v[76:79], v[116:119]\n"
"ds_read_b128 v[52:55], v16 offset:46080\n"
"v_mfma_f32_16x16x32_bf16 v[124:127], v[108:111], v[84:87], v[124:127]\n"
"v_mfma_f32_16x16x32_bf16 v[132:135], v[108:111], v[92:95], v[132:135]\n"
"s_waitcnt lgkmcnt(0)\n"
"s_barrier\n"
"v_mfma_f32_16x16x32_bf16 v[112:115], v[56:59], v[32:35], v[112:115]\n"
"ds_read_b64_tr_b16 v[96:97], v20 offset:0\n"
"ds_read_b64_tr_b16 v[98:99], v20 offset:2048\n"
"v_mfma_f32_16x16x32_bf16 v[120:123], v[56:59], v[40:43], v[120:123]\n"
"ds_read_b64_tr_b16 v[104:105], v23 offset:0\n"
"ds_read_b64_tr_b16 v[106:107], v23 offset:2048\n"
"v_mfma_f32_16x16x32_bf16 v[128:131], v[56:59], v[48:51], v[128:131]\n"
"ds_read_b128 v[72:75], v17 offset:0\n"
"ds_read_b128 v[80:83], v17 offset:2048\n"
"v_mfma_f32_16x16x32_bf16 v[116:119], v[64:67], v[32:35], v[116:119]\n"
"ds_read_b128 v[88:91], v17 offset:4096\n"
"ds_read_b64_tr_b16 v[100:101], v20 offset:4096\n"
"v_mfma_f32_16x16x32_bf16 v[124:127], v[64:67], v[40:43], v[124:127]\n"
"ds_read_b64_tr_b16 v[102:103], v20 offset:6144\n"
"v_mfma_f32_16x16x32_bf16 v[132:135], v[64:67], v[48:51], v[132:135]\n"
"ds_read_b64_tr_b16 v[108:109], v23 offset:4096\n"
"v_mfma_f32_16x16x32_bf16 v[112:115], v[60:63], v[36:39], v[112:115]\n"
"ds_read_b64_tr_b16 v[110:111], v23 offset:6144\n"
"v_mfma_f32_16x16x32_bf16 v[120:123], v[60:63], v[44:47], v[120:123]\n"
"ds_read_b128 v[76:79], v17 offset:1024\n"
"v_mfma_f32_16x16x32_bf16 v[128:131], v[60:63], v[52:55], v[128:131]\n"
"ds_read_b128 v[84:87], v17 offset:3072\n"
"v_mfma_f32_16x16x32_bf16 v[116:119], v[68:71], v[36:39], v[116:119]\n"
"ds_read_b128 v[92:95], v17 offset:5120\n"
"v_mfma_f32_16x16x32_bf16 v[124:127], v[68:71], v[44:47], v[124:127]\n"
"v_mfma_f32_16x16x32_bf16 v[132:135], v[68:71], v[52:55], v[132:135]\n"
"s_waitcnt lgkmcnt(0)\n"
"s_barrier\n"
"v_mfma_f32_16x16x32_bf16 v[112:115], v[96:99], v[72:75], v[112:115]\n"
"ds_read_b64_tr_b16 v[56:57], v20 offset:20480\n"
"ds_read_b64_tr_b16 v[58:59], v20 offset:22528\n"
"v_mfma_f32_16x16x32_bf16 v[120:123], v[96:99], v[80:83], v[120:123]\n"
"ds_read_b64_tr_b16 v[64:65], v23 offset:20480\n"
"ds_read_b64_tr_b16 v[66:67], v23 offset:22528\n"
"v_mfma_f32_16x16x32_bf16 v[128:131], v[96:99], v[88:91], v[128:131]\n"
"ds_read_b128 v[32:35], v17 offset:20480\n"
"ds_read_b128 v[40:43], v17 offset:22528\n"
"v_mfma_f32_16x16x32_bf16 v[116:119], v[104:107], v[72:75], v[116:119]\n"
"ds_read_b128 v[48:51], v17 offset:24576\n"
"ds_read_b64_tr_b16 v[60:61], v20 offset:24576\n"
"v_mfma_f32_16x16x32_bf16 v[124:127], v[104:107], v[80:83], v[124:127]\n"
"ds_read_b64_tr_b16 v[62:63], v20 offset:26624\n"
"v_mfma_f32_16x16x32_bf16 v[132:135], v[104:107], v[88:91], v[132:135]\n"
"ds_read_b64_tr_b16 v[68:69], v23 offset:24576\n"
"v_mfma_f32_16x16x32_bf16 v[112:115], v[100:103], v[76:79], v[112:115]\n"
"ds_read_b64_tr_b16 v[70:71], v23 offset:26624\n"
"v_mfma_f32_16x16x32_bf16 v[120:123], v[100:103], v[84:87], v[120:123]\n"
"ds_read_b128 v[36:39], v17 offset:21504\n"
"v_mfma_f32_16x16x32_bf16 v[128:131], v[100:103], v[92:95], v[128:131]\n"
"ds_read_b128 v[44:47], v17 offset:23552\n"
"v_mfma_f32_16x16x32_bf16 v[116:119], v[108:111], v[76:79], v[116:119]\n"
"ds_read_b128 v[52:55], v17 offset:25600\n"
"v_mfma_f32_16x16x32_bf16 v[124:127], v[108:111], v[84:87], v[124:127]\n"
"v_mfma_f32_16x16x32_bf16 v[132:135], v[108:111], v[92:95], v[132:135]\n"
"s_waitcnt lgkmcnt(0)\n"
"s_barrier\n"
"v_mfma_f32_16x16x32_bf16 v[112:115], v[56:59], v[32:35], v[112:115]\n"
"ds_read_b64_tr_b16 v[96:97], v20 offset:40960\n"
"ds_read_b64_tr_b16 v[98:99], v20 offset:43008\n"
"v_mfma_f32_16x16x32_bf16 v[120:123], v[56:59], v[40:43], v[120:123]\n"
"ds_read_b64_tr_b16 v[104:105], v23 offset:40960\n"
"ds_read_b64_tr_b16 v[106:107], v23 offset:43008\n"
"v_mfma_f32_16x16x32_bf16 v[128:131], v[56:59], v[48:51], v[128:131]\n"
"ds_read_b128 v[72:75], v17 offset:40960\n"
"ds_read_b128 v[80:83], v17 offset:43008\n"
"v_mfma_f32_16x16x32_bf16 v[116:119], v[64:67], v[32:35], v[116:119]\n"
"ds_read_b128 v[88:91], v17 offset:45056\n"
"ds_read_b64_tr_b16 v[100:101], v20 offset:45056\n"
"v_mfma_f32_16x16x32_bf16 v[124:127], v[64:67], v[40:43], v[124:127]\n"
"ds_read_b64_tr_b16 v[102:103], v20 offset:47104\n"
"v_mfma_f32_16x16x32_bf16 v[132:135], v[64:67], v[48:51], v[132:135]\n"
"ds_read_b64_tr_b16 v[108:109], v23 offset:45056\n"
"v_mfma_f32_16x16x32_bf16 v[112:115], v[60:63], v[36:39], v[112:115]\n"
"ds_read_b64_tr_b16 v[110:111], v23 offset:47104\n"
"v_mfma_f32_16x16x32_bf16 v[120:123], v[60:63], v[44:47], v[120:123]\n"
"ds_read_b128 v[76:79], v17 offset:41984\n"
"v_mfma_f32_16x16x32_bf16 v[128:131], v[60:63], v[52:55], v[128:131]\n"
"ds_read_b128 v[84:87], v17 offset:44032\n"
"v_mfma_f32_16x16x32_bf16 v[116:119], v[68:71], v[36:39], v[116:119]\n"
"ds_read_b128 v[92:95], v17 offset:46080\n"
"v_mfma_f32_16x16x32_bf16 v[124:127], v[68:71], v[44:47], v[124:127]\n"
"v_mfma_f32_16x16x32_bf16 v[132:135], v[68:71], v[52:55], v[132:135]\n"
"s_waitcnt lgkmcnt(0)\n"
"s_barrier\n"
"v_mfma_f32_16x16x32_bf16 v[112:115], v[96:99], v[72:75], v[112:115]\n"
"ds_read_b64_tr_b16 v[56:57], v21 offset:0\n"
"ds_read_b64_tr_b16 v[58:59], v21 offset:2048\n"
"v_mfma_f32_16x16x32_bf16 v[120:123], v[96:99], v[80:83], v[120:123]\n"
"ds_read_b64_tr_b16 v[64:65], v24 offset:0\n"
"ds_read_b64_tr_b16 v[66:67], v24 offset:2048\n"
"v_mfma_f32_16x16x32_bf16 v[128:131], v[96:99], v[88:91], v[128:131]\n"
"ds_read_b128 v[32:35], v18 offset:0\n"
"ds_read_b128 v[40:43], v18 offset:2048\n"
"v_mfma_f32_16x16x32_bf16 v[116:119], v[104:107], v[72:75], v[116:119]\n"
"ds_read_b128 v[48:51], v18 offset:4096\n"
"ds_read_b64_tr_b16 v[60:61], v21 offset:4096\n"
"v_mfma_f32_16x16x32_bf16 v[124:127], v[104:107], v[80:83], v[124:127]\n"
"ds_read_b64_tr_b16 v[62:63], v21 offset:6144\n"
"v_mfma_f32_16x16x32_bf16 v[132:135], v[104:107], v[88:91], v[132:135]\n"
"ds_read_b64_tr_b16 v[68:69], v24 offset:4096\n"
"v_mfma_f32_16x16x32_bf16 v[112:115], v[100:103], v[76:79], v[112:115]\n"
"ds_read_b64_tr_b16 v[70:71], v24 offset:6144\n"
"v_mfma_f32_16x16x32_bf16 v[120:123], v[100:103], v[84:87], v[120:123]\n"
"ds_read_b128 v[36:39], v18 offset:1024\n"
"v_mfma_f32_16x16x32_bf16 v[128:131], v[100:103], v[92:95], v[128:131]\n"
"ds_read_b128 v[44:47], v18 offset:3072\n"
"v_mfma_f32_16x16x32_bf16 v[116:119], v[108:111], v[76:79], v[116:119]\n"
"ds_read_b128 v[52:55], v18 offset:5120\n"
"v_mfma_f32_16x16x32_bf16 v[124:127], v[108:111], v[84:87], v[124:127]\n"
"v_mfma_f32_16x16x32_bf16 v[132:135], v[108:111], v[92:95], v[132:135]\n"
"s_waitcnt lgkmcnt(0)\n"
"s_barrier\n"
"v_mfma_f32_16x16x32_bf16 v[112:115], v[56:59], v[32:35], v[112:115]\n"
"ds_read_b64_tr_b16 v[96:97], v21 offset:20480\n"
"ds_read_b64_tr_b16 v[98:99], v21 offset:22528\n"
"v_mfma_f32_16x16x32_bf16 v[120:123], v[56:59], v[40:43], v[120:123]\n"
"ds_read_b64_tr_b16 v[104:105], v24 offset:20480\n"
"ds_read_b64_tr_b16 v[106:107], v24 offset:22528\n"
"v_mfma_f32_16x16x32_bf16 v[128:131], v[56:59], v[48:51], v[128:131]\n"
"ds_read_b128 v[72:75], v18 offset:20480\n"
"ds_read_b128 v[80:83], v18 offset:22528\n"
"v_mfma_f32_16x16x32_bf16 v[116:119], v[64:67], v[32:35], v[116:119]\n"
"ds_read_b128 v[88:91], v18 offset:24576\n"
"ds_read_b64_tr_b16 v[100:101], v21 offset:24576\n"
"v_mfma_f32_16x16x32_bf16 v[124:127], v[64:67], v[40:43], v[124:127]\n"
"ds_read_b64_tr_b16 v[102:103], v21 offset:26624\n"
"v_mfma_f32_16x16x32_bf16 v[132:135], v[64:67], v[48:51], v[132:135]\n"
"ds_read_b64_tr_b16 v[108:109], v24 offset:24576\n"
"v_mfma_f32_16x16x32_bf16 v[112:115], v[60:63], v[36:39], v[112:115]\n"
"ds_read_b64_tr_b16 v[110:111], v24 offset:26624\n"
"v_mfma_f32_16x16x32_bf16 v[120:123], v[60:63], v[44:47], v[120:123]\n"
"ds_read_b128 v[76:79], v18 offset:21504\n"
"v_mfma_f32_16x16x32_bf16 v[128:131], v[60:63], v[52:55], v[128:131]\n"
"ds_read_b128 v[84:87], v18 offset:23552\n"
"v_mfma_f32_16x16x32_bf16 v[116:119], v[68:71], v[36:39], v[116:119]\n"
"ds_read_b128 v[92:95], v18 offset:25600\n"
"v_mfma_f32_16x16x32_bf16 v[124:127], v[68:71], v[44:47], v[124:127]\n"
"v_mfma_f32_16x16x32_bf16 v[132:135], v[68:71], v[52:55], v[132:135]\n"
"s_waitcnt lgkmcnt(0)\n"
"s_barrier\n"
"v_mfma_f32_16x16x32_bf16 v[112:115], v[96:99], v[72:75], v[112:115]\n"
"ds_read_b64_tr_b16 v[56:57], v19 offset:0\n"
"ds_read_b64_tr_b16 v[58:59], v19 offset:2048\n"
"v_mfma_f32_16x16x32_bf16 v[120:123], v[96:99], v[80:83], v[120:123]\n"
"ds_read_b64_tr_b16 v[64:65], v22 offset:0\n"
"ds_read_b64_tr_b16 v[66:67], v22 offset:2048\n"
"v_mfma_f32_16x16x32_bf16 v[128:131], v[96:99], v[88:91], v[128:131]\n"
"ds_read_b128 v[32:35], v16 offset:0\n"
"ds_read_b128 v[40:43], v16 offset:2048\n"
"v_mfma_f32_16x16x32_bf16 v[116:119], v[104:107], v[72:75], v[116:119]\n"
"ds_read_b128 v[48:51], v16 offset:4096\n"
"ds_read_b64_tr_b16 v[60:61], v19 offset:4096\n"
"v_mfma_f32_16x16x32_bf16 v[124:127], v[104:107], v[80:83], v[124:127]\n"
"ds_read_b64_tr_b16 v[62:63], v19 offset:6144\n"
"v_mfma_f32_16x16x32_bf16 v[132:135], v[104:107], v[88:91], v[132:135]\n"
"ds_read_b64_tr_b16 v[68:69], v22 offset:4096\n"
"v_mfma_f32_16x16x32_bf16 v[112:115], v[100:103], v[76:79], v[112:115]\n"
"ds_read_b64_tr_b16 v[70:71], v22 offset:6144\n"
"v_mfma_f32_16x16x32_bf16 v[120:123], v[100:103], v[84:87], v[120:123]\n"
"ds_read_b128 v[36:39], v16 offset:1024\n"
"v_mfma_f32_16x16x32_bf16 v[128:131], v[100:103], v[92:95], v[128:131]\n"
"ds_read_b128 v[44:47], v16 offset:3072\n"
"v_mfma_f32_16x16x32_bf16 v[116:119], v[108:111], v[76:79], v[116:119]\n"
"ds_read_b128 v[52:55], v16 offset:5120\n"
"v_mfma_f32_16x16x32_bf16 v[124:127], v[108:111], v[84:87], v[124:127]\n"
"v_mfma_f32_16x16x32_bf16 v[132:135], v[108:111], v[92:95], v[132:135]\n"
"s_mov_b32 s57, 6\n"
".Lk2_c_%=:\n"
"s_waitcnt lgkmcnt(0)\n"
"s_barrier\n"
"v_mfma_f32_16x16x32_bf16 v[112:115], v[56:59], v[32:35], v[112:115]\n"
"ds_read_b64_tr_b16 v[96:97], v19 offset:20480\n"
"ds_read_b64_tr_b16 v[98:99], v19 offset:22528\n"
"v_mfma_f32_16x16x32_bf16 v[120:123], v[56:59], v[40:43], v[120:123]\n"
"ds_read_b64_tr_b16 v[104:105], v22 offset:20480\n"
"ds_read_b64_tr_b16 v[106:107], v22 offset:22528\n"
"v_mfma_f32_16x16x32_bf16 v[128:131], v[56:59], v[48:51], v[128:131]\n"
"ds_read_b128 v[72:75], v16 offset:20480\n"
"ds_read_b128 v[80:83], v16 offset:22528\n"
"v_mfma_f32_16x16x32_bf16 v[116:119], v[64:67], v[32:35], v[116:119]\n"
"ds_read_b128 v[88:91], v16 offset:24576\n"
"ds_read_b64_tr_b16 v[100:101], v19 offset:24576\n"
"v_mfma_f32_16x16x32_bf16 v[124:127], v[64:67], v[40:43], v[124:127]\n"
"ds_read_b64_tr_b16 v[102:103], v19 offset:26624\n"
"v_mfma_f32_16x16x32_bf16 v[132:135], v[64:67], v[48:51], v[132:135]\n"
"ds_read_b64_tr_b16 v[108:109], v22 offset:24576\n"
"v_mfma_f32_16x16x32_bf16 v[112:115], v[60:63], v[36:39], v[112:115]\n"
"ds_read_b64_tr_b16 v[110:111], v22 offset:26624\n"
"v_mfma_f32_16x16x32_bf16 v[120:123], v[60:63], v[44:47], v[120:123]\n"
"ds_read_b128 v[76:79], v16 offset:21504\n"
"v_mfma_f32_16x16x32_bf16 v[128:131], v[60:63], v[52:55], v[128:131]\n"
"ds_read_b128 v[84:87], v16 offset:23552\n"
"v_mfma_f32_16x16x32_bf16 v[116:119], v[68:71], v[36:39], v[116:119]\n"
"ds_read_b128 v[92:95], v16 offset:25600\n"
"v_mfma_f32_16x16x32_bf16 v[124:127], v[68:71], v[44:47], v[124:127]\n"
"v_mfma_f32_16x16x32_bf16 v[132:135], v[68:71], v[52:55], v[132:135]\n"
"s_waitcnt lgkmcnt(0)\n"
"s_barrier\n"
"v_mfma_f32_16x16x32_bf16 v[112:115], v[96:99], v[72:75], v[112:115]\n"
"ds_read_b64_tr_b16 v[56:57], v19 offset:40960\n"
"ds_read_b64_tr_b16 v[58:59], v19 offset:43008\n"
"v_mfma_f32_16x16x32_bf16 v[120:123], v[96:99], v[80:83], v[120:123]\n"
"ds_read_b64_tr_b16 v[64:65], v22 offset:40960\n"
"ds_read_b64_tr_b16 v[66:67], v22 offset:43008\n"
"v_mfma_f32_16x16x32_bf16 v[128:131], v[96:99], v[88:91], v[128:131]\n"
"ds_read_b128 v[32:35], v16 offset:40960\n"
"ds_read_b128 v[40:43], v16 offset:43008\n"
"v_mfma_f32_16x16x32_bf16 v[116:119], v[104:107], v[72:75], v[116:119]\n"
"ds_read_b128 v[48:51], v16 offset:45056\n"
"ds_read_b64_tr_b16 v[60:61], v19 offset:45056\n"
"v_mfma_f32_16x16x32_bf16 v[124:127], v[104:107], v[80:83], v[124:127]\n"
"ds_read_b64_tr_b16 v[62:63], v19 offset:47104\n"
"v_mfma_f32_16x16x32_bf16 v[132:135], v[104:107], v[88:91], v[132:135]\n"
"ds_read_b64_tr_b16 v[68:69], v22 offset:45056\n"
"v_mfma_f32_16x16x32_bf16 v[112:115], v[100:103], v[76:79], v[112:115]\n"
"ds_read_b64_tr_b16 v[70:71], v22 offset:47104\n"
"v_mfma_f32_16x16x32_bf16 v[120:123], v[100:103], v[84:87], v[120:123]\n"
"ds_read_b128 v[36:39], v16 offset:41984\n"
"v_mfma_f32_16x16x32_bf16 v[128:131], v[100:103], v[92:95], v[128:131]\n"
"ds_read_b128 v[44:47], v16 offset:44032\n"
"v_mfma_f32_16x16x32_bf16 v[116:119], v[108:111], v[76:79], v[116:119]\n"
"ds_read_b128 v[52:55], v16 offset:46080\n"
"v_mfma_f32_16x16x32_bf16 v[124:127], v[108:111], v[84:87], v[124:127]\n"
"v_mfma_f32_16x16x32_bf16 v[132:135], v[108:111], v[92:95], v[132:135]\n"
"s_waitcnt lgkmcnt(0)\n"
"s_barrier\n"
"v_mfma_f32_16x16x32_bf16 v[112:115], v[56:59], v[32:35], v[112:115]\n"
"ds_read_b64_tr_b16 v[96:97], v20 offset:0\n"
"ds_read_b64_tr_b16 v[98:99], v20 offset:2048\n"
"v_mfma_f32_16x16x32_bf16 v[120:123], v[56:59], v[40:43], v[120:123]\n"
"ds_read_b64_tr_b16 v[104:105], v23 offset:0\n"
"ds_read_b64_tr_b16 v[106:107], v23 offset:2048\n"
"v_mfma_f32_16x16x32_bf16 v[128:131], v[56:59], v[48:51], v[128:131]\n"
"ds_read_b128 v[72:75], v17 offset:0\n"
"ds_read_b128 v[80:83], v17 offset:2048\n"
"v_mfma_f32_16x16x32_bf16 v[116:119], v[64:67], v[32:35], v[116:119]\n"
"ds_read_b128 v[88:91], v17 offset:4096\n"
"ds_read_b64_tr_b16 v[100:101], v20 offset:4096\n"
"v_mfma_f32_16x16x32_bf16 v[124:127], v[64:67], v[40:43], v[124:127]\n"
"ds_read_b64_tr_b16 v[102:103], v20 offset:6144\n"
"v_mfma_f32_16x16x32_bf16 v[132:135], v[64:67], v[48:51], v[132:135]\n"
"ds_read_b64_tr_b16 v[108:109], v23 offset:4096\n"
"v_mfma_f32_16x16x32_bf16 v[112:115], v[60:63], v[36:39], v[112:115]\n"
"ds_read_b64_tr_b16 v[110:111], v23 offset:6144\n"
"v_mfma_f32_16x16x32_bf16 v[120:123], v[60:63], v[44:47], v[120:123]\n"
"ds_read_b128 v[76:79], v17 offset:1024\n"
"v_mfma_f32_16x16x32_bf16 v[128:131], v[60:63], v[52:55], v[128:131]\n"
"ds_read_b128 v[84:87], v17 offset:3072\n"
"v_mfma_f32_16x16x32_bf16 v[116:119], v[68:71], v[36:39], v[116:119]\n"
"ds_read_b128 v[92:95], v17 offset:5120\n"
"v_mfma_f32_16x16x32_bf16 v[124:127], v[68:71], v[44:47], v[124:127]\n"
"v_mfma_f32_16x16x32_bf16 v[132:135], v[68:71], v[52:55], v[132:135]\n"
"s_waitcnt lgkmcnt(0)\n"
"s_barrier\n"
"v_mfma_f32_16x16x32_bf16 v[112:115], v[96:99], v[72:75], v[112:115]\n"
"ds_read_b64_tr_b16 v[56:57], v20 offset:20480\n"
"ds_read_b64_tr_b16 v[58:59], v20 offset:22528\n"
"v_mfma_f32_16x16x32_bf16 v[120:123], v[96:99], v[80:83], v[120:123]\n"
"ds_read_b64_tr_b16 v[64:65], v23 offset:20480\n"
"ds_read_b64_tr_b16 v[66:67], v23 offset:22528\n"
"v_mfma_f32_16x16x32_bf16 v[128:131], v[96:99], v[88:91], v[128:131]\n"
"ds_read_b128 v[32:35], v17 offset:20480\n"
"ds_read_b128 v[40:43], v17 offset:22528\n"
"v_mfma_f32_16x16x32_bf16 v[116:119], v[104:107], v[72:75], v[116:119]\n"
"ds_read_b128 v[48:51], v17 offset:24576\n"
"ds_read_b64_tr_b16 v[60:61], v20 offset:24576\n"
"v_mfma_f32_16x16x32_bf16 v[124:127], v[104:107], v[80:83], v[124:127]\n"
"ds_read_b64_tr_b16 v[62:63], v20 offset:26624\n"
"v_mfma_f32_16x16x32_bf16 v[132:135], v[104:107], v[88:91], v[132:135]\n"
"ds_read_b64_tr_b16 v[68:69], v23 offset:24576\n"
"v_mfma_f32_16x16x32_bf16 v[112:115], v[100:103], v[76:79], v[112:115]\n"
"ds_read_b64_tr_b16 v[70:71], v23 offset:26624\n"
"v_mfma_f32_16x16x32_bf16 v[120:123], v[100:103], v[84:87], v[120:123]\n"
"ds_read_b128 v[36:39], v17 offset:21504\n"
"v_mfma_f32_16x16x32_bf16 v[128:131], v[100:103], v[92:95], v[128:131]\n"
"ds_read_b128 v[44:47], v17 offset:23552\n"
"v_mfma_f32_16x16x32_bf16 v[116:119], v[108:111], v[76:79], v[116:119]\n"
"ds_read_b128 v[52:55], v17 offset:25600\n"
"v_mfma_f32_16x16x32_bf16 v[124:127], v[108:111], v[84:87], v[124:127]\n"
"v_mfma_f32_16x16x32_bf16 v[132:135], v[108:111], v[92:95], v[132:135]\n"
"s_waitcnt lgkmcnt(0)\n"
"s_barrier\n"
"v_mfma_f32_16x16x32_bf16 v[112:115], v[56:59], v[32:35], v[112:115]\n"
"ds_read_b64_tr_b16 v[96:97], v20 offset:40960\n"
"ds_read_b64_tr_b16 v[98:99], v20 offset:43008\n"
"v_mfma_f32_16x16x32_bf16 v[120:123], v[56:59], v[40:43], v[120:123]\n"
"ds_read_b64_tr_b16 v[104:105], v23 offset:40960\n"
"ds_read_b64_tr_b16 v[106:107], v23 offset:43008\n"
"v_mfma_f32_16x16x32_bf16 v[128:131], v[56:59], v[48:51], v[128:131]\n"
"ds_read_b128 v[72:75], v17 offset:40960\n"
"ds_read_b128 v[80:83], v17 offset:43008\n"
"v_mfma_f32_16x16x32_bf16 v[116:119], v[64:67], v[32:35], v[116:119]\n"
"ds_read_b128 v[88:91], v17 offset:45056\n"
"ds_read_b64_tr_b16 v[100:101], v20 offset:45056\n"
"v_mfma_f32_16x16x32_bf16 v[124:127], v[64:67], v[40:43], v[124:127]\n"
"ds_read_b64_tr_b16 v[102:103], v20 offset:47104\n"
"v_mfma_f32_16x16x32_bf16 v[132:135], v[64:67], v[48:51], v[132:135]\n"
"ds_read_b64_tr_b16 v[108:109], v23 offset:45056\n"
"v_mfma_f32_16x16x32_bf16 v[112:115], v[60:63], v[36:39], v[112:115]\n"
"ds_read_b64_tr_b16 v[110:111], v23 offset:47104\n"
"v_mfma_f32_16x16x32_bf16 v[120:123], v[60:63], v[44:47], v[120:123]\n"
"ds_read_b128 v[76:79], v17 offset:41984\n"
"v_mfma_f32_16x16x32_bf16 v[128:131], v[60:63], v[52:55], v[128:131]\n"
"ds_read_b128 v[84:87], v17 offset:44032\n"
"v_mfma_f32_16x16x32_bf16 v[116:119], v[68:71], v[36:39], v[116:119]\n"
"ds_read_b128 v[92:95], v17 offset:46080\n"
"v_mfma_f32_16x16x32_bf16 v[124:127], v[68:71], v[44:47], v[124:127]\n"
"v_mfma_f32_16x16x32_bf16 v[132:135], v[68:71], v[52:55], v[132:135]\n"
"s_waitcnt lgkmcnt(0)\n"
"s_barrier\n"
"v_mfma_f32_16x16x32_bf16 v[112:115], v[96:99], v[72:75], v[112:115]\n"
"ds_read_b64_tr_b16 v[56:57], v21 offset:0\n"
"ds_read_b64_tr_b16 v[58:59], v21 offset:2048\n"
"v_mfma_f32_16x16x32_bf16 v[120:123], v[96:99], v[80:83], v[120:123]\n"
"ds_read_b64_tr_b16 v[64:65], v24 offset:0\n"
"ds_read_b64_tr_b16 v[66:67], v24 offset:2048\n"
"v_mfma_f32_16x16x32_bf16 v[128:131], v[96:99], v[88:91], v[128:131]\n"
"ds_read_b128 v[32:35], v18 offset:0\n"
"ds_read_b128 v[40:43], v18 offset:2048\n"
"v_mfma_f32_16x16x32_bf16 v[116:119], v[104:107], v[72:75], v[116:119]\n"
"ds_read_b128 v[48:51], v18 offset:4096\n"
"ds_read_b64_tr_b16 v[60:61], v21 offset:4096\n"
"v_mfma_f32_16x16x32_bf16 v[124:127], v[104:107], v[80:83], v[124:127]\n"
"ds_read_b64_tr_b16 v[62:63], v21 offset:6144\n"
"v_mfma_f32_16x16x32_bf16 v[132:135], v[104:107], v[88:91], v[132:135]\n"
"ds_read_b64_tr_b16 v[68:69], v24 offset:4096\n"
"v_mfma_f32_16x16x32_bf16 v[112:115], v[100:103], v[76:79], v[112:115]\n"
"ds_read_b64_tr_b16 v[70:71], v24 offset:6144\n"
"v_mfma_f32_16x16x32_bf16 v[120:123], v[100:103], v[84:87], v[120:123]\n"
"ds_read_b128 v[36:39], v18 offset:1024\n"
"v_mfma_f32_16x16x32_bf16 v[128:131], v[100:103], v[92:95], v[128:131]\n"
"ds_read_b128 v[44:47], v18 offset:3072\n"
"v_mfma_f32_16x16x32_bf16 v[116:119], v[108:111], v[76:79], v[116:119]\n"
"ds_read_b128 v[52:55], v18 offset:5120\n"
"v_mfma_f32_16x16x32_bf16 v[124:127], v[108:111], v[84:87], v[124:127]\n"
"v_mfma_f32_16x16x32_bf16 v[132:135], v[108:111], v[92:95], v[132:135]\n"
"s_waitcnt lgkmcnt(0)\n"
"s_barrier\n"
"v_mfma_f32_16x16x32_bf16 v[112:115], v[56:59], v[32:35], v[112:115]\n"
"ds_read_b64_tr_b16 v[96:97], v21 offset:20480\n"
"ds_read_b64_tr_b16 v[98:99], v21 offset:22528\n"
"v_mfma_f32_16x16x32_bf16 v[120:123], v[56:59], v[40:43], v[120:123]\n"
"ds_read_b64_tr_b16 v[104:105], v24 offset:20480\n"
"ds_read_b64_tr_b16 v[106:107], v24 offset:22528\n"
"v_mfma_f32_16x16x32_bf16 v[128:131], v[56:59], v[48:51], v[128:131]\n"
"ds_read_b128 v[72:75], v18 offset:20480\n"
"ds_read_b128 v[80:83], v18 offset:22528\n"
"v_mfma_f32_16x16x32_bf16 v[116:119], v[64:67], v[32:35], v[116:119]\n"
"ds_read_b128 v[88:91], v18 offset:24576\n"
"ds_read_b64_tr_b16 v[100:101], v21 offset:24576\n"
"v_mfma_f32_16x16x32_bf16 v[124:127], v[64:67], v[40:43], v[124:127]\n"
"ds_read_b64_tr_b16 v[102:103], v21 offset:26624\n"
"v_mfma_f32_16x16x32_bf16 v[132:135], v[64:67], v[48:51], v[132:135]\n"
"ds_read_b64_tr_b16 v[108:109], v24 offset:24576\n"
"v_mfma_f32_16x16x32_bf16 v[112:115], v[60:63], v[36:39], v[112:115]\n"
"ds_read_b64_tr_b16 v[110:111], v24 offset:26624\n"
"v_mfma_f32_16x16x32_bf16 v[120:123], v[60:63], v[44:47], v[120:123]\n"
"ds_read_b128 v[76:79], v18 offset:21504\n"
"v_mfma_f32_16x16x32_bf16 v[128:131], v[60:63], v[52:55], v[128:131]\n"
"ds_read_b128 v[84:87], v18 offset:23552\n"
"v_mfma_f32_16x16x32_bf16 v[116:119], v[68:71], v[36:39], v[116:119]\n"
"ds_read_b128 v[92:95], v18 offset:25600\n"
"v_mfma_f32_16x16x32_bf16 v[124:127], v[68:71], v[44:47], v[124:127]\n"
"v_mfma_f32_16x16x32_bf16 v[132:135], v[68:71], v[52:55], v[132:135]\n"
"s_waitcnt lgkmcnt(0)\n"
"s_barrier\n"
"v_mfma_f32_16x16x32_bf16 v[112:115], v[96:99], v[72:75], v[112:115]\n"
"ds_read_b64_tr_b16 v[56:57], v19 offset:0\n"
"ds_read_b64_tr_b16 v[58:59], v19 offset:2048\n"
"v_mfma_f32_16x16x32_bf16 v[120:123], v[96:99], v[80:83], v[120:123]\n"
"ds_read_b64_tr_b16 v[64:65], v22 offset:0\n"
"ds_read_b64_tr_b16 v[66:67], v22 offset:2048\n"
"v_mfma_f32_16x16x32_bf16 v[128:131], v[96:99], v[88:91], v[128:131]\n"
"ds_read_b128 v[32:35], v16 offset:0\n"
"ds_read_b128 v[40:43], v16 offset:2048\n"
"v_mfma_f32_16x16x32_bf16 v[116:119], v[104:107], v[72:75], v[116:119]\n"
"ds_read_b128 v[48:51], v16 offset:4096\n"
"ds_read_b64_tr_b16 v[60:61], v19 offset:4096\n"
"v_mfma_f32_16x16x32_bf16 v[124:127], v[104:107], v[80:83], v[124:127]\n"
"ds_read_b64_tr_b16 v[62:63], v19 offset:6144\n"
"v_mfma_f32_16x16x32_bf16 v[132:135], v[104:107], v[88:91], v[132:135]\n"
"ds_read_b64_tr_b16 v[68:69], v22 offset:4096\n"
"v_mfma_f32_16x16x32_bf16 v[112:115], v[100:103], v[76:79], v[112:115]\n"
"ds_read_b64_tr_b16 v[70:71], v22 offset:6144\n"
"v_mfma_f32_16x16x32_bf16 v[120:123], v[100:103], v[84:87], v[120:123]\n"
"ds_read_b128 v[36:39], v16 offset:1024\n"
"v_mfma_f32_16x16x32_bf16 v[128:131], v[100:103], v[92:95], v[128:131]\n"
"ds_read_b128 v[44:47], v16 offset:3072\n"
"v_mfma_f32_16x16x32_bf16 v[116:119], v[108:111], v[76:79], v[116:119]\n"
"ds_read_b128 v[52:55], v16 offset:5120\n"
"v_mfma_f32_16x16x32_bf16 v[124:127], v[108:111], v[84:87], v[124:127]\n"
"v_mfma_f32_16x16x32_bf16 v[132:135], v[108:111], v[92:95], v[132:135]\n"
"s_sub_u32 s57, s57, 1\n"
"s_cmp_lg_u32 s57, 0\n"
"s_cbranch_scc1 .Lk2_c_%=\n"
"s_waitcnt lgkmcnt(0)\n"
"s_barrier\n"
"v_mfma_f32_16x16x32_bf16 v[112:115], v[56:59], v[32:35], v[112:115]\n"
"ds_read_b64_tr_b16 v[96:97], v19 offset:20480\n"
"ds_read_b64_tr_b16 v[98:99], v19 offset:22528\n"
"v_mfma_f32_16x16x32_bf16 v[120:123], v[56:59], v[40:43], v[120:123]\n"
"ds_read_b64_tr_b16 v[104:105], v22 offset:20480\n"
"ds_read_b64_tr_b16 v[106:107], v22 offset:22528\n"
"v_mfma_f32_16x16x32_bf16 v[128:131], v[56:59], v[48:51], v[128:131]\n"
"ds_read_b128 v[72:75], v16 offset:20480\n"
"ds_read_b128 v[80:83], v16 offset:22528\n"
"v_mfma_f32_16x16x32_bf16 v[116:119], v[64:67], v[32:35], v[116:119]\n"
"ds_read_b128 v[88:91], v16 offset:24576\n"
"ds_read_b64_tr_b16 v[100:101], v19 offset:24576\n"
"v_mfma_f32_16x16x32_bf16 v[124:127], v[64:67], v[40:43], v[124:127]\n"
"ds_read_b64_tr_b16 v[102:103], v19 offset:26624\n"
"v_mfma_f32_16x16x32_bf16 v[132:135], v[64:67], v[48:51], v[132:135]\n"
"ds_read_b64_tr_b16 v[108:109], v22 offset:24576\n"
"v_mfma_f32_16x16x32_bf16 v[112:115], v[60:63], v[36:39], v[112:115]\n"
"ds_read_b64_tr_b16 v[110:111], v22 offset:26624\n"
"v_mfma_f32_16x16x32_bf16 v[120:123], v[60:63], v[44:47], v[120:123]\n"
"ds_read_b128 v[76:79], v16 offset:21504\n"
"v_mfma_f32_16x16x32_bf16 v[128:131], v[60:63], v[52:55], v[128:131]\n"
"ds_read_b128 v[84:87], v16 offset:23552\n"
"v_mfma_f32_16x16x32_bf16 v[116:119], v[68:71], v[36:39], v[116:119]\n"
"ds_read_b128 v[92:95], v16 offset:25600\n"
"v_mfma_f32_16x16x32_bf16 v[124:127], v[68:71], v[44:47], v[124:127]\n"
"v_mfma_f32_16x16x32_bf16 v[132:135], v[68:71], v[52:55], v[132:135]\n"
"s_waitcnt lgkmcnt(0)\n"
"s_barrier\n"
"v_mfma_f32_16x16x32_bf16 v[112:115], v[96:99], v[72:75], v[112:115]\n"
"ds_read_b64_tr_b16 v[56:57], v19 offset:40960\n"
"ds_read_b64_tr_b16 v[58:59], v19 offset:43008\n"
"v_mfma_f32_16x16x32_bf16 v[120:123], v[96:99], v[80:83], v[120:123]\n"
"ds_read_b64_tr_b16 v[64:65], v22 offset:40960\n"
"ds_read_b64_tr_b16 v[66:67], v22 offset:43008\n"
"v_mfma_f32_16x16x32_bf16 v[128:131], v[96:99], v[88:91], v[128:131]\n"
"ds_read_b128 v[32:35], v16 offset:40960\n"
"ds_read_b128 v[40:43], v16 offset:43008\n"
"v_mfma_f32_16x16x32_bf16 v[116:119], v[104:107], v[72:75], v[116:119]\n"
"ds_read_b128 v[48:51], v16 offset:45056\n"
"ds_read_b64_tr_b16 v[60:61], v19 offset:45056\n"
"v_mfma_f32_16x16x32_bf16 v[124:127], v[104:107], v[80:83], v[124:127]\n"
"ds_read_b64_tr_b16 v[62:63], v19 offset:47104\n"
"v_mfma_f32_16x16x32_bf16 v[132:135], v[104:107], v[88:91], v[132:135]\n"
"ds_read_b64_tr_b16 v[68:69], v22 offset:45056\n"
"v_mfma_f32_16x16x32_bf16 v[112:115], v[100:103], v[76:79], v[112:115]\n"
"ds_read_b64_tr_b16 v[70:71], v22 offset:47104\n"
"v_mfma_f32_16x16x32_bf16 v[120:123], v[100:103], v[84:87], v[120:123]\n"
"ds_read_b128 v[36:39], v16 offset:41984\n"
"v_mfma_f32_16x16x32_bf16 v[128:131], v[100:103], v[92:95], v[128:131]\n"
"ds_read_b128 v[44:47], v16 offset:44032\n"
"v_mfma_f32_16x16x32_bf16 v[116:119], v[108:111], v[76:79], v[116:119]\n"
"ds_read_b128 v[52:55], v16 offset:46080\n"
"v_mfma_f32_16x16x32_bf16 v[124:127], v[108:111], v[84:87], v[124:127]\n"
"v_mfma_f32_16x16x32_bf16 v[132:135], v[108:111], v[92:95], v[132:135]\n"
"s_waitcnt lgkmcnt(0)\n"
"s_barrier\n"
"v_mfma_f32_16x16x32_bf16 v[112:115], v[56:59], v[32:35], v[112:115]\n"
"ds_read_b64_tr_b16 v[96:97], v20 offset:0\n"
"ds_read_b64_tr_b16 v[98:99], v20 offset:2048\n"
"v_mfma_f32_16x16x32_bf16 v[120:123], v[56:59], v[40:43], v[120:123]\n"
"ds_read_b64_tr_b16 v[104:105], v23 offset:0\n"
"ds_read_b64_tr_b16 v[106:107], v23 offset:2048\n"
"v_mfma_f32_16x16x32_bf16 v[128:131], v[56:59], v[48:51], v[128:131]\n"
"ds_read_b128 v[72:75], v17 offset:0\n"
"ds_read_b128 v[80:83], v17 offset:2048\n"
"v_mfma_f32_16x16x32_bf16 v[116:119], v[64:67], v[32:35], v[116:119]\n"
"ds_read_b128 v[88:91], v17 offset:4096\n"
"ds_read_b64_tr_b16 v[100:101], v20 offset:4096\n"
"v_mfma_f32_16x16x32_bf16 v[124:127], v[64:67], v[40:43], v[124:127]\n"
"ds_read_b64_tr_b16 v[102:103], v20 offset:6144\n"
"v_mfma_f32_16x16x32_bf16 v[132:135], v[64:67], v[48:51], v[132:135]\n"
"ds_read_b64_tr_b16 v[108:109], v23 offset:4096\n"
"v_mfma_f32_16x16x32_bf16 v[112:115], v[60:63], v[36:39], v[112:115]\n"
"ds_read_b64_tr_b16 v[110:111], v23 offset:6144\n"
"v_mfma_f32_16x16x32_bf16 v[120:123], v[60:63], v[44:47], v[120:123]\n"
"ds_read_b128 v[76:79], v17 offset:1024\n"
"v_mfma_f32_16x16x32_bf16 v[128:131], v[60:63], v[52:55], v[128:131]\n"
"ds_read_b128 v[84:87], v17 offset:3072\n"
"v_mfma_f32_16x16x32_bf16 v[116:119], v[68:71], v[36:39], v[116:119]\n"
"ds_read_b128 v[92:95], v17 offset:5120\n"
"v_mfma_f32_16x16x32_bf16 v[124:127], v[68:71], v[44:47], v[124:127]\n"
"v_mfma_f32_16x16x32_bf16 v[132:135], v[68:71], v[52:55], v[132:135]\n"
"s_waitcnt lgkmcnt(0)\n"
"s_barrier\n"
"v_mfma_f32_16x16x32_bf16 v[112:115], v[96:99], v[72:75], v[112:115]\n"
"ds_read_b64_tr_b16 v[56:57], v20 offset:20480\n"
"ds_read_b64_tr_b16 v[58:59], v20 offset:22528\n"
"v_mfma_f32_16x16x32_bf16 v[120:123], v[96:99], v[80:83], v[120:123]\n"
"ds_read_b64_tr_b16 v[64:65], v23 offset:20480\n"
"ds_read_b64_tr_b16 v[66:67], v23 offset:22528\n"
"v_mfma_f32_16x16x32_bf16 v[128:131], v[96:99], v[88:91], v[128:131]\n"
"ds_read_b128 v[32:35], v17 offset:20480\n"
"ds_read_b128 v[40:43], v17 offset:22528\n"
"v_mfma_f32_16x16x32_bf16 v[116:119], v[104:107], v[72:75], v[116:119]\n"
"ds_read_b128 v[48:51], v17 offset:24576\n"
"ds_read_b64_tr_b16 v[60:61], v20 offset:24576\n"
"v_mfma_f32_16x16x32_bf16 v[124:127], v[104:107], v[80:83], v[124:127]\n"
"ds_read_b64_tr_b16 v[62:63], v20 offset:26624\n"
"v_mfma_f32_16x16x32_bf16 v[132:135], v[104:107], v[88:91], v[132:135]\n"
"ds_read_b64_tr_b16 v[68:69], v23 offset:24576\n"
"v_mfma_f32_16x16x32_bf16 v[112:115], v[100:103], v[76:79], v[112:115]\n"
"ds_read_b64_tr_b16 v[70:71], v23 offset:26624\n"
"v_mfma_f32_16x16x32_bf16 v[120:123], v[100:103], v[84:87], v[120:123]\n"
"ds_read_b128 v[36:39], v17 offset:21504\n"
"v_mfma_f32_16x16x32_bf16 v[128:131], v[100:103], v[92:95], v[128:131]\n"
"ds_read_b128 v[44:47], v17 offset:23552\n"
"v_mfma_f32_16x16x32_bf16 v[116:119], v[108:111], v[76:79], v[116:119]\n"
"ds_read_b128 v[52:55], v17 offset:25600\n"
"v_mfma_f32_16x16x32_bf16 v[124:127], v[108:111], v[84:87], v[124:127]\n"
"v_mfma_f32_16x16x32_bf16 v[132:135], v[108:111], v[92:95], v[132:135]\n"
"s_waitcnt lgkmcnt(0)\n"
"s_barrier\n"
"v_mfma_f32_16x16x32_bf16 v[112:115], v[56:59], v[32:35], v[112:115]\n"
"ds_read_b64_tr_b16 v[96:97], v20 offset:40960\n"
"ds_read_b64_tr_b16 v[98:99], v20 offset:43008\n"
"v_mfma_f32_16x16x32_bf16 v[120:123], v[56:59], v[40:43], v[120:123]\n"
"ds_read_b64_tr_b16 v[104:105], v23 offset:40960\n"
"ds_read_b64_tr_b16 v[106:107], v23 offset:43008\n"
"v_mfma_f32_16x16x32_bf16 v[128:131], v[56:59], v[48:51], v[128:131]\n"
"ds_read_b128 v[72:75], v17 offset:40960\n"
"ds_read_b128 v[80:83], v17 offset:43008\n"
"v_mfma_f32_16x16x32_bf16 v[116:119], v[64:67], v[32:35], v[116:119]\n"
"ds_read_b128 v[88:91], v17 offset:45056\n"
"ds_read_b64_tr_b16 v[100:101], v20 offset:45056\n"
"v_mfma_f32_16x16x32_bf16 v[124:127], v[64:67], v[40:43], v[124:127]\n"
"ds_read_b64_tr_b16 v[102:103], v20 offset:47104\n"
"v_mfma_f32_16x16x32_bf16 v[132:135], v[64:67], v[48:51], v[132:135]\n"
"ds_read_b64_tr_b16 v[108:109], v23 offset:45056\n"
"v_mfma_f32_16x16x32_bf16 v[112:115], v[60:63], v[36:39], v[112:115]\n"
"ds_read_b64_tr_b16 v[110:111], v23 offset:47104\n"
"v_mfma_f32_16x16x32_bf16 v[120:123], v[60:63], v[44:47], v[120:123]\n"
"ds_read_b128 v[76:79], v17 offset:41984\n"
"v_mfma_f32_16x16x32_bf16 v[128:131], v[60:63], v[52:55], v[128:131]\n"
"ds_read_b128 v[84:87], v17 offset:44032\n"
"v_mfma_f32_16x16x32_bf16 v[116:119], v[68:71], v[36:39], v[116:119]\n"
"ds_read_b128 v[92:95], v17 offset:46080\n"
"v_mfma_f32_16x16x32_bf16 v[124:127], v[68:71], v[44:47], v[124:127]\n"
"v_mfma_f32_16x16x32_bf16 v[132:135], v[68:71], v[52:55], v[132:135]\n"
"s_waitcnt lgkmcnt(0)\n"
"s_barrier\n"
"v_mfma_f32_16x16x32_bf16 v[112:115], v[96:99], v[72:75], v[112:115]\n"
"ds_read_b64_tr_b16 v[56:57], v21 offset:0\n"
"ds_read_b64_tr_b16 v[58:59], v21 offset:2048\n"
"v_mfma_f32_16x16x32_bf16 v[120:123], v[96:99], v[80:83], v[120:123]\n"
"ds_read_b64_tr_b16 v[64:65], v24 offset:0\n"
"ds_read_b64_tr_b16 v[66:67], v24 offset:2048\n"
"v_mfma_f32_16x16x32_bf16 v[128:131], v[96:99], v[88:91], v[128:131]\n"
"ds_read_b128 v[32:35], v18 offset:0\n"
"ds_read_b128 v[40:43], v18 offset:2048\n"
"v_mfma_f32_16x16x32_bf16 v[116:119], v[104:107], v[72:75], v[116:119]\n"
"ds_read_b128 v[48:51], v18 offset:4096\n"
"ds_read_b64_tr_b16 v[60:61], v21 offset:4096\n"
"v_mfma_f32_16x16x32_bf16 v[124:127], v[104:107], v[80:83], v[124:127]\n"
"ds_read_b64_tr_b16 v[62:63], v21 offset:6144\n"
"v_mfma_f32_16x16x32_bf16 v[132:135], v[104:107], v[88:91], v[132:135]\n"
"ds_read_b64_tr_b16 v[68:69], v24 offset:4096\n"
"v_mfma_f32_16x16x32_bf16 v[112:115], v[100:103], v[76:79], v[112:115]\n"
"ds_read_b64_tr_b16 v[70:71], v24 offset:6144\n"
"v_mfma_f32_16x16x32_bf16 v[120:123], v[100:103], v[84:87], v[120:123]\n"
"ds_read_b128 v[36:39], v18 offset:1024\n"
"v_mfma_f32_16x16x32_bf16 v[128:131], v[100:103], v[92:95], v[128:131]\n"
"ds_read_b128 v[44:47], v18 offset:3072\n"
"v_mfma_f32_16x16x32_bf16 v[116:119], v[108:111], v[76:79], v[116:119]\n"
"ds_read_b128 v[52:55], v18 offset:5120\n"
"v_mfma_f32_16x16x32_bf16 v[124:127], v[108:111], v[84:87], v[124:127]\n"
"v_mfma_f32_16x16x32_bf16 v[132:135], v[108:111], v[92:95], v[132:135]\n"
"s_waitcnt lgkmcnt(0)\n"
"s_barrier\n"
"v_mfma_f32_16x16x32_bf16 v[112:115], v[56:59], v[32:35], v[112:115]\n"
"ds_read_b64_tr_b16 v[96:97], v21 offset:20480\n"
"ds_read_b64_tr_b16 v[98:99], v21 offset:22528\n"
"v_mfma_f32_16x16x32_bf16 v[120:123], v[56:59], v[40:43], v[120:123]\n"
"ds_read_b64_tr_b16 v[104:105], v24 offset:20480\n"
"ds_read_b64_tr_b16 v[106:107], v24 offset:22528\n"
"v_mfma_f32_16x16x32_bf16 v[128:131], v[56:59], v[48:51], v[128:131]\n"
"ds_read_b128 v[72:75], v18 offset:20480\n"
"ds_read_b128 v[80:83], v18 offset:22528\n"
"v_mfma_f32_16x16x32_bf16 v[116:119], v[64:67], v[32:35], v[116:119]\n"
"ds_read_b128 v[88:91], v18 offset:24576\n"
"ds_read_b64_tr_b16 v[100:101], v21 offset:24576\n"
"v_mfma_f32_16x16x32_bf16 v[124:127], v[64:67], v[40:43], v[124:127]\n"
"ds_read_b64_tr_b16 v[102:103], v21 offset:26624\n"
"v_mfma_f32_16x16x32_bf16 v[132:135], v[64:67], v[48:51], v[132:135]\n"
"ds_read_b64_tr_b16 v[108:109], v24 offset:24576\n"
"v_mfma_f32_16x16x32_bf16 v[112:115], v[60:63], v[36:39], v[112:115]\n"
"ds_read_b64_tr_b16 v[110:111], v24 offset:26624\n"
"v_mfma_f32_16x16x32_bf16 v[120:123], v[60:63], v[44:47], v[120:123]\n"
"ds_read_b128 v[76:79], v18 offset:21504\n"
"v_mfma_f32_16x16x32_bf16 v[128:131], v[60:63], v[52:55], v[128:131]\n"
"ds_read_b128 v[84:87], v18 offset:23552\n"
"v_mfma_f32_16x16x32_bf16 v[116:119], v[68:71], v[36:39], v[116:119]\n"
"ds_read_b128 v[92:95], v18 offset:25600\n"
"v_mfma_f32_16x16x32_bf16 v[124:127], v[68:71], v[44:47], v[124:127]\n"
"v_mfma_f32_16x16x32_bf16 v[132:135], v[68:71], v[52:55], v[132:135]\n"
"s_waitcnt lgkmcnt(0)\n"
"s_barrier\n"
"v_mfma_f32_16x16x32_bf16 v[112:115], v[96:99], v[72:75], v[112:115]\n"
"v_mfma_f32_16x16x32_bf16 v[120:123], v[96:99], v[80:83], v[120:123]\n"
"v_mfma_f32_16x16x32_bf16 v[128:131], v[96:99], v[88:91], v[128:131]\n"
"v_mfma_f32_16x16x32_bf16 v[116:119], v[104:107], v[72:75], v[116:119]\n"
"v_mfma_f32_16x16x32_bf16 v[124:127], v[104:107], v[80:83], v[124:127]\n"
"v_mfma_f32_16x16x32_bf16 v[132:135], v[104:107], v[88:91], v[132:135]\n"
"v_mfma_f32_16x16x32_bf16 v[112:115], v[100:103], v[76:79], v[112:115]\n"
"v_mfma_f32_16x16x32_bf16 v[120:123], v[100:103], v[84:87], v[120:123]\n"
"v_mfma_f32_16x16x32_bf16 v[128:131], v[100:103], v[92:95], v[128:131]\n"
"v_mfma_f32_16x16x32_bf16 v[116:119], v[108:111], v[76:79], v[116:119]\n"
"v_mfma_f32_16x16x32_bf16 v[124:127], v[108:111], v[84:87], v[124:127]\n"
"v_mfma_f32_16x16x32_bf16 v[132:135], v[108:111], v[92:95], v[132:135]\n"
"s_nop 7\n"
"v_cvt_pk_bf16_f32 v140, v112, v113\n"
"v_cvt_pk_bf16_f32 v141, v114, v115\n"
"v_cvt_pk_bf16_f32 v142, v116, v117\n"
"v_cvt_pk_bf16_f32 v143, v118, v119\n"
"v_cvt_pk_bf16_f32 v144, v120, v121\n"
"v_cvt_pk_bf16_f32 v145, v122, v123\n"
"v_cvt_pk_bf16_f32 v146, v124, v125\n"
"v_cvt_pk_bf16_f32 v147, v126, v127\n"
"v_cvt_pk_bf16_f32 v148, v128, v129\n"
"v_cvt_pk_bf16_f32 v149, v130, v131\n"
"v_cvt_pk_bf16_f32 v150, v132, v133\n"
"v_cvt_pk_bf16_f32 v151, v134, v135\n"
"s_mov_b32 s58, 0\n"
"buffer_store_dwordx2 v[140:141], %10, s[48:51], s58 offen\n"
"buffer_store_dwordx2 v[142:143], %10, s[48:51], s58 offen offset:32\n"
"s_mov_b32 s59, 36864\n"
"buffer_store_dwordx2 v[144:145], %10, s[48:51], s59 offen\n"
"buffer_store_dwordx2 v[146:147], %10, s[48:51], s59 offen offset:32\n"
"s_mov_b32 s60, 73728\n"
"buffer_store_dwordx2 v[148:149], %10, s[48:51], s60 offen\n"
"buffer_store_dwordx2 v[150:151], %10, s[48:51], s60 offen offset:32\n"
"s_branch .Lk2_end_%=\n"
".Lk2_ld_%=:\n"
"s_mov_b64 s[40:41], %1\n"
"s_and_b32 s41, s41, 0xffff\n"
"s_mov_b32 s42, 18878464\n"
"s_mov_b32 s43, 131072\n"
"s_mov_b64 s[44:45], %2\n"
"s_and_b32 s45, s45, 0xffff\n"
"s_mov_b32 s46, 4718592\n"
"s_mov_b32 s47, 131072\n"
"s_sub_u32 s56, %3, 4\n"
"s_mul_i32 s52, s56, 3072\n"
"s_lshl_b32 s53, s56, 11\n"
"s_mul_i32 s54, %12, 36864\n"
"s_mul_i32 s55, %12, 147456\n"
"v_add_u32 v25, 8192, %7\n"
"s_add_u32 m0, s52, 0\n"
"s_nop 0\n"
"buffer_load_dwordx4 %4, s[40:43], s54 offen lds\n"
"buffer_load_dwordx4 %5, s[40:43], s54 offen offset:1024 lds\n"
"buffer_load_dwordx4 %6, s[40:43], s54 offen offset:2048 lds\n"
"s_add_u32 m0, s53, 12288\n"
"s_nop 0\n"
"buffer_load_dwordx4 %7, s[44:47], s55 offen lds\n"
"buffer_load_dwordx4 v25, s[44:47], s55 offen offset:1024 lds\n"
"s_add_u32 s54, s54, 2048\n"
"s_add_u32 s55, s55, 73728\n"
"s_add_u32 m0, s52, 20480\n"
"s_nop 0\n"
"buffer_load_dwordx4 %4, s[40:43], s54 offen lds\n"
"buffer_load_dwordx4 %5, s[40:43], s54 offen offset:1024 lds\n"
"buffer_load_dwordx4 %6, s[40:43], s54 offen offset:2048 lds\n"
"s_add_u32 m0, s53, 32768\n"
"s_nop 0\n"
"buffer_load_dwordx4 %7, s[44:47], s55 offen lds\n"
"buffer_load_dwordx4 v25, s[44:47], s55 offen offset:1024 lds\n"
"s_add_u32 s54, s54, 34816\n"
"s_add_u32 s55, s55, 73728\n"
"s_cmp_eq_u32 s55, 4718592\n"
"s_cselect_b32 s55, 0, s55\n"
"s_cselect_b32 s54, 0, s54\n"
"s_add_u32 m0, s52, 40960\n"
"s_nop 0\n"
"buffer_load_dwordx4 %4, s[40:43], s54 offen lds\n"
"buffer_load_dwordx4 %5, s[40:43], s54 offen offset:1024 lds\n"
"buffer_load_dwordx4 %6, s[40:43], s54 offen offset:2048 lds\n"
"s_add_u32 m0, s53, 53248\n"
"s_nop 0\n"
"buffer_load_dwordx4 %7, s[44:47], s55 offen lds\n"
"buffer_load_dwordx4 v25, s[44:47], s55 offen offset:1024 lds\n"
"s_add_u32 s54, s54, 2048\n"
"s_add_u32 s55, s55, 73728\n"
"s_add_u32 m0, s52, 61440\n"
"s_nop 0\n"
"buffer_load_dwordx4 %4, s[40:43], s54 offen lds\n"
"buffer_load_dwordx4 %5, s[40:43], s54 offen offset:1024 lds\n"
"buffer_load_dwordx4 %6, s[40:43], s54 offen offset:2048 lds\n"
"s_add_u32 m0, s53, 73728\n"
"s_nop 0\n"
"buffer_load_dwordx4 %7, s[44:47], s55 offen lds\n"
"buffer_load_dwordx4 v25, s[44:47], s55 offen offset:1024 lds\n"
"s_add_u32 s54, s54, 34816\n"
"s_add_u32 s55, s55, 73728\n"
"s_cmp_eq_u32 s55, 4718592\n"
"s_cselect_b32 s55, 0, s55\n"
"s_cselect_b32 s54, 0, s54\n"
"s_add_u32 m0, s52, 81920\n"
"s_nop 0\n"
"buffer_load_dwordx4 %4, s[40:43], s54 offen lds\n"
"buffer_load_dwordx4 %5, s[40:43], s54 offen offset:1024 lds\n"
"buffer_load_dwordx4 %6, s[40:43], s54 offen offset:2048 lds\n"
"s_add_u32 m0, s53, 94208\n"
"s_nop 0\n"
"buffer_load_dwordx4 %7, s[44:47], s55 offen lds\n"
"buffer_load_dwordx4 v25, s[44:47], s55 offen offset:1024 lds\n"
"s_add_u32 s54, s54, 2048\n"
"s_add_u32 s55, s55, 73728\n"
"s_add_u32 m0, s52, 102400\n"
"s_nop 0\n"
"buffer_load_dwordx4 %4, s[40:43], s54 offen lds\n"
"buffer_load_dwordx4 %5, s[40:43], s54 offen offset:1024 lds\n"
"buffer_load_dwordx4 %6, s[40:43], s54 offen offset:2048 lds\n"
"s_add_u32 m0, s53, 114688\n"
"s_nop 0\n"
"buffer_load_dwordx4 %7, s[44:47], s55 offen lds\n"
"buffer_load_dwordx4 v25, s[44:47], s55 offen offset:1024 lds\n"
"s_add_u32 s54, s54, 34816\n"
"s_add_u32 s55, s55, 73728\n"
"s_cmp_eq_u32 s55, 4718592\n"
"s_cselect_b32 s55, 0, s55\n"
"s_cselect_b32 s54, 0, s54\n"
"s_add_u32 m0, s52, 122880\n"
"s_nop 0\n"
"buffer_load_dwordx4 %4, s[40:43], s54 offen lds\n"
"buffer_load_dwordx4 %5, s[40:43], s54 offen offset:1024 lds\n"
"buffer_load_dwordx4 %6, s[40:43], s54 offen offset:2048 lds\n"
"s_add_u32 m0, s53, 135168\n"
"s_nop 0\n"
"buffer_load_dwordx4 %7, s[44:47], s55 offen lds\n"
"buffer_load_dwordx4 v25, s[44:47], s55 offen offset:1024 lds\n"
"s_add_u32 s54, s54, 2048\n"
"s_add_u32 s55, s55, 73728\n"
"s_add_u32 m0, s52, 143360\n"
"s_nop 0\n"
"buffer_load_dwordx4 %4, s[40:43], s54 offen lds\n"
"buffer_load_dwordx4 %5, s[40:43], s54 offen offset:1024 lds\n"
"buffer_load_dwordx4 %6, s[40:43], s54 offen offset:2048 lds\n"
"s_add_u32 m0, s53, 155648\n"
"s_nop 0\n"
"buffer_load_dwordx4 %7, s[44:47], s55 offen lds\n"
"buffer_load_dwordx4 v25, s[44:47], s55 offen offset:1024 lds\n"
"s_add_u32 s54, s54, 34816\n"
"s_add_u32 s55, s55, 73728\n"
"s_cmp_eq_u32 s55, 4718592\n"
"s_cselect_b32 s55, 0, s55\n"
"s_cselect_b32 s54, 0, s54\n"
"s_waitcnt vmcnt(35)\n"
"s_barrier\n"
"s_waitcnt vmcnt(30)\n"
"s_barrier\n"
"s_add_u32 m0, s52, 0\n"
"s_nop 0\n"
"buffer_load_dwordx4 %4, s[40:43], s54 offen lds\n"
"buffer_load_dwordx4 %5, s[40:43], s54 offen offset:1024 lds\n"
"buffer_load_dwordx4 %6, s[40:43], s54 offen offset:2048 lds\n"
"s_add_u32 m0, s53, 12288\n"
"s_nop 0\n"
"buffer_load_dwordx4 %7, s[44:47], s55 offen lds\n"
"buffer_load_dwordx4 v25, s[44:47], s55 offen offset:1024 lds\n"
"s_add_u32 s54, s54, 2048\n"
"s_add_u32 s55, s55, 73728\n"
"s_waitcnt vmcnt(30)\n"
"s_barrier\n"
"s_add_u32 m0, s52, 20480\n"
"s_nop 0\n"
"buffer_load_dwordx4 %4, s[40:43], s54 offen lds\n"
"buffer_load_dwordx4 %5, s[40:43], s54 offen offset:1024 lds\n"
"buffer_load_dwordx4 %6, s[40:43], s54 offen offset:2048 lds\n"
"s_add_u32 m0, s53, 32768\n"
"s_nop 0\n"
"buffer_load_dwordx4 %7, s[44:47], s55 offen lds\n"
"buffer_load_dwordx4 v25, s[44:47], s55 offen offset:1024 lds\n"
"s_add_u32 s54, s54, 34816\n"
"s_add_u32 s55, s55, 73728\n"
"s_cmp_eq_u32 s55, 4718592\n"
"s_cselect_b32 s55, 0, s55\n"
"s_cselect_b32 s54, 0, s54\n"
"s_waitcnt vmcnt(30)\n"
"s_barrier\n"
"s_add_u32 m0, s52, 40960\n"
"s_nop 0\n"
"buffer_load_dwordx4 %4, s[40:43], s54 offen lds\n"
"buffer_load_dwordx4 %5, s[40:43], s54 offen offset:1024 lds\n"
"buffer_load_dwordx4 %6, s[40:43], s54 offen offset:2048 lds\n"
"s_add_u32 m0, s53, 53248\n"
"s_nop 0\n"
"buffer_load_dwordx4 %7, s[44:47], s55 offen lds\n"
"buffer_load_dwordx4 v25, s[44:47], s55 offen offset:1024 lds\n"
"s_add_u32 s54, s54, 2048\n"
"s_add_u32 s55, s55, 73728\n"
"s_waitcnt vmcnt(30)\n"
"s_barrier\n"
"s_add_u32 m0, s52, 61440\n"
"s_nop 0\n"
"buffer_load_dwordx4 %4, s[40:43], s54 offen lds\n"
"buffer_load_dwordx4 %5, s[40:43], s54 offen offset:1024 lds\n"
"buffer_load_dwordx4 %6, s[40:43], s54 offen offset:2048 lds\n"
"s_add_u32 m0, s53, 73728\n"
"s_nop 0\n"
"buffer_load_dwordx4 %7, s[44:47], s55 offen lds\n"
"buffer_load_dwordx4 v25, s[44:47], s55 offen offset:1024 lds\n"
"s_add_u32 s54, s54, 34816\n"
"s_add_u32 s55, s55, 73728\n"
"s_cmp_eq_u32 s55, 4718592\n"
"s_cselect_b32 s55, 0, s55\n"
"s_cselect_b32 s54, 0, s54\n"
"s_waitcnt vmcnt(30)\n"
"s_barrier\n"
"s_add_u32 m0, s52, 81920\n"
"s_nop 0\n"
"buffer_load_dwordx4 %4, s[40:43], s54 offen lds\n"
"buffer_load_dwordx4 %5, s[40:43], s54 offen offset:1024 lds\n"
"buffer_load_dwordx4 %6, s[40:43], s54 offen offset:2048 lds\n"
"s_add_u32 m0, s53, 94208\n"
"s_nop 0\n"
"buffer_load_dwordx4 %7, s[44:47], s55 offen lds\n"
"buffer_load_dwordx4 v25, s[44:47], s55 offen offset:1024 lds\n"
"s_add_u32 s54, s54, 2048\n"
"s_add_u32 s55, s55, 73728\n"
"s_waitcnt vmcnt(30)\n"
"s_barrier\n"
"s_add_u32 m0, s52, 102400\n"
"s_nop 0\n"
"buffer_load_dwordx4 %4, s[40:43], s54 offen lds\n"
"buffer_load_dwordx4 %5, s[40:43], s54 offen offset:1024 lds\n"
"buffer_load_dwordx4 %6, s[40:43], s54 offen offset:2048 lds\n"
"s_add_u32 m0, s53, 114688\n"
"s_nop 0\n"
"buffer_load_dwordx4 %7, s[44:47], s55 offen lds\n"
"buffer_load_dwordx4 v25, s[44:47], s55 offen offset:1024 lds\n"
"s_add_u32 s54, s54, 34816\n"
"s_add_u32 s55, s55, 73728\n"
"s_cmp_eq_u32 s55, 4718592\n"
"s_cselect_b32 s55, 0, s55\n"
"s_cselect_b32 s54, 0, s54\n"
"s_waitcnt vmcnt(30)\n"
"s_barrier\n"
"s_add_u32 m0, s52, 122880\n"
"s_nop 0\n"
"buffer_load_dwordx4 %4, s[40:43], s54 offen lds\n"
"buffer_load_dwordx4 %5, s[40:43], s54 offen offset:1024 lds\n"
"buffer_load_dwordx4 %6, s[40:43], s54 offen offset:2048 lds\n"
"s_add_u32 m0, s53, 135168\n"
"s_nop 0\n"
"buffer_load_dwordx4 %7, s[44:47], s55 offen lds\n"
"buffer_load_dwordx4 v25, s[44:47], s55 offen offset:1024 lds\n"
"s_add_u32 s54, s54, 2048\n"
"s_add_u32 s55, s55, 73728\n"
"s_waitcnt vmcnt(30)\n"
"s_barrier\n"
"s_add_u32 m0, s52, 143360\n"
"s_nop 0\n"
"buffer_load_dwordx4 %4, s[40:43], s54 offen lds\n"
"buffer_load_dwordx4 %5, s[40:43], s54 offen offset:1024 lds\n"
"buffer_load_dwordx4 %6, s[40:43], s54 offen offset:2048 lds\n"
"s_add_u32 m0, s53, 155648\n"
"s_nop 0\n"
"buffer_load_dwordx4 %7, s[44:47], s55 offen lds\n"
"buffer_load_dwordx4 v25, s[44:47], s55 offen offset:1024 lds\n"
"s_add_u32 s54, s54, 34816\n"
"s_add_u32 s55, s55, 73728\n"
"s_cmp_eq_u32 s55, 4718592\n"
"s_cselect_b32 s55, 0, s55\n"
"s_cselect_b32 s54, 0, s54\n"
"s_mov_b32 s57, 6\n"
".Lk2_l_%=:\n"
"s_waitcnt vmcnt(30)\n"
"s_barrier\n"
"s_add_u32 m0, s52, 0\n"
"s_nop 0\n"
"buffer_load_dwordx4 %4, s[40:43], s54 offen lds\n"
"buffer_load_dwordx4 %5, s[40:43], s54 offen offset:1024 lds\n"
"buffer_load_dwordx4 %6, s[40:43], s54 offen offset:2048 lds\n"
"s_add_u32 m0, s53, 12288\n"
"s_nop 0\n"
"buffer_load_dwordx4 %7, s[44:47], s55 offen lds\n"
"buffer_load_dwordx4 v25, s[44:47], s55 offen offset:1024 lds\n"
"s_add_u32 s54, s54, 2048\n"
"s_add_u32 s55, s55, 73728\n"
"s_waitcnt vmcnt(30)\n"
"s_barrier\n"
"s_add_u32 m0, s52, 20480\n"
"s_nop 0\n"
"buffer_load_dwordx4 %4, s[40:43], s54 offen lds\n"
"buffer_load_dwordx4 %5, s[40:43], s54 offen offset:1024 lds\n"
"buffer_load_dwordx4 %6, s[40:43], s54 offen offset:2048 lds\n"
"s_add_u32 m0, s53, 32768\n"
"s_nop 0\n"
"buffer_load_dwordx4 %7, s[44:47], s55 offen lds\n"
"buffer_load_dwordx4 v25, s[44:47], s55 offen offset:1024 lds\n"
"s_add_u32 s54, s54, 34816\n"
"s_add_u32 s55, s55, 73728\n"
"s_cmp_eq_u32 s55, 4718592\n"
"s_cselect_b32 s55, 0, s55\n"
"s_cselect_b32 s54, 0, s54\n"
"s_waitcnt vmcnt(30)\n"
"s_barrier\n"
"s_add_u32 m0, s52, 40960\n"
"s_nop 0\n"
"buffer_load_dwordx4 %4, s[40:43], s54 offen lds\n"
"buffer_load_dwordx4 %5, s[40:43], s54 offen offset:1024 lds\n"
"buffer_load_dwordx4 %6, s[40:43], s54 offen offset:2048 lds\n"
"s_add_u32 m0, s53, 53248\n"
"s_nop 0\n"
"buffer_load_dwordx4 %7, s[44:47], s55 offen lds\n"
"buffer_load_dwordx4 v25, s[44:47], s55 offen offset:1024 lds\n"
"s_add_u32 s54, s54, 2048\n"
"s_add_u32 s55, s55, 73728\n"
"s_waitcnt vmcnt(30)\n"
"s_barrier\n"
"s_add_u32 m0, s52, 61440\n"
"s_nop 0\n"
"buffer_load_dwordx4 %4, s[40:43], s54 offen lds\n"
"buffer_load_dwordx4 %5, s[40:43], s54 offen offset:1024 lds\n"
"buffer_load_dwordx4 %6, s[40:43], s54 offen offset:2048 lds\n"
"s_add_u32 m0, s53, 73728\n"
"s_nop 0\n"
"buffer_load_dwordx4 %7, s[44:47], s55 offen lds\n"
"buffer_load_dwordx4 v25, s[44:47], s55 offen offset:1024 lds\n"
"s_add_u32 s54, s54, 34816\n"
"s_add_u32 s55, s55, 73728\n"
"s_cmp_eq_u32 s55, 4718592\n"
"s_cselect_b32 s55, 0, s55\n"
"s_cselect_b32 s54, 0, s54\n"
"s_waitcnt vmcnt(30)\n"
"s_barrier\n"
"s_add_u32 m0, s52, 81920\n"
"s_nop 0\n"
"buffer_load_dwordx4 %4, s[40:43], s54 offen lds\n"
"buffer_load_dwordx4 %5, s[40:43], s54 offen offset:1024 lds\n"
"buffer_load_dwordx4 %6, s[40:43], s54 offen offset:2048 lds\n"
"s_add_u32 m0, s53, 94208\n"
"s_nop 0\n"
"buffer_load_dwordx4 %7, s[44:47], s55 offen lds\n"
"buffer_load_dwordx4 v25, s[44:47], s55 offen offset:1024 lds\n"
"s_add_u32 s54, s54, 2048\n"
"s_add_u32 s55, s55, 73728\n"
"s_waitcnt vmcnt(30)\n"
"s_barrier\n"
"s_add_u32 m0, s52, 102400\n"
"s_nop 0\n"
"buffer_load_dwordx4 %4, s[40:43], s54 offen lds\n"
"buffer_load_dwordx4 %5, s[40:43], s54 offen offset:1024 lds\n"
"buffer_load_dwordx4 %6, s[40:43], s54 offen offset:2048 lds\n"
"s_add_u32 m0, s53, 114688\n"
"s_nop 0\n"
"buffer_load_dwordx4 %7, s[44:47], s55 offen lds\n"
"buffer_load_dwordx4 v25, s[44:47], s55 offen offset:1024 lds\n"
"s_add_u32 s54, s54, 34816\n"
"s_add_u32 s55, s55, 73728\n"
"s_cmp_eq_u32 s55, 4718592\n"
"s_cselect_b32 s55, 0, s55\n"
"s_cselect_b32 s54, 0, s54\n"
"s_waitcnt vmcnt(30)\n"
"s_barrier\n"
"s_add_u32 m0, s52, 122880\n"
"s_nop 0\n"
"buffer_load_dwordx4 %4, s[40:43], s54 offen lds\n"
"buffer_load_dwordx4 %5, s[40:43], s54 offen offset:1024 lds\n"
"buffer_load_dwordx4 %6, s[40:43], s54 offen offset:2048 lds\n"
"s_add_u32 m0, s53, 135168\n"
"s_nop 0\n"
"buffer_load_dwordx4 %7, s[44:47], s55 offen lds\n"
"buffer_load_dwordx4 v25, s[44:47], s55 offen offset:1024 lds\n"
"s_add_u32 s54, s54, 2048\n"
"s_add_u32 s55, s55, 73728\n"
"s_waitcnt vmcnt(30)\n"
"s_barrier\n"
"s_add_u32 m0, s52, 143360\n"
"s_nop 0\n"
"buffer_load_dwordx4 %4, s[40:43], s54 offen lds\n"
"buffer_load_dwordx4 %5, s[40:43], s54 offen offset:1024 lds\n"
"buffer_load_dwordx4 %6, s[40:43], s54 offen offset:2048 lds\n"
"s_add_u32 m0, s53, 155648\n"
"s_nop 0\n"
"buffer_load_dwordx4 %7, s[44:47], s55 offen lds\n"
"buffer_load_dwordx4 v25, s[44:47], s55 offen offset:1024 lds\n"
"s_add_u32 s54, s54, 34816\n"
"s_add_u32 s55, s55, 73728\n"
"s_cmp_eq_u32 s55, 4718592\n"
"s_cselect_b32 s55, 0, s55\n"
"s_cselect_b32 s54, 0, s54\n"
"s_sub_u32 s57, s57, 1\n"
"s_cmp_lg_u32 s57, 0\n"
"s_cbranch_scc1 .Lk2_l_%=\n"
"s_waitcnt vmcnt(30)\n"
"s_barrier\n"
"s_waitcnt vmcnt(25)\n"
"s_barrier\n"
"s_waitcnt vmcnt(20)\n"
"s_barrier\n"
"s_waitcnt vmcnt(15)\n"
"s_barrier\n"
"s_waitcnt vmcnt(10)\n"
"s_barrier\n"
"s_waitcnt vmcnt(5)\n"
"s_barrier\n"
"s_waitcnt vmcnt(0)\n"
"s_barrier\n"
"s_barrier\n"
".Lk2_end_%=:\n"
"k2_code_end:\n"

      :
      : "s"(out), "s"(o - 4096), "s"(wo), "s"(w), "v"(vO[0]), "v"(vO[1]), "v"(vO[2]), "v"(vW0), "v"(vB), "v"(vA0), "v"(vOut), "s"(dbg), "s"(rot2)
      : K2_CLOBBERS);
}

extern "C" void kernel_launch(void* const* d_in, const int* in_sizes, int n_in, void* d_out, int out_size,
                              void* d_ws, size_t ws_size, hipStream_t stream) {
  K1Args a;
  for (int p = 0; p < 2; ++p) {
    a.x[p] = (const u16*)d_in[5 * p + 0];
    a.k[p] = (const u16*)d_in[5 * p + 1];
    a.v[p] = (const u16*)d_in[5 * p + 2];
    a.wq[p] = (const u16*)d_in[5 * p + 3];
    a.wo[p] = (const u16*)d_in[5 * p + 4];
  }
  a.o = (unsigned char*)d_ws;
  (void)hipFuncSetAttribute((const void*)attn_fused_kernel, hipFuncAttributeMaxDynamicSharedMemorySize, 163840);
  attn_fused_kernel<<<256, 256, 163840, stream>>>(a);
  (void)hipFuncSetAttribute((const void*)oproj_kernel, hipFuncAttributeMaxDynamicSharedMemorySize, 163840);
  oproj_kernel<<<216, 512, 163840, stream>>>((const unsigned char*)d_ws, (const u16*)d_in[4], (const u16*)d_in[9], (u16*)d_out);
}
```
